```python
import torch

def _softmax(x, axis=-1):
    return torch.softmax(x, dim=axis)

def _gelu(x):
    return torch.nn.functional.gelu(x, approximate='tanh')

def _silu(x):
    return torch.nn.functional.silu(x)

def _sigmoid(x):
    return torch.sigmoid(x)

def _layernorm(x, axis=-1, eps=1e-05):
    m = x.mean(dim=axis, keepdim=True)
    v = x.var(dim=axis, keepdim=True, unbiased=False)
    return (x - m) / torch.sqrt(v + eps)

def _scatter_add(tgt, idx, src, axis=0):
    return tgt.clone().index_add_(axis, idx, src)

def _segment_sum(src, seg, n):
    out = torch.zeros((n,) + src.shape[1:], dtype=src.dtype)
    return out.index_add_(0, seg, src)

def _pad(x, pw):
    flat = tuple((v for pair in reversed(pw) for v in pair))
    return torch.nn.functional.pad(x, flat)
kernel_name = 'parallel_sum__sliding_local_dilated__mixer_gmlp__prompt'

def setup_inputs():

    _w = lambda a: (lambda t: t.to(torch.bfloat16) if t.is_floating_point() else t)(torch.as_tensor(a))

    def _A_setup():
        B, S, D = (1, 2048, 576)
        return {'x': _w((lambda *s: torch.randn(*s, dtype=torch.bfloat16) * 0.05)(B, S, D)), 'wqkv': _w((lambda *s: torch.randn(*s, dtype=torch.bfloat16) * 0.2)(D, 3 * D)), 'wo': _w((lambda *s: torch.randn(*s, dtype=torch.bfloat16) * 0.1)(D, D))}
    _A_in = _A_setup()

    def _B_setup():
        B, S, D, F = (1, 2048, 576, 4608)
        Fh = F // 2
        return {'x': _w((lambda *s: torch.randn(*s, dtype=torch.bfloat16) * 0.05)(B, S, D)), 'w_in': _w((lambda *s: torch.randn(*s, dtype=torch.bfloat16) * 0.8)(D, 2 * Fh)), 'w_sp': _w(0.01 * (lambda *s: torch.randn(*s, dtype=torch.bfloat16) * 0.8)(S, S)), 'b_sp': _w(1.0 + 0.01 * (lambda *s: torch.randn(*s, dtype=torch.bfloat16) * 0.05)(S)), 'w_out': _w((lambda *s: torch.randn(*s, dtype=torch.bfloat16) * 0.05)(Fh, D))}
    _B_in = _B_setup()
    return {'A_x': _A_in['x'], 'A_wqkv': _A_in['wqkv'], 'A_wo': _A_in['wo'], 'B_x': _B_in['x'], 'B_w_in': _B_in['w_in'], 'B_w_sp': _B_in['w_sp'], 'B_b_sp': _B_in['b_sp'], 'B_w_out': _B_in['w_out']}

def reference(A_x, A_wqkv, A_wo, B_x, B_w_in, B_w_sp, B_b_sp, B_w_out):

    def _A_ref(x, wqkv, wo):
        B, S, D = x.shape
        H = 32
        Dh = D // H
        W, dil = (8, 2)
        qkv = (lambda x, *d: x.permute(*d))((x @ wqkv).reshape(B, S, 3, H, Dh), 2, 0, 3, 1, 4)
        q, k, v = (qkv[0], qkv[1], qkv[2])
        scale = Dh ** (-0.5)
        pos = torch.arange(S)
        off = torch.arange(W) * dil
        idx = pos[:, None] - off[None, :]
        mask = idx >= 0
        idx_c = torch.where(mask, idx, 0).reshape(-1)
        kg = k[:, :, idx_c, :].reshape(B, H, S, W, Dh)
        vg = v[:, :, idx_c, :].reshape(B, H, S, W, Dh)
        logits = torch.einsum('bhsd,bhswd->bhsw', q, kg) * scale
        logits = torch.where(mask[None, None, :, :], logits, -1000000000.0)
        attn = _softmax(logits, axis=-1)
        o = torch.einsum('bhsw,bhswd->bhsd', attn, vg)
        return (lambda x, *d: x.permute(*d))(o, 0, 2, 1, 3).reshape(B, S, D) @ wo

    def _B_ref(x, w_in, w_sp, b_sp, w_out):
        B, S, D = x.shape
        F = w_in.shape[-1]
        Fh = F // 2
        z = _gelu(torch.einsum('bsd,df->bsf', x, w_in))
        z1 = z[..., :Fh].clone()
        z2 = _layernorm(z[..., Fh:], axis=-1)
        g = torch.einsum('ts,bsf->btf', w_sp, z2) + b_sp[None, :, None]
        return torch.einsum('bsf,fd->bsd', z1 * g, w_out)
    x = A_x
    xn = _layernorm(x, axis=-1)
    return x + _A_ref(xn, A_wqkv, A_wo) + _B_ref(xn, B_w_in, B_w_sp, B_b_sp, B_w_out)
```

```cpp
#include <hip/hip_runtime.h>
#include <hip/hip_bf16.h>
#include <stdint.h>
namespace nv {
constexpr int S=2048, D=576, F=4608, FH=2304, H=32, DH=18;
typedef unsigned short u16;
__device__ __forceinline__ float b2f(u16 v){ return __uint_as_float(((uint32_t)v)<<16); }
__device__ __forceinline__ u16 f2b(float f){ uint32_t u=__float_as_uint(f); u += 0x7fff + ((u>>16)&1); return (u16)(u>>16); }
__device__ __forceinline__ float rb(float f){ return b2f(f2b(f)); }

__global__ void ln_rows(const u16* x, u16* y, int ld, int off, int n, int ldy){
  int r = blockIdx.x; __shared__ float red[256];
  float s=0; for(int i=threadIdx.x;i<n;i+=256) s+=b2f(x[(size_t)r*ld+off+i]);
  red[threadIdx.x]=s; __syncthreads();
  for(int o=128;o>0;o>>=1){ if(threadIdx.x<o) red[threadIdx.x]+=red[threadIdx.x+o]; __syncthreads(); }
  float m=red[0]/n; __syncthreads();
  float v=0; for(int i=threadIdx.x;i<n;i+=256){ float d=b2f(x[(size_t)r*ld+off+i])-m; v+=d*d; }
  red[threadIdx.x]=v; __syncthreads();
  for(int o=128;o>0;o>>=1){ if(threadIdx.x<o) red[threadIdx.x]+=red[threadIdx.x+o]; __syncthreads(); }
  float var=rb(red[0]/n); float mb=rb(m);
  float sd=rb(sqrtf(rb(var+1e-5f)));
  for(int i=threadIdx.x;i<n;i+=256){ float d=rb(b2f(x[(size_t)r*ld+off+i])-mb); y[(size_t)r*ldy+i]=f2b(d/sd); }
}
template<int MODE>
__global__ void gemm(const u16* A, const u16* B, u16* C, int M, int N, int K, int lda, int ldb, int ldc){
  __shared__ float As[16][17], Bs[16][17];
  int tx=threadIdx.x&15, ty=threadIdx.x>>4; int row=blockIdx.y*16+ty, col=blockIdx.x*16+tx; float acc=0;
  for(int k0=0;k0<K;k0+=16){ As[ty][tx]=b2f(A[(size_t)row*lda+k0+tx]); Bs[ty][tx]=b2f(B[(size_t)(k0+ty)*ldb+col]); __syncthreads();
    #pragma unroll
    for(int k=0;k<16;k++) acc+=As[ty][k]*Bs[k][tx]; __syncthreads(); }
  float v=rb(acc);
  if(MODE==1){ float u=0.7978845608028654f*(v+0.044715f*v*v*v); v=0.5f*v*(1.f+tanhf(u)); }
  C[(size_t)row*ldc+col]=f2b(v);
}
__global__ void attn(const u16* qkv, u16* o){
  int idx=blockIdx.x*blockDim.x+threadIdx.x; int t=idx/H, h=idx%H; if(t>=S) return;
  float q[DH]; for(int d=0;d<DH;d++) q[d]=b2f(qkv[(size_t)t*3*D+h*DH+d]);
  float lg[8]; float mx=-1e30f; float scale=0.23570226039551584f;
  for(int w=0;w<8;w++){ int s=t-2*w; if(s<0){ lg[w]=-1e9f; } else { float a=0; for(int d=0;d<DH;d++) a+=q[d]*b2f(qkv[(size_t)s*3*D+D+h*DH+d]); lg[w]=rb(rb(a)*scale);} mx=fmaxf(mx,lg[w]); }
  float sum=0; for(int w=0;w<8;w++){ lg[w]=expf(lg[w]-mx); sum+=lg[w]; }
  for(int w=0;w<8;w++) lg[w]=rb(lg[w]/sum);
  for(int d=0;d<DH;d++){ float a=0; for(int w=0;w<8;w++){ int s=t-2*w; if(s<0) s=0; a+=lg[w]*b2f(qkv[(size_t)s*3*D+2*D+h*DH+d]); } o[(size_t)t*D+h*DH+d]=f2b(a); }
}
__global__ void hmul(const u16* z, const u16* g, const u16* b, u16* h){
  int i=blockIdx.x*blockDim.x+threadIdx.x; int t=i/FH, f=i%FH;
  float gg=rb(b2f(g[i])+b2f(b[t])); h[i]=f2b(b2f(z[(size_t)t*F+f])*gg);
}
__global__ void fin(const u16* x, const u16* a, const u16* b, u16* o){
  int i=blockIdx.x*blockDim.x+threadIdx.x; o[i]=f2b(rb(b2f(x[i])+b2f(a[i]))+b2f(b[i]));
}
struct Bufs { u16 *xn,*qkv,*o,*aout,*z,*z2n,*g,*h,*bout; };
inline Bufs carve(void* ws){ Bufs b; u16* p=(u16*)ws; b.xn=p; p+=S*D; b.qkv=p; p+=S*3*D; b.o=p; p+=S*D; b.aout=p; p+=S*D; b.z=p; p+=(size_t)S*F; b.z2n=p; p+=S*FH; b.g=p; p+=S*FH; b.h=p; p+=S*FH; b.bout=p; p+=S*D; return b; }
inline void run(void* const* d_in, void* d_out, void* ws, hipStream_t st){
  const u16* x=(const u16*)d_in[0]; const u16* wqkv=(const u16*)d_in[1]; const u16* wo=(const u16*)d_in[2];
  const u16* win=(const u16*)d_in[4]; const u16* wsp=(const u16*)d_in[5]; const u16* bsp=(const u16*)d_in[6]; const u16* wout=(const u16*)d_in[7];
  Bufs b=carve(ws);
  ln_rows<<<S,256,0,st>>>(x,b.xn,D,0,D,D);
  gemm<0><<<dim3(3*D/16,S/16),256,0,st>>>(b.xn,wqkv,b.qkv,S,3*D,D,D,3*D,3*D);
  attn<<<S*H/256,256,0,st>>>(b.qkv,b.o);
  gemm<0><<<dim3(D/16,S/16),256,0,st>>>(b.o,wo,b.aout,S,D,D,D,D,D);
  gemm<1><<<dim3(F/16,S/16),256,0,st>>>(b.xn,win,b.z,S,F,D,D,F,F);
  ln_rows<<<S,256,0,st>>>(b.z,b.z2n,F,FH,FH,FH);
  gemm<0><<<dim3(FH/16,S/16),256,0,st>>>(wsp,b.z2n,b.g,S,FH,S,S,FH,FH);
  hmul<<<S*FH/256,256,0,st>>>(b.z,b.g,bsp,b.h);
  gemm<0><<<dim3(D/16,S/16),256,0,st>>>(b.h,wout,b.bout,S,D,FH,FH,D,D);
  fin<<<S*D/256,256,0,st>>>(x,b.aout,b.bout,(u16*)d_out);
}
}
extern "C" void kernel_launch(void* const* d_in, const int* in_sizes, int n_in, void* d_out, int out_size, void* d_ws, size_t ws_size, hipStream_t stream){
  nv::run(d_in,d_out,d_ws,stream);
}
```

```cpp
#include <hip/hip_runtime.h>
#include <hip/hip_bf16.h>
#include <stdint.h>

typedef unsigned short u16;
typedef __attribute__((ext_vector_type(8))) short bf16x8;
typedef __attribute__((ext_vector_type(4))) short bf16x4;
typedef __attribute__((ext_vector_type(4))) float f32x4;
typedef __attribute__((ext_vector_type(4))) unsigned u32x4;
typedef __attribute__((ext_vector_type(2))) unsigned u32x2;
typedef __amdgpu_buffer_rsrc_t rsrc_t;

constexpr int S_ = 2048, D_ = 576, F_ = 4608, FH_ = 2304;
constexpr size_t OFF_Z2T = 0;
constexpr size_t SZ_Z = (size_t)16 * 64 * 9 * 1024;
constexpr size_t OFF_Z1T = OFF_Z2T + SZ_Z;
constexpr size_t OFF_HT = OFF_Z1T + SZ_Z;
constexpr size_t OFF_WT = OFF_HT + SZ_Z;
constexpr size_t SZ_WT = (size_t)16 * 64 * 8 * 1024;
constexpr size_t OFF_OT = OFF_WT + SZ_WT;
constexpr size_t SZ_OT = (size_t)128 * 144 * 128;
constexpr size_t OFF_WC = OFF_OT + SZ_OT;
constexpr size_t SZ_WC = (size_t)8 * 90 * 5 * 1024;
constexpr size_t OFF_XT = OFF_WC + SZ_WC;
constexpr size_t SZ_XT = (size_t)128 * 18 * 1024;
constexpr size_t OFF_W1 = OFF_XT + SZ_XT;
constexpr size_t SZ_W1 = (size_t)32 * 234 * 1024;
constexpr size_t OFF_RS = OFF_W1 + SZ_W1;
constexpr size_t OFF_CNT = OFF_RS + 2048 * 16;
constexpr size_t OFF_ST = OFF_CNT + 16 * 256;
constexpr size_t WS_END = OFF_ST + (1 << 20);

#define RFL(x) __builtin_amdgcn_readfirstlane(x)
#define SCHED() __builtin_amdgcn_sched_barrier(0)
__device__ __forceinline__ rsrc_t mk_rsrc(const void* p, int bytes) { return __builtin_amdgcn_make_buffer_rsrc((void*)p, (short)0, bytes, 0x00020000); }
__device__ __forceinline__ float bf_lo(unsigned w) { return __uint_as_float(w << 16); }
__device__ __forceinline__ float bf_hi(unsigned w) { return __uint_as_float(w & 0xffff0000u); }
__device__ __forceinline__ unsigned pk_bf16(float lo, float hi) {
  typedef __attribute__((ext_vector_type(2))) float f32x2; typedef __attribute__((ext_vector_type(2))) __bf16 bf2;
  f32x2 v = {lo, hi}; bf2 r = __builtin_convertvector(v, bf2); return __builtin_bit_cast(unsigned, r);
}
__device__ __forceinline__ float rbf(float f) { return bf_hi(pk_bf16(0.f, f)); }
__host__ __device__ __forceinline__ int prow(int tt, int i) { return 32 * (tt >> 1) + 8 * (i >> 2) + 4 * (tt & 1) + (i & 3); }
#define MFMA(a, b, c) __builtin_amdgcn_mfma_f32_16x16x32_bf16(a, b, c, 0, 0, 0)

#define DMA16(lds, voff, srd, soff, imm) asm volatile("s_mov_b32 m0, %0\n\ts_nop 0\n\tbuffer_load_dwordx4 %1, %2, %3 offen offset:" #imm " lds" :: "s"(lds), "v"(voff), "s"(srd), "s"(soff) : "memory")
#define DMA16H(lds, voff, srd, soff) asm volatile("s_mov_b32 m0, %0\n\ts_lshr_b64 exec, -1, 32\n\tbuffer_load_dwordx4 %1, %2, %3 offen lds\n\ts_mov_b64 exec, -1" :: "s"(lds), "v"(voff), "s"(srd), "s"(soff) : "memory")
typedef __attribute__((address_space(3))) char lds_char;
#define LDS_RD128(addr) (*(const __attribute__((address_space(3))) bf16x8*)(addr))
#define AMFMA(acc, a, b) asm volatile("v_mfma_f32_16x16x32_bf16 %0, %1, %2, %0" : "+v"(acc) : "v"(a), "v"(b))
#define ADSRD(dst, addr, off) asm volatile("ds_read_b128 %0, %1 offset:%2" : "=v"(dst) : "v"(addr), "i"(off))
#define OPAQUE(x) asm volatile("" : "+v"(x))
#define BLOAD16(dst, voff, srd, soff, imm) asm volatile("buffer_load_dwordx4 %0, %1, %2, %3 offen offset:" #imm : "=v"(dst) : "v"(voff), "s"(srd), "s"(soff))

constexpr int KA_SLOT = 10240, KA_ATT = 6 * KA_SLOT, KA_PITCH = 144, KA_LDS = KA_ATT + 142 * KA_PITCH;
template <int HALF> struct KaCfg {
  static constexpr int NZ2 = 5 - HALF, NZ1 = 4 + HALF;
  static constexpr int nt(int s) { return s < 9 ? 4 : (s < 18 ? NZ2 : NZ1); }
  static constexpr int goff(int s) { return s < 9 ? s * 8192 : (s < 18 ? 73728 + (s - 9) * NZ2 * 2048 : 73728 + 9 * NZ2 * 2048 + (s - 18) * NZ1 * 2048); }
  static constexpr int ops(int s) { return s > 26 ? 0 : 2 + (nt(s) == 5 ? 1 : 0) + (s < 9 ? 4 + ((s & 1) ? 0 : 1) : 0); }
  static constexpr int wait(int s) { return s == 20 ? ops(23) + ops(24) : ops(s + 2) + ops(s + 3) + ops(s + 4) + ((s >= 9 && s <= 12) ? 4 : 0); }
};
template <int V> struct IC { static constexpr int v = V; };
#define BLD(dst, voff, srd, soff, IMM) asm volatile("buffer_load_dwordx4 %0, %1, %2, %3 offen offset:%4" : "=v"(dst) : "v"(voff), "s"(srd), "s"(soff), "i"(IMM))
#define AMFMA0(acc, a, b) asm volatile("v_mfma_f32_16x16x32_bf16 %0, %1, %2, 0" : "=&v"(acc) : "v"(a), "v"(b))
typedef __attribute__((ext_vector_type(2))) float f32x2;
typedef __attribute__((ext_vector_type(2))) __bf16 bf2_t;
__device__ __forceinline__ float dot2(unsigned a, unsigned b, float c) { return __builtin_amdgcn_fdot2_f32_bf16(__builtin_bit_cast(bf2_t, a), __builtin_bit_cast(bf2_t, b), c, false); }
__device__ __forceinline__ f32x2 gelu2(f32x2 x) {
  f32x2 t = x * x, p = t * (-0.10294324f) + (-2.3022082f), e = x * p;
  f32x2 d = {__builtin_amdgcn_exp2f(e[0]) + 1.f, __builtin_amdgcn_exp2f(e[1]) + 1.f};
  f32x2 r = {__builtin_amdgcn_rcpf(d[0]), __builtin_amdgcn_rcpf(d[1])};
  return x * r;
}
template <int CTRL> __device__ __forceinline__ float dpp_add(float v) {
  return v + __builtin_bit_cast(float, __builtin_amdgcn_update_dpp(0, __builtin_bit_cast(int, v), CTRL, 0xf, 0xf, true));
}

template <int HALF>
__device__ __forceinline__ void ka_body(char* smem, const char* __restrict__ ws_c, char* __restrict__ ws, const int p, const int cg) {
  typedef KaCfg<HALF> C;
  const int tid = threadIdx.x, lane = tid & 63, li = lane & 15, lg = lane >> 4;
  const int w = RFL(tid >> 6), fb = cg >> 1;
  const rsrc_t rW = mk_rsrc(ws_c + OFF_W1 + (size_t)cg * (234 * 1024), 234 * 1024);
  const rsrc_t rX = mk_rsrc(ws_c + OFF_XT, (int)SZ_XT);
  const unsigned ldsb = (unsigned)(size_t)(__attribute__((address_space(3))) char*)smem;
  const unsigned vl16 = lane * 16;
  const unsigned vX = vl16 + (unsigned)(p * 8 + w * 2) * 18432u;
  const unsigned vH = p == 0 ? 0x80000000u : (unsigned)(p * 8 - 2 + (li >> 3)) * 18432u + (unsigned)(lg * 16 + (li | 8)) * 16u + (unsigned)w * 1024u;
  const unsigned sF = RFL((unsigned)w * 2048u), sH = RFL(8192u + (unsigned)w * 512u);
  const unsigned rd = ldsb + vl16;
  bf16x8 X[2][18], HX[5], F[5];
  f32x4 acc[2][5], hacc[3];
#pragma unroll
  for (int n = 0; n < 3; ++n) hacc[n] = (f32x4){0.f, 0.f, 0.f, 0.f};

  bf16x8 (&X_)[2][18] = X; bf16x8 (&HX_)[5] = HX; bf16x8 (&F_)[5] = F; f32x4 (&acc_)[2][5] = acc; f32x4 (&hacc_)[3] = hacc;
  const rsrc_t rW_ = rW, rX_ = rX; const unsigned vl16_ = vl16, vX_ = vX, vH_ = vH, rd_ = rd;
#define KA_CAP bf16x8 (&X)[2][18] = X_; bf16x8 (&HX)[5] = HX_; bf16x8 (&F)[5] = F_; f32x4 (&acc)[2][5] = acc_; f32x4 (&hacc)[3] = hacc_; \
    const rsrc_t rW = rW_, rX = rX_; const unsigned vl16 = vl16_, vX = vX_, vH = vH_, rd = rd_; (void)X; (void)HX; (void)F; (void)acc; (void)hacc; (void)rW; (void)rX; (void)vl16; (void)vX; (void)vH; (void)rd;
  auto vop = [&](auto S_, auto K_) {
    KA_CAP
    constexpr int s = decltype(S_)::v, k = decltype(K_)::v;
    if constexpr (s <= 26) {
      constexpr int slot = s % 6, nd = 2 + (C::nt(s) == 5 ? 1 : 0);
      if constexpr (k == 0) { unsigned lo = ldsb + slot * KA_SLOT + sF, so = C::goff(s) + sF; DMA16(lo, vl16, rW, so, 0); }
      else if constexpr (k == 1) { unsigned lo = ldsb + slot * KA_SLOT + sF, so = C::goff(s) + sF; DMA16(lo, vl16, rW, so, 1024); }
      else if constexpr (k == 2 && nd == 3) { unsigned lo = ldsb + slot * KA_SLOT + sH, so = C::goff(s) + sH; DMA16H(lo, vl16, rW, so); }
      else if constexpr (s < 9 && k >= 2 && k < 6) {
        constexpr int m = (k - 2) & 1, ks = 2 * s + ((k - 2) >> 1);
        BLD(X[m][ks], vX, rX, m * 18432 + (ks & ~3) * 1024, (ks & 3) * 1024);
      } else if constexpr (s < 9 && k == 6 && !(s & 1)) { BLD(HX[s >> 1], vH, rX, (s >> 1) * 4096, 0); }
    }
  };
  auto group = [&](auto S_) { vop(S_, IC<0>{}); vop(S_, IC<1>{}); vop(S_, IC<2>{}); vop(S_, IC<3>{}); vop(S_, IC<4>{}); vop(S_, IC<5>{}); vop(S_, IC<6>{}); };
  auto sync = [&](auto S_) {
    KA_CAP
    constexpr int s = decltype(S_)::v;
    if constexpr (s < 26) {
      if constexpr (s + 1 < 9) {
        constexpr int k0 = 2 * (s + 1);
        asm volatile("s_waitcnt vmcnt(%5)" : "+v"(X[0][k0]), "+v"(X[1][k0]), "+v"(X[0][k0 + 1]), "+v"(X[1][k0 + 1]), "+v"(HX[(s + 1) >> 1]) : "i"(C::wait(s)) : "memory");
      } else asm volatile("s_waitcnt vmcnt(%0)" :: "i"(C::wait(s)) : "memory");
      __builtin_amdgcn_s_barrier();
    }
  };
  auto step = [&](auto S_, auto H_) {
    KA_CAP
    constexpr int s = decltype(S_)::v, h = decltype(H_)::v;
    constexpr int NT = C::nt(s), job = s / 9, ks = 2 * (s % 9) + h;
    constexpr int s2 = h ? s + 1 : s, h2 = h ^ 1, NT2 = s2 <= 26 ? C::nt(s2) : 0;
    constexpr int roff = (s2 % 6) * KA_SLOT + h2 * NT2 * 1024;
    if constexpr (ks == 0) asm volatile("s_waitcnt lgkmcnt(0)" ::: "memory");
    if constexpr (job == 0) {
      if (w == (ks & 3)) {
        asm volatile("s_waitcnt lgkmcnt(1)" ::: "memory");
#pragma unroll
        for (int n = 0; n < 3; ++n) AMFMA(hacc[n], F[n], HX[ks >> 2]);
      }
    }
#pragma unroll
    for (int n = 0; n < NT; ++n) {
      if constexpr (ks != 0) asm volatile("s_waitcnt lgkmcnt(%0)" :: "i"(NT - 1) : "memory");
      if constexpr (job == 1) {
        if constexpr (ks == 0) { AMFMA0(acc[0][n], X[0][ks], F[n]); AMFMA0(acc[1][n], X[1][ks], F[n]); }
        else { AMFMA(acc[0][n], X[0][ks], F[n]); AMFMA(acc[1][n], X[1][ks], F[n]); }
      } else {
        if constexpr (ks == 0) { AMFMA0(acc[0][n], F[n], X[0][ks]); AMFMA0(acc[1][n], F[n], X[1][ks]); }
        else { AMFMA(acc[0][n], F[n], X[0][ks]); AMFMA(acc[1][n], F[n], X[1][ks]); }
      }
      if (n < NT2) ADSRD(F[n], rd, roff + n * 1024);
      if constexpr (h == 1) {
        if (n == 0) { vop(IC<s + 5>{}, IC<0>{}); vop(IC<s + 5>{}, IC<2>{}); }
        if (n == 1) { vop(IC<s + 5>{}, IC<1>{}); vop(IC<s + 5>{}, IC<3>{}); }
        if (n == 2) { vop(IC<s + 5>{}, IC<4>{}); vop(IC<s + 5>{}, IC<5>{}); }
        if (n == 3) { vop(IC<s + 5>{}, IC<6>{}); }
      }
    }
    if constexpr (NT2 > NT) ADSRD(F[4], rd, roff + 4 * 1024);
  };
  auto iter = [&](auto S_) { step(S_, IC<0>{}); sync(S_); step(S_, IC<1>{}); };

  group(IC<0>{}); group(IC<1>{}); group(IC<2>{}); group(IC<3>{}); group(IC<4>{});
  asm volatile("s_waitcnt vmcnt(%5)" : "+v"(X[0][0]), "+v"(X[1][0]), "+v"(X[0][1]), "+v"(X[1][1]), "+v"(HX[0]) : "i"(C::ops(1) + C::ops(2) + C::ops(3) + C::ops(4)) : "memory");
  __builtin_amdgcn_s_barrier();
#pragma unroll
  for (int n = 0; n < 4; ++n) ADSRD(F[n], rd, n * 1024);

  iter(IC<0>{}); iter(IC<1>{}); iter(IC<2>{}); iter(IC<3>{}); iter(IC<4>{}); iter(IC<5>{}); iter(IC<6>{}); iter(IC<7>{}); iter(IC<8>{});
  asm volatile("s_nop 15" ::: "memory");
  SCHED();
  {
    char* att = smem + KA_ATT;
#pragma unroll
    for (int n = 0; n < 3; ++n) *(f32x4*)(att + (w * 3 + n) * 1024 + vl16) = hacc[n];
    __syncthreads();
    unsigned hv0 = 0, hv1 = 0;
    if (w < 3) {
      f32x4 a = *(const f32x4*)(att + w * 1024 + vl16);
#pragma unroll
      for (int q = 1; q < 4; ++q) a += *(const f32x4*)(att + (q * 3 + w) * 1024 + vl16);
      hv0 = pk_bf16(a[0], a[1]); hv1 = pk_bf16(a[2], a[3]);
    }
    __syncthreads();
    if (w < 3) {
      int ho = (li < 8) ? 8 * (li >> 2) + (li & 3) : 8 * ((li - 8) >> 2) + 4 + (li & 3);
      if (ho >= 2) *(u32x2*)(att + (ho - 2) * KA_PITCH + w * 32 + lg * 8) = (u32x2){hv0, hv1};
    }
#pragma unroll
    for (int m = 0; m < 2; ++m) {
      char* rp = att + (14 + w * 32 + 8 * (li >> 2) + 4 * m + (li & 3)) * KA_PITCH + lg * 8;
#pragma unroll
      for (int n = 0; n < 4; ++n) *(u32x2*)(rp + n * 32) = (u32x2){pk_bf16(acc[m][n][0], acc[m][n][1]), pk_bf16(acc[m][n][2], acc[m][n][3])};
    }
    __syncthreads();
    const int t = tid >> 1, hf = tid & 1;
    const char* qr = att + (14 + t) * KA_PITCH;
    unsigned q[9];
    { u32x2 a = *(const u32x2*)(qr + 88), b = *(const u32x2*)(qr + 96), c = *(const u32x2*)(qr + 104), d = *(const u32x2*)(qr + 112);
      q[0] = a[0]; q[1] = a[1]; q[2] = b[0]; q[3] = b[1]; q[4] = c[0]; q[5] = c[1]; q[6] = d[0]; q[7] = d[1]; q[8] = *(const unsigned*)(qr + 120); }
    float lgt[4];
    const char* kr = qr - hf * 8 * KA_PITCH;
#pragma unroll
    for (int j = 0; j < 4; ++j) {
      const char* r = kr - j * 2 * KA_PITCH;
      u32x4 a = *(const u32x4*)r, b = *(const u32x4*)(r + 16); unsigned c = *(const unsigned*)(r + 32);
      float d = dot2(a[0], q[0], 0.f);
      d = dot2(a[1], q[1], d); d = dot2(a[2], q[2], d); d = dot2(a[3], q[3], d);
      d = dot2(b[0], q[4], d); d = dot2(b[1], q[5], d); d = dot2(b[2], q[6], d); d = dot2(b[3], q[7], d); d = dot2(c, q[8], d);
      d = rbf(rbf(d) * 0.23570226f);
      lgt[j] = (p * 128 + t - 2 * (hf * 4 + j) < 0) ? -1e9f : d;
    }
    float mx = fmaxf(fmaxf(lgt[0], lgt[1]), fmaxf(lgt[2], lgt[3]));
    mx = fmaxf(mx, __shfl_xor(mx, 1));
    float sum = 0.f;
#pragma unroll
    for (int j = 0; j < 4; ++j) { lgt[j] = __builtin_amdgcn_exp2f((lgt[j] - mx) * 1.44269504f); sum += lgt[j]; }
    sum += __shfl_xor(sum, 1);
    const float inv = __builtin_amdgcn_rcpf(sum);
    float o[18];
#pragma unroll
    for (int d = 0; d < 18; ++d) o[d] = 0.f;
#pragma unroll
    for (int j = 0; j < 4; ++j) {
      const char* r = kr - j * 2 * KA_PITCH + 48;
      u32x4 a = *(const u32x4*)r, b = *(const u32x4*)(r + 16); unsigned c = *(const unsigned*)(r + 32);
      const float pw = rbf(lgt[j] * inv);
#pragma unroll
      for (int e = 0; e < 4; ++e) { o[2 * e] += pw * bf_lo(a[e]); o[2 * e + 1] += pw * bf_hi(a[e]); o[8 + 2 * e] += pw * bf_lo(b[e]); o[9 + 2 * e] += pw * bf_hi(b[e]); }
      o[16] += pw * bf_lo(c); o[17] += pw * bf_hi(c);
    }
#pragma unroll
    for (int d = 0; d < 18; ++d) o[d] += __shfl_xor(o[d], 1);
    const int tl = t & 31, tt = p * 8 + (t >> 5) * 2 + ((tl >> 2) & 1), slot = (tl >> 3) * 4 + (tl & 3);
    char* ob = ws + OFF_OT + (size_t)tt * 18432 + slot * 8;
#pragma unroll
    for (int e = 0; e < 5; ++e) {
      float a = hf ? o[10 + 2 * (e & 3)] : o[2 * e], b = hf ? o[11 + 2 * (e & 3)] : o[2 * e + 1];
      int d = cg * 18 + (hf ? 10 + 2 * e : 2 * e);
      if (e < 4 || !hf) *(unsigned*)(ob + (d >> 2) * 128 + (d & 3) * 2) = pk_bf16(a, b);
    }
  }
  SCHED();

  iter(IC<9>{}); iter(IC<10>{}); iter(IC<11>{}); iter(IC<12>{}); iter(IC<13>{}); iter(IC<14>{}); iter(IC<15>{}); iter(IC<16>{}); iter(IC<17>{});
  asm volatile("s_nop 15" ::: "memory");
  SCHED();
  unsigned zs[C::NZ2][4];
  {
#pragma unroll
    for (int n = 0; n < C::NZ2; ++n) {
#pragma unroll
      for (int m = 0; m < 2; ++m) {
        f32x2 a = gelu2((f32x2){acc[m][n][0], acc[m][n][1]}), b = gelu2((f32x2){acc[m][n][2], acc[m][n][3]});
        zs[n][2 * m] = pk_bf16(a[0], a[1]); zs[n][2 * m + 1] = pk_bf16(b[0], b[1]);
      }
    }
    f32x2 s1[4], s2[4];
#pragma unroll
    for (int j = 0; j < 4; ++j) {
      s1[j] = (f32x2){0.f, 0.f}; s2[j] = s1[j];
#pragma unroll
      for (int n = 0; n < C::NZ2; ++n) { f32x2 v = {bf_lo(zs[n][j]), bf_hi(zs[n][j])}; s1[j] += v; s2[j] += v * v; }
    }
    float r[16];
#pragma unroll
    for (int j = 0; j < 4; ++j) { r[2 * j] = s1[j][0]; r[2 * j + 1] = s1[j][1]; r[8 + 2 * j] = s2[j][0]; r[9 + 2 * j] = s2[j][1]; }
#pragma unroll
    for (int j = 0; j < 16; ++j) { float v = r[j]; v = dpp_add<0x128>(v); v = dpp_add<0x124>(v); v = dpp_add<0x122>(v); v = dpp_add<0x121>(v); r[j] = v; }
    float sel = r[0];
#pragma unroll
    for (int j = 1; j < 16; ++j) sel = (li == j) ? r[j] : sel;
    double* dst = (double*)(ws + OFF_RS) + (size_t)(p * 128 + w * 32 + lg * 8 + (li & 7)) * 2 + (li >> 3);
    double dv = (double)sel;
    asm volatile("global_atomic_add_f64 %0, %1, off sc1" :: "v"(dst), "v"(dv) : "memory");
  }
  SCHED();

  unsigned* cnt = (unsigned*)(ws + OFF_CNT + p * 256);
  iter(IC<18>{}); iter(IC<19>{});
  step(IC<20>{}, IC<0>{}); sync(IC<20>{});
  if (tid == 0) { unsigned one = 1; asm volatile("global_atomic_add %0, %1, off sc1" :: "v"(cnt), "v"(one) : "memory"); }
  step(IC<20>{}, IC<1>{});
  iter(IC<21>{}); iter(IC<22>{}); iter(IC<23>{}); iter(IC<24>{}); iter(IC<25>{});
  unsigned seen;
  asm volatile("global_load_dword %0, %1, off sc1" : "=v"(seen) : "v"(cnt) : "memory");
  iter(IC<26>{});
  asm volatile("s_nop 15\n\ts_waitcnt vmcnt(0)" : "+v"(seen) :: "memory");
  while (RFL(seen) < 32u) {
    __builtin_amdgcn_s_sleep(2);
    asm volatile("global_load_dword %0, %1, off sc1\n\ts_waitcnt vmcnt(0)" : "=v"(seen) : "v"(cnt) : "memory");
  }
  u32x4 st;
  { const char* sp = ws + OFF_RS + (size_t)(p * 128 + w * 32 + (lane & 31)) * 16;
    asm volatile("global_load_dwordx4 %0, %1, off sc1" : "=v"(st) : "v"(sp) : "memory"); }
  SCHED();
#pragma unroll
  for (int m = 0; m < 2; ++m) {
    char* zp = ws + OFF_Z1T + ((size_t)(p * 8 + w * 2 + m) * 16 + fb) * 4608;
    unsigned pk[C::NZ1][2];
#pragma unroll
    for (int n = 0; n < C::NZ1; ++n) {
      f32x2 a = gelu2((f32x2){acc[m][n][0], acc[m][n][1]}), b = gelu2((f32x2){acc[m][n][2], acc[m][n][3]});
      pk[n][0] = pk_bf16(a[0], a[1]); pk[n][1] = pk_bf16(b[0], b[1]);
    }
#pragma unroll
    for (int q = 0; q < 2; ++q) *(u32x4*)(zp + (HALF * 2 + q) * 1024 + vl16) = (u32x4){pk[2 * q][0], pk[2 * q][1], pk[2 * q + 1][0], pk[2 * q + 1][1]};
    if constexpr (HALF) *(u32x2*)(zp + 4096 + lane * 8) = (u32x2){pk[4][0], pk[4][1]};
  }
  SCHED();
  asm volatile("s_waitcnt vmcnt(%1)" : "+v"(st) : "i"(HALF ? 6 : 4) : "memory");
  {
    const double sm = __builtin_bit_cast(double, (u32x2){st[0], st[1]}), sq = __builtin_bit_cast(double, (u32x2){st[2], st[3]});
    const double mean = sm * (1.0 / 2304.0);
    const float var = fmaxf((float)(sq * (1.0 / 2304.0) - mean * mean), 0.f);
    const float mb = rbf((float)mean), rs = 1.f / rbf(sqrtf(rbf(rbf(var) + 1e-5f)));
    f32x2* sl = (f32x2*)(smem + KA_ATT + w * 256);
    if (lane < 32) sl[lane] = (f32x2){mb, rs};
    __builtin_amdgcn_s_waitcnt(0xC07F);
    __builtin_amdgcn_wave_barrier();
    f32x4 ms[4];
#pragma unroll
    for (int j = 0; j < 4; ++j) ms[j] = *(const f32x4*)((const char*)sl + lg * 64 + j * 16);
    char* zp = ws + OFF_Z2T + ((size_t)(fb * 64 + p * 4 + w) * 9 + HALF * 5) * 1024 + vl16;
#pragma unroll
    for (int n = 0; n < C::NZ2; ++n) {
      u32x4 o;
#pragma unroll
      for (int j = 0; j < 4; ++j) o[j] = pk_bf16((bf_lo(zs[n][j]) - ms[j][0]) * ms[j][1], (bf_hi(zs[n][j]) - ms[j][2]) * ms[j][3]);
      *(u32x4*)(zp + n * 1024) = o;
    }
  }
}
__global__ __launch_bounds__(256, 2) void kA(const char* __restrict__ ws_c, char* __restrict__ ws) {
  extern __shared__ __attribute__((aligned(16))) char smem[];
  const int b = blockIdx.x, xcd = b & 7, idx = b >> 3;
  const int p = (xcd & 1) * 8 + (idx & 7), cg = (xcd >> 1) * 8 + (idx >> 3);
  if (cg & 1) ka_body<1>(smem, ws_c, ws, p, cg); else ka_body<0>(smem, ws_c, ws, p, cg);
}

constexpr int KB_STAGE = 36864;
constexpr int KB_LDS = 4 * KB_STAGE;
__global__ __launch_bounds__(512, 2) void kB(const char* __restrict__ ws_c, char* __restrict__ ws, const u16* __restrict__ bsp) {
  extern __shared__ __attribute__((aligned(16))) char smem[];
  const int tid = threadIdx.x, lane = tid & 63;
  const int w = RFL(tid >> 6), wm = w & 3, kh = w >> 2;
  const int b = blockIdx.x, xcd = b & 7, idx = b >> 3;
  const int tp = (xcd & 1) * 8 + (idx & 7), fb = (xcd >> 1) * 4 + (idx >> 3);
  const rsrc_t rZ = mk_rsrc(ws_c + OFF_Z2T + (size_t)fb * (64 * 9216), 64 * 9216);
  const rsrc_t rW = mk_rsrc(ws_c + OFF_WT + (size_t)tp * (64 * 8192), 64 * 8192);
  const unsigned ldsb = (unsigned)(size_t)(__attribute__((address_space(3))) char*)smem;
  const unsigned vl16 = lane * 16;
  const unsigned vW = vl16 + wm * 2048 + kh * 16384;
  const unsigned dmaF = w * 4096, dmaH = 32768 + w * 512;
  unsigned rd01 = ldsb + kh * 18432 + lane * 16;
  unsigned rd23 = rd01 + 2 * KB_STAGE;

  f32x4 acc[2][9];
#pragma unroll
  for (int m = 0; m < 2; ++m)
#pragma unroll
    for (int n = 0; n < 9; ++n) acc[m][n] = (f32x4){0.f, 0.f, 0.f, 0.f};
  bf16x8 Wr[4][4];
  bf16x8 ZA[9], ZB[9];

#define KB_DMA1(stage, slot, imm) do { \
    unsigned so_ = RFL((unsigned)(stage) * KB_STAGE + dmaF), lo_ = RFL(ldsb + (slot) * KB_STAGE + dmaF); DMA16(lo_, vl16, rZ, so_, imm); } while (0)
#define KB_DMAH(stage, slot) do { \
    unsigned so2_ = RFL((unsigned)(stage) * KB_STAGE + dmaH), lo2_ = RFL(ldsb + (slot) * KB_STAGE + dmaH); DMA16H(lo2_, vl16, rZ, so2_); } while (0)
#define KB_W1(stage, slot, e) do { unsigned sw_ = RFL((unsigned)(stage) * 32768u + ((e) >> 1) * 8192u); \
    if ((e) & 1) BLOAD16(Wr[slot][e], vW, rW, sw_, 1024); else BLOAD16(Wr[slot][e], vW, rW, sw_, 0); } while (0)
#define KB_ALL(stage, slot) do { KB_DMA1(stage, slot, 0); KB_DMA1(stage, slot, 1024); KB_DMA1(stage, slot, 2048); KB_DMA1(stage, slot, 3072); KB_DMAH(stage, slot); \
    KB_W1(stage, slot, 0); KB_W1(stage, slot, 1); KB_W1(stage, slot, 2); KB_W1(stage, slot, 3); } while (0)
#define KB_WAIT(slot, N) asm volatile("s_waitcnt vmcnt(" #N ") lgkmcnt(0)" : "+v"(Wr[slot][0]), "+v"(Wr[slot][1]), "+v"(Wr[slot][2]), "+v"(Wr[slot][3]) :: "memory")

  KB_ALL(0, 0);
  __builtin_amdgcn_s_barrier();
  KB_ALL(1, 1);
  KB_WAIT(0, 9);
  __builtin_amdgcn_s_barrier();

  const rsrc_t rZ1 = mk_rsrc(ws_c + OFF_Z1T + ((size_t)(tp * 8 + wm * 2) * 16 + fb) * 4608, 17 * 4608);
  const rsrc_t rB = mk_rsrc(bsp, 4096);
  u32x4 z1q[2][2]; u32x2 z1s[2]; unsigned biasu[2];
  const unsigned vz1 = lane * 16 + kh * 2048, vz8 = lane * 8 + 4096;
  const unsigned vb = (tp * 128 + wm * 32 + ((lane & 15) >> 2) * 8 + (lane & 3)) * 2;
#define KB_EPI_LD(e) do { \
    if ((e) == 0) BLOAD16(z1q[0][0], vz1, rZ1, 0, 0); \
    if ((e) == 1) BLOAD16(z1q[0][1], vz1, rZ1, 0, 1024); \
    if ((e) == 2) asm volatile("buffer_load_dwordx4 %0, %1, %2, %3 offen" : "=v"(z1q[1][0]) : "v"(vz1), "s"(rZ1), "s"(16 * 4608)); \
    if ((e) == 3) asm volatile("buffer_load_dwordx4 %0, %1, %2, %3 offen offset:1024" : "=v"(z1q[1][1]) : "v"(vz1), "s"(rZ1), "s"(16 * 4608)); \
    if ((e) == 4) asm volatile("buffer_load_dwordx2 %0, %1, %2, 0 offen" : "=v"(z1s[0]) : "v"(vz8), "s"(rZ1)); \
    if ((e) == 5) asm volatile("buffer_load_dwordx2 %0, %1, %2, %3 offen" : "=v"(z1s[1]) : "v"(vz8), "s"(rZ1), "s"(16 * 4608)); \
    if ((e) == 6) asm volatile("buffer_load_ushort %0, %1, %2, 0 offen" : "=v"(biasu[0]) : "v"(vb), "s"(rB)); \
    if ((e) == 7) asm volatile("buffer_load_ushort %0, %1, %2, 0 offen offset:8" : "=v"(biasu[1]) : "v"(vb), "s"(rB)); } while (0)

#define KB_ITER(i, j, MODE, WN) do { \
    _Pragma("unroll") for (int n = 0; n < 9; ++n) { \
      ADSRD(ZA[n], ((j) < 2 ? rd01 : rd23), ((j) & 1) * KB_STAGE + n * 1024); \
      if ((MODE) != 1) { AMFMA(acc[0][n], ZB[n], Wr[((j) + 3) & 3][2]); AMFMA(acc[1][n], ZB[n], Wr[((j) + 3) & 3][3]); } \
      if ((MODE) == 0) { \
        if (n == 0) KB_DMA1((i) + 3, ((j) + 3) & 3, 0); \
        if (n == 2) KB_DMA1((i) + 3, ((j) + 3) & 3, 1024); \
        if (n == 4) KB_W1((i) + 3, ((j) + 3) & 3, 0); \
        if (n == 6) KB_DMA1((i) + 3, ((j) + 3) & 3, 2048); \
        if (n == 8) KB_W1((i) + 3, ((j) + 3) & 3, 1); } \
      if ((MODE) == 2 && !(n & 1)) KB_EPI_LD(n >> 1); } \
    if ((MODE) == 1) { KB_ALL(2, 2); KB_DMA1(3, 3, 0); KB_DMA1(3, 3, 1024); KB_W1(3, 3, 0); KB_DMA1(3, 3, 2048); KB_W1(3, 3, 1); } \
    _Pragma("unroll") for (int n = 0; n < 9; ++n) { \
      if (n == 0) asm volatile("s_waitcnt lgkmcnt(6)" ::: "memory"); \
      if (n == 2) asm volatile("s_waitcnt lgkmcnt(5)" ::: "memory"); \
      if (n == 5) asm volatile("s_waitcnt lgkmcnt(5)" ::: "memory"); \
      ADSRD(ZB[n], ((j) < 2 ? rd01 : rd23), ((j) & 1) * KB_STAGE + 9216 + n * 1024); \
      AMFMA(acc[0][n], ZA[n], Wr[j][0]); AMFMA(acc[1][n], ZA[n], Wr[j][1]); \
      if ((MODE) < 2) { \
        if (n == 0) KB_DMA1((i) + 3, ((j) + 3) & 3, 3072); \
        if (n == 2) KB_W1((i) + 3, ((j) + 3) & 3, 2); \
        if (n == 4) KB_DMAH((i) + 3, ((j) + 3) & 3); \
        if (n == 6) KB_W1((i) + 3, ((j) + 3) & 3, 3); } \
      if ((MODE) == 2 && (n == 0 || n == 2 || n == 4)) KB_EPI_LD(5 + (n >> 1)); } \
    KB_WAIT(((j) + 1) & 3, WN); \
    __builtin_amdgcn_s_barrier(); \
    } while (0)

  KB_ITER(0, 0, 1, 18);
  KB_ITER(1, 1, 0, 18);
  KB_ITER(2, 2, 0, 18);
  KB_ITER(3, 3, 0, 18);
  for (int io = 4; io < 12; io += 4) {
    KB_ITER(io + 0, 0, 0, 18);
    KB_ITER(io + 1, 1, 0, 18);
    KB_ITER(io + 2, 2, 0, 18);
    KB_ITER(io + 3, 3, 0, 18);
  }
  KB_ITER(12, 0, 0, 18);
  KB_ITER(13, 1, 2, 17);
  KB_ITER(14, 2, 3, 8);
  {
#pragma unroll
    for (int n = 0; n < 9; ++n) { ADSRD(ZA[n], rd23, KB_STAGE + n * 1024); AMFMA(acc[0][n], ZB[n], Wr[2][2]); AMFMA(acc[1][n], ZB[n], Wr[2][3]); }
#pragma unroll
    for (int n = 0; n < 9; ++n) {
      if (n == 0) asm volatile("s_waitcnt lgkmcnt(6)" ::: "memory");
      if (n == 2) asm volatile("s_waitcnt lgkmcnt(5)" ::: "memory");
      if (n == 5) asm volatile("s_waitcnt lgkmcnt(5)" ::: "memory");
      ADSRD(ZB[n], rd23, KB_STAGE + 9216 + n * 1024);
      AMFMA(acc[0][n], ZA[n], Wr[3][0]); AMFMA(acc[1][n], ZA[n], Wr[3][1]); }
    asm volatile("s_waitcnt lgkmcnt(0)" ::: "memory");
#pragma unroll
    for (int n = 0; n < 9; ++n) { AMFMA(acc[0][n], ZB[n], Wr[3][2]); AMFMA(acc[1][n], ZB[n], Wr[3][3]); }
  }
  asm volatile("s_nop 15\n\ts_waitcnt vmcnt(0)" : "+v"(z1q[0][0]), "+v"(z1q[0][1]), "+v"(z1q[1][0]), "+v"(z1q[1][1]), "+v"(z1s[0]), "+v"(z1s[1]), "+v"(biasu[0]), "+v"(biasu[1]) :: "memory");
  SCHED();
  char* hp = ws + OFF_HT + ((size_t)(tp * 8 + wm * 2) * 16 + fb) * 4608;
  float bias[2] = {bf_lo(biasu[0]), bf_lo(biasu[1])};
  char* ex = smem + wm * 18432 + lane * 16;
  if (kh == 0) {
#pragma unroll
    for (int m = 0; m < 2; ++m)
#pragma unroll
      for (int n = 4; n < 8; ++n) *(f32x4*)(ex + (m * 9 + n) * 1024) = acc[m][n];
  } else {
#pragma unroll
    for (int m = 0; m < 2; ++m) {
#pragma unroll
      for (int n = 0; n < 4; ++n) *(f32x4*)(ex + (m * 9 + n) * 1024) = acc[m][n];
      *(f32x4*)(ex + (m * 9 + 8) * 1024) = acc[m][8];
    }
  }
  __syncthreads();
#pragma unroll
  for (int m = 0; m < 2; ++m) {
#pragma unroll
    for (int q = 0; q < 2; ++q) {
      f32x4 g0, g1;
      if (kh == 0) { g0 = acc[m][2 * q] + *(const f32x4*)(ex + (m * 9 + 2 * q) * 1024); g1 = acc[m][2 * q + 1] + *(const f32x4*)(ex + (m * 9 + 2 * q + 1) * 1024); }
      else { g0 = acc[m][4 + 2 * q] + *(const f32x4*)(ex + (m * 9 + 4 + 2 * q) * 1024); g1 = acc[m][5 + 2 * q] + *(const f32x4*)(ex + (m * 9 + 5 + 2 * q) * 1024); }
      u32x4 z = z1q[m][q], o;
      o[0] = pk_bf16(bf_lo(z[0]) * (g0[0] + bias[m]), bf_hi(z[0]) * (g0[1] + bias[m]));
      o[1] = pk_bf16(bf_lo(z[1]) * (g0[2] + bias[m]), bf_hi(z[1]) * (g0[3] + bias[m]));
      o[2] = pk_bf16(bf_lo(z[2]) * (g1[0] + bias[m]), bf_hi(z[2]) * (g1[1] + bias[m]));
      o[3] = pk_bf16(bf_lo(z[3]) * (g1[2] + bias[m]), bf_hi(z[3]) * (g1[3] + bias[m]));
      *(u32x4*)(hp + m * (16 * 4608) + (kh * 2 + q) * 1024 + lane * 16) = o;
    }
    if (kh == 0) {
      f32x4 g0 = acc[m][8] + *(const f32x4*)(ex + (m * 9 + 8) * 1024);
      u32x2 z = z1s[m], o;
      o[0] = pk_bf16(bf_lo(z[0]) * (g0[0] + bias[m]), bf_hi(z[0]) * (g0[1] + bias[m]));
      o[1] = pk_bf16(bf_lo(z[1]) * (g0[2] + bias[m]), bf_hi(z[1]) * (g0[3] + bias[m]));
      *(u32x2*)(hp + m * (16 * 4608) + 4096 + lane * 8) = o;
    }
  }
}

constexpr int KC_PITCH = 336;
constexpr int KC_PART = 64 * KC_PITCH;
constexpr int KC_LDS = 5 * KC_PART;
template <bool ISO>
__device__ __forceinline__ void kc_main(f32x4 (&acc)[4][5], rsrc_t rA, rsrc_t rW, unsigned vA16, unsigned vA8, unsigned vW, unsigned sA, unsigned sW) {
  constexpr unsigned TS = ISO ? 18432u : 16u * 4608u;
  constexpr int PD = 3;
  bf16x8 A[PD + 1][4], W[PD + 1][5];
  auto load = [&](int u, int slot) {
#pragma unroll
    for (int m = 0; m < 4; ++m) {
      if (ISO || (u % 9) == 4) {
        unsigned o0, o1;
        if (ISO) { o0 = sA + m * TS + u * 1024; o1 = o0 + 512; }
        else { o0 = sA + m * TS + (u / 9) * 2 * 4608 + 4096; o1 = o0 + 4608; }
        u32x2 lo = __builtin_amdgcn_raw_buffer_load_b64(rA, vA8, o0, 0);
        u32x2 hi = __builtin_amdgcn_raw_buffer_load_b64(rA, vA8, o1, 0);
        u32x4 v = {lo[0], lo[1], hi[0], hi[1]};
        A[slot][m] = __builtin_bit_cast(bf16x8, v);
      } else {
        int i = u % 9;
        unsigned o = sA + m * TS + (u / 9) * 2 * 4608 + (i < 4 ? i * 1024 : 4608 + (i - 5) * 1024);
        A[slot][m] = __builtin_bit_cast(bf16x8, __builtin_amdgcn_raw_buffer_load_b128(rA, vA16, o, 0));
      }
    }
#pragma unroll
    for (int n = 0; n < 5; ++n) W[slot][n] = __builtin_bit_cast(bf16x8, __builtin_amdgcn_raw_buffer_load_b128(rW, vW, sW + (u * 5 + n) * 1024, 0));
  };
#pragma unroll
  for (int u = 0; u < PD; ++u) load(u, u);
  SCHED();
#pragma unroll
  for (int u = 0; u < 18; ++u) {
    if (u + PD < 18) load(u + PD, (u + PD) % (PD + 1));
    SCHED();
#pragma unroll
    for (int m = 0; m < 4; ++m)
#pragma unroll
      for (int n = 0; n < 5; ++n) acc[m][n] = MFMA(W[u % (PD + 1)][n], A[u % (PD + 1)][m], acc[m][n]);
    SCHED();
  }
}
__global__ __launch_bounds__(320, 2) void kC(const char* __restrict__ ws_c, const u16* __restrict__ x, u16* __restrict__ out) {
  extern __shared__ __attribute__((aligned(16))) char smem[];
  const int tid = threadIdx.x, lane = tid & 63;
  const int w = RFL(tid >> 6);
  const int b = blockIdx.x, xcd = b & 7, idx = b >> 3;
  const int rt = (xcd & 3) * 8 + (idx & 7), ct = (xcd >> 2) * 4 + (idx >> 3);
  f32x4 acc[4][5];
#pragma unroll
  for (int m = 0; m < 4; ++m)
#pragma unroll
    for (int n = 0; n < 5; ++n) acc[m][n] = (f32x4){0.f, 0.f, 0.f, 0.f};
  const rsrc_t rW = mk_rsrc(ws_c + OFF_WC, (int)SZ_WC);
  const unsigned sW = RFL((unsigned)((ct * 90 + w * 18) * 5 * 1024));
  const int dstart = ct < 7 ? ct * 72 : 496;
  if (w < 4) {
    const rsrc_t rA = mk_rsrc(ws_c + OFF_HT, (int)SZ_Z);
    kc_main<false>(acc, rA, rW, lane * 16, lane * 8, lane * 16, RFL((unsigned)((rt * 4 * 16 + w * 4) * 4608)), sW);
  } else {
    const rsrc_t rA = mk_rsrc(ws_c + OFF_OT, (int)SZ_OT);
    kc_main<true>(acc, rA, rW, lane * 16, lane * 8, lane * 16, RFL((unsigned)(rt * 4 * 18432)), sW);
  }
  {
    char* p = smem + w * KC_PART + (lane & 15) * KC_PITCH + (lane >> 4) * 16;
#pragma unroll
    for (int m = 0; m < 4; ++m)
#pragma unroll
      for (int n = 0; n < 5; ++n) *(f32x4*)(p + m * 16 * KC_PITCH + n * 64) = acc[m][n];
  }
  __syncthreads();
  const int own0 = ct * 72 - dstart;
  for (int c = tid; c < 576; c += 320) {
    int r = c / 9, ch = c - r * 9;
    const char* p = smem + r * KC_PITCH + (own0 + ch * 8) * 4;
    f32x4 s0 = *(const f32x4*)p, s1 = *(const f32x4*)(p + 16);
#pragma unroll
    for (int ww = 1; ww < 5; ++ww) { s0 += *(const f32x4*)(p + ww * KC_PART); s1 += *(const f32x4*)(p + ww * KC_PART + 16); }
    size_t go = (size_t)(rt * 64 + prow(r >> 4, r & 15)) * 576 + ct * 72 + ch * 8;
    u32x4 xv = *(const u32x4*)(x + go), o;
    o[0] = pk_bf16(s0[0] + bf_lo(xv[0]), s0[1] + bf_hi(xv[0]));
    o[1] = pk_bf16(s0[2] + bf_lo(xv[1]), s0[3] + bf_hi(xv[1]));
    o[2] = pk_bf16(s1[0] + bf_lo(xv[2]), s1[1] + bf_hi(xv[2]));
    o[3] = pk_bf16(s1[2] + bf_lo(xv[3]), s1[3] + bf_hi(xv[3]));
    *(u32x4*)(out + go) = o;
  }
}

__device__ __forceinline__ int wc_krow(int ks, int g, int j) {
  int r = j & 3, hi = j >> 2;
  if (ks >= 72) { int u = ks - 72; return 2304 + 4 * (8 * u + 4 * hi + g) + r; }
  int w = ks / 18, pi = (ks % 18) / 9, i = ks % 9, fb0 = 4 * w + 2 * pi;
  if (i == 4) return 144 * (fb0 + hi) + 128 + 4 * g + r;
  if (i < 4) return 144 * fb0 + 32 * i + 16 * hi + 4 * g + r;
  return 144 * (fb0 + 1) + 32 * (i - 5) + 16 * hi + 4 * g + r;
}
constexpr int K0_BLOCKS = 1024;
__global__ __launch_bounds__(256) void k0(const u16* __restrict__ x, const u16* __restrict__ wqkv, const u16* __restrict__ wo, const u16* __restrict__ win,
                                          const u16* __restrict__ wsp, const u16* __restrict__ wout, char* __restrict__ ws) {
  const int lane = threadIdx.x & 63, i = lane & 15, g = lane >> 4;
  const int gw = blockIdx.x * 4 + (threadIdx.x >> 6);
  if (blockIdx.x == K0_BLOCKS - 1) {
    for (int c = threadIdx.x; c < (2048 * 16 + 16 * 256) / 16; c += 256) *(u32x4*)(ws + OFF_RS + c * 16) = (u32x4){0, 0, 0, 0};
  }
  u32x4 wt[2];
#pragma unroll
  for (int e = 0; e < 2; ++e) {
    int fr = gw * 2 + e, m = fr & 7, ks = (fr >> 3) & 63, tp = fr >> 9;
    wt[e] = *(const u32x4*)(wsp + (size_t)prow(tp * 8 + m, i) * 2048 + ks * 32 + g * 8);
  }
  unsigned w1[2][8];
#pragma unroll
  for (int e = 0; e < 2; ++e) {
    int fr = gw + e * 4096;
    if (fr < 7488) {
      int cg = fr / 234, r = fr - cg * 234, half = cg & 1, fb = cg >> 1;
      const u16* src; int ld, ks; bool ok = true;
      if (r < 72) {
        ks = r >> 2; int c = (r & 3) * 16 + i, col;
        if (c < 18) col = 576 + 18 * cg + c; else if (c >= 24 && c < 42) col = 1152 + 18 * cg + c - 24; else if (c >= 44 && c < 62) col = 18 * cg + c - 44; else { col = 0; ok = false; }
        src = wqkv + col; ld = 1728;
      } else {
        int r2 = r - 72, n2 = 5 - half, col;
        if (r2 < 18 * n2) { ks = r2 / n2; int n = r2 - ks * n2; col = 2304 + 144 * fb + 16 * (half * 5 + n) + i; }
        else { int r3 = r2 - 18 * n2, n1 = 4 + half; ks = r3 / n1; int n = r3 - ks * n1; col = 144 * fb + 16 * (half * 4 + n) + i; }
        src = win + col; ld = 4608;
      }
      src += (size_t)(ks * 32 + g * 8) * ld;
#pragma unroll
      for (int j = 0; j < 8; ++j) { unsigned v = src[(size_t)j * ld]; w1[e][j] = ok ? v : 0u; }
    }
  }
  unsigned wc[8];
  if (gw < 3600) {
    int n = gw % 5, ks = (gw / 5) % 90, ct = gw / 450;
    int d = (ct < 7 ? ct * 72 : 496) + n * 16 + i;
#pragma unroll
    for (int j = 0; j < 8; ++j) { int k = wc_krow(ks, g, j); wc[j] = k < 2304 ? wout[(size_t)k * 576 + d] : wo[(size_t)(k - 2304) * 576 + d]; }
  }
#pragma unroll
  for (int e = 0; e < 2; ++e) *(u32x4*)(ws + OFF_WT + (size_t)(gw * 2 + e) * 1024 + lane * 16) = wt[e];
#pragma unroll
  for (int e = 0; e < 2; ++e) {
    int fr = gw + e * 4096;
    if (fr < 7488) {
      u32x4 v = {w1[e][0] | (w1[e][1] << 16), w1[e][2] | (w1[e][3] << 16), w1[e][4] | (w1[e][5] << 16), w1[e][6] | (w1[e][7] << 16)};
      *(u32x4*)(ws + OFF_W1 + (size_t)fr * 1024 + lane * 16) = v;
    }
  }
  if (gw < 3600) {
    u32x4 v = {wc[0] | (wc[1] << 16), wc[2] | (wc[3] << 16), wc[4] | (wc[5] << 16), wc[6] | (wc[7] << 16)};
    *(u32x4*)(ws + OFF_WC + (size_t)gw * 1024 + lane * 16) = v;
  }
  if (gw >= 3840) {
    const int u = gw - 3840, tt = u >> 1, slot = (u & 1) * 8 + (lane >> 3), c0 = lane & 7;
    const u16* xr = x + (size_t)prow(tt, slot) * 576 + c0 * 8;
    u32x4 v[9];
#pragma unroll
    for (int k = 0; k < 9; ++k) v[k] = *(const u32x4*)(xr + k * 64);
    float s = 0.f;
#pragma unroll
    for (int k = 0; k < 9; ++k)
#pragma unroll
      for (int j = 0; j < 4; ++j) s += bf_lo(v[k][j]) + bf_hi(v[k][j]);
    s += __shfl_xor(s, 1); s += __shfl_xor(s, 2); s += __shfl_xor(s, 4);
    const float mean = s * (1.f / 576.f);
    float q = 0.f;
#pragma unroll
    for (int k = 0; k < 9; ++k)
#pragma unroll
      for (int j = 0; j < 4; ++j) { float a = bf_lo(v[k][j]) - mean, b = bf_hi(v[k][j]) - mean; q += a * a + b * b; }
    q += __shfl_xor(q, 1); q += __shfl_xor(q, 2); q += __shfl_xor(q, 4);
    const float mb = rbf(mean), sd = rbf(sqrtf(rbf(rbf(q * (1.f / 576.f)) + 1e-5f))), rs = 1.f / sd;
    char* dst = ws + OFF_XT + (size_t)tt * 18432 + slot * 16;
#pragma unroll
    for (int k = 0; k < 9; ++k) {
      u32x4 o;
#pragma unroll
      for (int j = 0; j < 4; ++j) o[j] = pk_bf16(rbf(bf_lo(v[k][j]) - mb) * rs, rbf(bf_hi(v[k][j]) - mb) * rs);
      int c = c0 + 8 * k;
      *(u32x4*)(dst + (c >> 2) * 1024 + (c & 3) * 256) = o;
    }
  }
}

extern "C" void kernel_launch(void* const* d_in, const int* in_sizes, int n_in, void* d_out, int out_size, void* d_ws, size_t ws_size, hipStream_t stream) {
  (void)in_sizes; (void)n_in; (void)out_size; (void)ws_size;
  const u16* x = (const u16*)d_in[0];
  char* ws = (char*)d_ws;
  (void)hipFuncSetAttribute((const void*)kA, hipFuncAttributeMaxDynamicSharedMemorySize, KA_LDS);
  (void)hipFuncSetAttribute((const void*)kB, hipFuncAttributeMaxDynamicSharedMemorySize, KB_LDS);
  (void)hipFuncSetAttribute((const void*)kC, hipFuncAttributeMaxDynamicSharedMemorySize, KC_LDS);
  k0<<<K0_BLOCKS, 256, 0, stream>>>(x, (const u16*)d_in[1], (const u16*)d_in[2], (const u16*)d_in[4], (const u16*)d_in[5], (const u16*)d_in[7], ws);
  kA<<<512, 256, KA_LDS, stream>>>(ws, ws);
  kB<<<256, 512, KB_LDS, stream>>>(ws, ws, (const u16*)d_in[6]);
  kC<<<256, 320, KC_LDS, stream>>>(ws, x, (u16*)d_out);
}
```

```cpp
#include <hip/hip_runtime.h>
#include <hip/hip_bf16.h>
#include <stdint.h>

typedef unsigned short u16;
typedef __attribute__((ext_vector_type(8))) short bf16x8;
typedef __attribute__((ext_vector_type(4))) short bf16x4;
typedef __attribute__((ext_vector_type(4))) float f32x4;
typedef __attribute__((ext_vector_type(4))) unsigned u32x4;
typedef __attribute__((ext_vector_type(2))) unsigned u32x2;
typedef __amdgpu_buffer_rsrc_t rsrc_t;

constexpr int S_ = 2048, D_ = 576, F_ = 4608, FH_ = 2304;
constexpr size_t OFF_Z2T = 0;
constexpr size_t SZ_Z = (size_t)16 * 64 * 9 * 1024;
constexpr size_t OFF_Z1T = OFF_Z2T + SZ_Z;
constexpr size_t OFF_HT = OFF_Z1T + SZ_Z;
constexpr size_t OFF_WT = OFF_HT + SZ_Z;
constexpr size_t SZ_WT = (size_t)16 * 64 * 8 * 1024;
constexpr size_t OFF_OT = OFF_WT + SZ_WT;
constexpr size_t SZ_OT = (size_t)128 * 144 * 128;
constexpr size_t OFF_WC = OFF_OT + SZ_OT;
constexpr size_t SZ_WC = (size_t)8 * 90 * 5 * 1024;
constexpr size_t OFF_XT = OFF_WC + SZ_WC;
constexpr size_t SZ_XT = (size_t)128 * 18 * 1024;
constexpr size_t OFF_W1 = OFF_XT + SZ_XT;
constexpr size_t SZ_W1 = (size_t)32 * 234 * 1024;
constexpr size_t OFF_RS = OFF_W1 + SZ_W1;
constexpr size_t OFF_CNT = OFF_RS + 2048 * 16;
constexpr size_t OFF_ST = OFF_CNT + 16 * 256;
constexpr size_t WS_END = OFF_ST + (1 << 20);

#define RFL(x) __builtin_amdgcn_readfirstlane(x)
#define SCHED() __builtin_amdgcn_sched_barrier(0)
__device__ __forceinline__ rsrc_t mk_rsrc(const void* p, int bytes) { return __builtin_amdgcn_make_buffer_rsrc((void*)p, (short)0, bytes, 0x00020000); }
__device__ __forceinline__ float bf_lo(unsigned w) { return __uint_as_float(w << 16); }
__device__ __forceinline__ float bf_hi(unsigned w) { return __uint_as_float(w & 0xffff0000u); }
__device__ __forceinline__ unsigned pk_bf16(float lo, float hi) {
  typedef __attribute__((ext_vector_type(2))) float f32x2; typedef __attribute__((ext_vector_type(2))) __bf16 bf2;
  f32x2 v = {lo, hi}; bf2 r = __builtin_convertvector(v, bf2); return __builtin_bit_cast(unsigned, r);
}
__device__ __forceinline__ float rbf(float f) { return bf_hi(pk_bf16(0.f, f)); }
__host__ __device__ __forceinline__ int prow(int tt, int i) { return 32 * (tt >> 1) + 8 * (i >> 2) + 4 * (tt & 1) + (i & 3); }
__device__ __forceinline__ void st16(void* p, u32x4 v) { asm volatile("global_store_dwordx4 %0, %1, off sc1\n\ts_nop 1" :: "v"(p), "v"(v) : "memory"); }
#define MFMA(a, b, c) __builtin_amdgcn_mfma_f32_16x16x32_bf16(a, b, c, 0, 0, 0)

#define DMA16(lds, voff, srd, soff, imm) asm volatile("s_mov_b32 m0, %0\n\ts_nop 0\n\tbuffer_load_dwordx4 %1, %2, %3 offen offset:" #imm " lds" :: "s"(lds), "v"(voff), "s"(srd), "s"(soff) : "memory")
#define DMA16H(lds, voff, srd, soff) asm volatile("s_mov_b32 m0, %0\n\ts_lshr_b64 exec, -1, 32\n\tbuffer_load_dwordx4 %1, %2, %3 offen lds\n\ts_mov_b64 exec, -1" :: "s"(lds), "v"(voff), "s"(srd), "s"(soff) : "memory")
typedef __attribute__((address_space(3))) char lds_char;
#define LDS_RD128(addr) (*(const __attribute__((address_space(3))) bf16x8*)(addr))
#define AMFMA(acc, a, b) asm volatile("v_mfma_f32_16x16x32_bf16 %0, %1, %2, %0" : "+v"(acc) : "v"(a), "v"(b))
#define ADSRD(dst, addr, off) asm volatile("ds_read_b128 %0, %1 offset:%2" : "=v"(dst) : "v"(addr), "i"(off))
#define OPAQUE(x) asm volatile("" : "+v"(x))
#define BLOAD16(dst, voff, srd, soff, imm) asm volatile("buffer_load_dwordx4 %0, %1, %2, %3 offen offset:" #imm : "=v"(dst) : "v"(voff), "s"(srd), "s"(soff))

constexpr int K0_PITCH = 1168;
__device__ __forceinline__ int wc_krow(int ks, int g, int j) {
  int r = j & 3, hi = j >> 2;
  if (ks >= 72) { int u = ks - 72; return 2304 + 4 * (8 * u + 4 * hi + g) + r; }
  int w = ks / 18, pi = (ks % 18) / 9, i = ks % 9, fb0 = 4 * w + 2 * pi;
  if (i == 4) return 144 * (fb0 + hi) + 128 + 4 * g + r;
  if (i < 4) return 144 * fb0 + 32 * i + 16 * hi + 4 * g + r;
  return 144 * (fb0 + 1) + 32 * (i - 5) + 16 * hi + 4 * g + r;
}
__device__ __forceinline__ u32x4 tr_frag(unsigned a0, unsigned a1) {
  u32x2 lo, hi;
  asm volatile("ds_read_b64_tr_b16 %0, %2\n\tds_read_b64_tr_b16 %1, %3\n\ts_waitcnt lgkmcnt(0)" : "=&v"(lo), "=&v"(hi) : "v"(a0), "v"(a1) : "memory");
  return (u32x4){lo[0], lo[1], hi[0], hi[1]};
}
#define KA_SLEEP 32
constexpr int KA_SLOT = 10240, KA_ATT = 6 * KA_SLOT, KA_PITCH = 144, KA_LDS = KA_ATT + 142 * KA_PITCH;
template <int FAST> struct KaCfg {
  static constexpr int NZ2 = 4 + FAST, NZ1 = 4 + FAST;
  static constexpr int nt(int s) { return s < 9 ? 4 : 4 + FAST; }
  static constexpr int goff(int s) { return s < 9 ? s * 8192 : 73728 + (s - 9) * (4 + FAST) * 2048; }
  static constexpr int ops(int s) { return s > 26 ? 0 : 2 + (nt(s) == 5 ? 1 : 0) + (s < 9 ? 4 + ((s & 1) ? 0 : 1) : 0); }
  static constexpr int wait(int s) { return s == 20 ? ops(23) + ops(24) : ops(s + 2) + ops(s + 3) + ops(s + 4) + ((s >= 9 && s <= 12) ? 6 : 0); }
};
template <int V> struct IC { static constexpr int v = V; };
#define BLD(dst, voff, srd, soff, IMM) asm volatile("buffer_load_dwordx4 %0, %1, %2, %3 offen offset:%4" : "=v"(dst) : "v"(voff), "s"(srd), "s"(soff), "i"(IMM))
#define AMFMA0(acc, a, b) asm volatile("v_mfma_f32_16x16x32_bf16 %0, %1, %2, 0" : "=&v"(acc) : "v"(a), "v"(b))
typedef __attribute__((ext_vector_type(2))) float f32x2;
typedef __attribute__((ext_vector_type(2))) __bf16 bf2_t;
__device__ __forceinline__ float dot2(unsigned a, unsigned b, float c) { return __builtin_amdgcn_fdot2_f32_bf16(__builtin_bit_cast(bf2_t, a), __builtin_bit_cast(bf2_t, b), c, false); }
__device__ __forceinline__ f32x2 gelu2(f32x2 x) {
  f32x2 t = x * x, p = t * (-0.10294324f) + (-2.3022082f), e = x * p;
  f32x2 d = {__builtin_amdgcn_exp2f(e[0]) + 1.f, __builtin_amdgcn_exp2f(e[1]) + 1.f};
  f32x2 r = {__builtin_amdgcn_rcpf(d[0]), __builtin_amdgcn_rcpf(d[1])};
  return x * r;
}
template <int CTRL> __device__ __forceinline__ float dpp_add(float v) {
  return v + __builtin_bit_cast(float, __builtin_amdgcn_update_dpp(0, __builtin_bit_cast(int, v), CTRL, 0xf, 0xf, true));
}

__device__ __forceinline__ float xmax16(float v) { auto r = __builtin_amdgcn_permlane16_swap(__float_as_uint(v), __float_as_uint(v), false, false); return fmaxf(__uint_as_float(r[0]), __uint_as_float(r[1])); }
__device__ __forceinline__ float xmax32(float v) { auto r = __builtin_amdgcn_permlane32_swap(__float_as_uint(v), __float_as_uint(v), false, false); return fmaxf(__uint_as_float(r[0]), __uint_as_float(r[1])); }
__device__ __forceinline__ float xadd16(float v) { auto r = __builtin_amdgcn_permlane16_swap(__float_as_uint(v), __float_as_uint(v), false, false); return __uint_as_float(r[0]) + __uint_as_float(r[1]); }
__device__ __forceinline__ float xadd32(float v) { auto r = __builtin_amdgcn_permlane32_swap(__float_as_uint(v), __float_as_uint(v), false, false); return __uint_as_float(r[0]) + __uint_as_float(r[1]); }
#define PRIO_HI()
#define PRIO_LO()
#define LBAR() asm volatile("s_waitcnt lgkmcnt(0)\n\ts_barrier" ::: "memory")
__device__ __forceinline__ float swap1(float v) { return __builtin_bit_cast(float, __builtin_amdgcn_update_dpp(0, __builtin_bit_cast(int, v), 0xB1, 0xf, 0xf, true)); }
template <int FAST>
__device__ __forceinline__ void ka_body(char* smem, const char* __restrict__ ws_c, char* __restrict__ ws, const int p, const int cg, const int sj, const u16* __restrict__ wsp, const u16* __restrict__ wout, const u16* __restrict__ wo) {
  typedef KaCfg<FAST> C;
  const int tid = threadIdx.x, lane = tid & 63, li = lane & 15, lg = lane >> 4;
  const int w = RFL(tid >> 6), fb = cg >> 1;
  const rsrc_t rW = mk_rsrc(ws_c + OFF_W1 + (size_t)fb * (468 * 1024) + (FAST ? 0 : 252 * 1024), (FAST ? 252 : 216) * 1024);
  const rsrc_t rX = mk_rsrc(ws_c + OFF_XT, (int)SZ_XT);
  const unsigned ldsb = (unsigned)(size_t)(__attribute__((address_space(3))) char*)smem;
  const unsigned vl16 = lane * 16;
  const unsigned vX = vl16 + (unsigned)(p * 8 + w * 2) * 18432u;
  const unsigned vH = p == 0 ? 0x80000000u : (unsigned)(p * 8 - 2 + (li >> 3)) * 18432u + (unsigned)(lg * 16 + (li | 8)) * 16u + (unsigned)w * 1024u;
  const unsigned sF = RFL((unsigned)w * 2048u), sH = RFL(8192u + (unsigned)w * 512u);
  const unsigned rd = ldsb + vl16;
  constexpr int KA_WARM = 0;
  bf16x8 X[2][18], HX[5], F[5];
  f32x4 acc[2][5], hacc[3];
#pragma unroll
  for (int n = 0; n < 3; ++n) hacc[n] = (f32x4){0.f, 0.f, 0.f, 0.f};

  bf16x8 (&X_)[2][18] = X; bf16x8 (&HX_)[5] = HX; bf16x8 (&F_)[5] = F; f32x4 (&acc_)[2][5] = acc; f32x4 (&hacc_)[3] = hacc;
  const rsrc_t rW_ = rW, rX_ = rX; const unsigned vl16_ = vl16, vX_ = vX, vH_ = vH, rd_ = rd;
#define KA_CAP bf16x8 (&X)[2][18] = X_; bf16x8 (&HX)[5] = HX_; bf16x8 (&F)[5] = F_; f32x4 (&acc)[2][5] = acc_; f32x4 (&hacc)[3] = hacc_; \
    const rsrc_t rW = rW_, rX = rX_; const unsigned vl16 = vl16_, vX = vX_, vH = vH_, rd = rd_; (void)X; (void)HX; (void)F; (void)acc; (void)hacc; (void)rW; (void)rX; (void)vl16; (void)vX; (void)vH; (void)rd;
  auto vop = [&](auto S_, auto K_) {
    KA_CAP
    constexpr int s = decltype(S_)::v, k = decltype(K_)::v;
    if constexpr (s <= 26) {
      constexpr int slot = s % 6, nd = 2 + (C::nt(s) == 5 ? 1 : 0);
      if constexpr (k == 0) { unsigned lo = ldsb + slot * KA_SLOT + sF, so = C::goff(s) + sF; DMA16(lo, vl16, rW, so, 0); }
      else if constexpr (k == 1) { unsigned lo = ldsb + slot * KA_SLOT + sF, so = C::goff(s) + sF; DMA16(lo, vl16, rW, so, 1024); }
      else if constexpr (k == 2 && nd == 3) { unsigned lo = ldsb + slot * KA_SLOT + sH, so = C::goff(s) + sH; DMA16H(lo, vl16, rW, so); }
      else if constexpr (s < 9 && k >= 2 && k < 6) {
        constexpr int m = (k - 2) & 1, ks = 2 * s + ((k - 2) >> 1);
        BLD(X[m][ks], vX, rX, m * 18432 + (ks & ~3) * 1024, (ks & 3) * 1024);
      } else if constexpr (s < 9 && k == 6 && !(s & 1)) { BLD(HX[s >> 1], vH, rX, (s >> 1) * 4096, 0); }
    }
  };
  auto group = [&](auto S_) { vop(S_, IC<0>{}); vop(S_, IC<1>{}); vop(S_, IC<2>{}); vop(S_, IC<3>{}); vop(S_, IC<4>{}); vop(S_, IC<5>{}); vop(S_, IC<6>{}); };
  auto sync = [&](auto S_) {
    KA_CAP
    constexpr int s = decltype(S_)::v;
    if constexpr (s < 26) {
      if constexpr (s + 1 < 9) {
        constexpr int k0 = 2 * (s + 1);
        asm volatile("s_waitcnt vmcnt(%5)" : "+v"(X[0][k0]), "+v"(X[1][k0]), "+v"(X[0][k0 + 1]), "+v"(X[1][k0 + 1]), "+v"(HX[(s + 1) >> 1]) : "i"(C::wait(s)) : "memory");
      } else asm volatile("s_waitcnt vmcnt(%0)" :: "i"(C::wait(s)) : "memory");
      __builtin_amdgcn_s_barrier();
    }
  };
  auto step = [&](auto S_, auto H_) {
    KA_CAP
    constexpr int s = decltype(S_)::v, h = decltype(H_)::v;
    constexpr int NT = C::nt(s), job = s / 9, ks = 2 * (s % 9) + h;
    constexpr int s2 = h ? s + 1 : s, h2 = h ^ 1, NT2 = s2 <= 26 ? C::nt(s2) : 0;
    constexpr int roff = (s2 % 6) * KA_SLOT + h2 * NT2 * 1024;
    if constexpr (ks == 0) asm volatile("s_waitcnt lgkmcnt(0)" ::: "memory");
    if constexpr (job == 0) {
      if (w == (ks & 3)) {
        asm volatile("s_waitcnt lgkmcnt(1)" ::: "memory");
#pragma unroll
        for (int n = 0; n < 3; ++n) AMFMA(hacc[n], F[n], HX[ks >> 2]);
      }
    }
#pragma unroll
    for (int n = 0; n < NT; ++n) {
      if constexpr (ks != 0) asm volatile("s_waitcnt lgkmcnt(%0)" :: "i"(NT - 1) : "memory");
      if constexpr (job == 1) {
        if constexpr (ks == 0) { AMFMA0(acc[0][n], X[0][ks], F[n]); AMFMA0(acc[1][n], X[1][ks], F[n]); }
        else { AMFMA(acc[0][n], X[0][ks], F[n]); AMFMA(acc[1][n], X[1][ks], F[n]); }
      } else {
        if constexpr (ks == 0) { AMFMA0(acc[0][n], F[n], X[0][ks]); AMFMA0(acc[1][n], F[n], X[1][ks]); }
        else { AMFMA(acc[0][n], F[n], X[0][ks]); AMFMA(acc[1][n], F[n], X[1][ks]); }
      }
      if (n < NT2) ADSRD(F[n], rd, roff + n * 1024);
      if constexpr (h == 1) {
        if (n == 0) { vop(IC<s + 5>{}, IC<0>{}); vop(IC<s + 5>{}, IC<2>{}); }
        if (n == 1) { vop(IC<s + 5>{}, IC<1>{}); vop(IC<s + 5>{}, IC<3>{}); }
        if (n == 2) { vop(IC<s + 5>{}, IC<4>{}); vop(IC<s + 5>{}, IC<5>{}); }
        if (n == 3) { vop(IC<s + 5>{}, IC<6>{}); }
      }
    }
    if constexpr (NT2 > NT) ADSRD(F[4], rd, roff + 4 * 1024);
  };
#define KA_ACC_DONE() do { asm volatile("s_nop 15" : "+v"(acc[0][0]), "+v"(acc[0][1]), "+v"(acc[0][2]), "+v"(acc[0][3]), "+v"(acc[1][0]), "+v"(acc[1][1]), "+v"(acc[1][2]), "+v"(acc[1][3]) :: "memory"); \
    if constexpr (FAST) asm volatile("" : "+v"(acc[0][4]), "+v"(acc[1][4]) :: "memory"); } while (0)
  auto iter = [&](auto S_) { step(S_, IC<0>{}); sync(S_); step(S_, IC<1>{}); };

  group(IC<0>{}); group(IC<1>{});
  asm volatile("s_waitcnt vmcnt(%5)" : "+v"(X[0][0]), "+v"(X[1][0]), "+v"(X[0][1]), "+v"(X[1][1]), "+v"(HX[0]) : "i"(C::ops(1) + KA_WARM) : "memory");
  __builtin_amdgcn_s_barrier();
#pragma unroll
  for (int n = 0; n < 4; ++n) ADSRD(F[n], rd, n * 1024);

  step(IC<0>{}, IC<0>{}); group(IC<2>{}); group(IC<3>{}); group(IC<4>{}); sync(IC<0>{}); step(IC<0>{}, IC<1>{});
  iter(IC<1>{}); iter(IC<2>{}); iter(IC<3>{}); iter(IC<4>{}); iter(IC<5>{}); iter(IC<6>{}); iter(IC<7>{}); iter(IC<8>{});
  asm volatile("s_nop 15" : "+v"(acc[0][0]), "+v"(acc[0][1]), "+v"(acc[0][2]), "+v"(acc[0][3]), "+v"(acc[1][0]), "+v"(acc[1][1]), "+v"(acc[1][2]), "+v"(acc[1][3]), "+v"(hacc[0]), "+v"(hacc[1]), "+v"(hacc[2]) :: "memory");

  SCHED();
  {
    char* att = smem + KA_ATT;
    char* hp = smem + 2 * KA_SLOT;
#pragma unroll
    for (int n = 0; n < 3; ++n) if (n != w) *(f32x4*)(hp + (n * 3 + (w > n ? w - 1 : w)) * 1024 + vl16) = hacc[n];
#pragma unroll
    for (int m = 0; m < 2; ++m) {
      char* rp = att + (14 + w * 32 + 8 * (li >> 2) + 4 * m + (li & 3)) * KA_PITCH + lg * 8;
#pragma unroll
      for (int n = 0; n < 4; ++n) *(u32x2*)(rp + n * 32) = (u32x2){pk_bf16(acc[m][n][0], acc[m][n][1]), pk_bf16(acc[m][n][2], acc[m][n][3])};
    }
    LBAR();
    if (w < 3) {
      f32x4 a = w == 0 ? hacc[0] : (w == 1 ? hacc[1] : hacc[2]);
#pragma unroll
      for (int q = 0; q < 3; ++q) a += *(const f32x4*)(hp + (w * 3 + q) * 1024 + vl16);
      int ho = (li < 8) ? 8 * (li >> 2) + (li & 3) : 8 * ((li - 8) >> 2) + 4 + (li & 3);
      if (ho >= 2) *(u32x2*)(att + (ho - 2) * KA_PITCH + w * 32 + lg * 8) = (u32x2){pk_bf16(a[0], a[1]), pk_bf16(a[2], a[3])};
    }
    LBAR();
    const unsigned attb = ldsb + KA_ATT;
    float mbias[8];
#pragma unroll
    for (int e = 0; e < 8; ++e) { int d = (e >> 2) * 16 + lg * 4 + (e & 3) - li; mbias[e] = (!(d & 1) && d >= 2 && d <= 16) ? 0.f : -__builtin_inff(); }
    bf16x8 Q[2], K[2][2]; u32x2 V[2][2][2];
#pragma unroll
    for (int jj = 0; jj < 2; ++jj) {
      const int j = w * 2 + jj;
      Q[jj] = *(const bf16x8*)(att + (14 + 16 * j + li) * KA_PITCH + 80 + (lg < 3 ? lg : 2) * 16);
#pragma unroll
      for (int kt = 0; kt < 2; ++kt) {
        int R = 16 * j - 2 + 16 * kt + li; R = R < 0 ? 0 : R;
        K[jj][kt] = *(const bf16x8*)(att + R * KA_PITCH + lg * 16);
        int Rv = 16 * j - 2 + 16 * kt + 4 * lg + (li >> 2); Rv = Rv < 0 ? 0 : Rv;
        const unsigned va = attb + Rv * KA_PITCH + 40 + (li & 3) * 8;
        asm volatile("ds_read_b64_tr_b16 %0, %2\n\tds_read_b64_tr_b16 %1, %2 offset:32" : "=&v"(V[jj][0][kt]), "=&v"(V[jj][1][kt]) : "v"(va) : "memory");
      }
    }
    f32x4 S[2][2];
#pragma unroll
    for (int jj = 0; jj < 2; ++jj) {
      if (lg == 3) Q[jj] = (bf16x8){0, 0, 0, 0, 0, 0, 0, 0};
#pragma unroll
      for (int kt = 0; kt < 2; ++kt) S[jj][kt] = MFMA(K[jj][kt], Q[jj], ((f32x4){0.f, 0.f, 0.f, 0.f}));
    }
    bf16x8 P[2];
#pragma unroll
    for (int jj = 0; jj < 2; ++jj) {
      float l[8];
#pragma unroll
      for (int e = 0; e < 8; ++e) l[e] = S[jj][e >> 2][e & 3] + mbias[e];
      if (p == 0 && w == 0 && jj == 0) {
#pragma unroll
        for (int e = 0; e < 4; ++e) l[e] = -__builtin_inff();
      }
      float mx = fmaxf(fmaxf(fmaxf(l[0], l[1]), fmaxf(l[2], l[3])), fmaxf(fmaxf(l[4], l[5]), fmaxf(l[6], l[7])));
      mx = xmax16(mx); mx = xmax32(mx);
      constexpr float CE = 0.23570226f * 1.44269504f;
      const float nm = -mx * CE;
      float sum = 0.f;
#pragma unroll
      for (int e = 0; e < 8; ++e) { l[e] = __builtin_amdgcn_exp2f(l[e] * CE + nm); sum += l[e]; }
      sum = xadd16(sum); sum = xadd32(sum);
      const float inv = __builtin_amdgcn_rcpf(sum);
      u32x4 pk = {pk_bf16(l[0] * inv, l[1] * inv), pk_bf16(l[2] * inv, l[3] * inv), pk_bf16(l[4] * inv, l[5] * inv), pk_bf16(l[6] * inv, l[7] * inv)};
      P[jj] = __builtin_bit_cast(bf16x8, pk);
    }
    asm volatile("s_waitcnt lgkmcnt(0)" : "+v"(V[0][0][0]), "+v"(V[0][0][1]), "+v"(V[0][1][0]), "+v"(V[0][1][1]), "+v"(V[1][0][0]), "+v"(V[1][0][1]), "+v"(V[1][1][0]), "+v"(V[1][1][1]) :: "memory");
#pragma unroll
    for (int jj = 0; jj < 2; ++jj) {
      f32x4 O[2];
#pragma unroll
      for (int dt = 0; dt < 2; ++dt) {
        u32x4 vv = {V[jj][dt][0][0], V[jj][dt][0][1], V[jj][dt][1][0], V[jj][dt][1][1]};
        O[dt] = MFMA(__builtin_bit_cast(bf16x8, vv), P[jj], ((f32x4){0.f, 0.f, 0.f, 0.f}));
      }
      const int tl = jj * 16 + li, tt = p * 8 + w * 2 + ((tl >> 2) & 1), slot = (tl >> 3) * 4 + (tl & 3);
      char* ob = ws + OFF_OT + (size_t)tt * 18432 + slot * 8;
      const int d0 = cg * 18 + lg * 4, d1 = d0 + 2, d2 = cg * 18 + 16;
      *(unsigned*)(ob + (d0 >> 2) * 128 + (d0 & 3) * 2) = pk_bf16(O[0][0], O[0][1]);
      *(unsigned*)(ob + (d1 >> 2) * 128 + (d1 & 3) * 2) = pk_bf16(O[0][2], O[0][3]);
      if (lg == 0) *(unsigned*)(ob + (d2 >> 2) * 128 + (d2 & 3) * 2) = pk_bf16(O[1][0], O[1][1]);
    }
  }
  SCHED();
  PRIO_LO();

  iter(IC<9>{}); iter(IC<10>{}); iter(IC<11>{}); iter(IC<12>{}); iter(IC<13>{}); iter(IC<14>{}); iter(IC<15>{}); iter(IC<16>{}); iter(IC<17>{});
  KA_ACC_DONE();

  SCHED();
  PRIO_HI();
  unsigned zs[C::NZ2][4];
  {
#pragma unroll
    for (int n = 0; n < C::NZ2; ++n) {
#pragma unroll
      for (int m = 0; m < 2; ++m) {
        f32x2 a = gelu2((f32x2){acc[m][n][0], acc[m][n][1]}), b = gelu2((f32x2){acc[m][n][2], acc[m][n][3]});
        zs[n][2 * m] = pk_bf16(a[0], a[1]); zs[n][2 * m + 1] = pk_bf16(b[0], b[1]);
      }
    }
    f32x2 s1[4], s2[4];
#pragma unroll
    for (int j = 0; j < 4; ++j) {
      s1[j] = (f32x2){0.f, 0.f}; s2[j] = s1[j];
#pragma unroll
      for (int n = 0; n < C::NZ2; ++n) { f32x2 v = {bf_lo(zs[n][j]), bf_hi(zs[n][j])}; s1[j] += v; s2[j] += v * v; }
    }
    float r[16];
#pragma unroll
    for (int j = 0; j < 4; ++j) { r[2 * j] = s1[j][0]; r[2 * j + 1] = s1[j][1]; r[8 + 2 * j] = s2[j][0]; r[9 + 2 * j] = s2[j][1]; }
#pragma unroll
    for (int j = 0; j < 16; ++j) { float v = r[j]; v = dpp_add<0x128>(v); v = dpp_add<0x124>(v); v = dpp_add<0x122>(v); v = dpp_add<0x121>(v); r[j] = v; }
    float sel = r[0];
#pragma unroll
    for (int j = 1; j < 16; ++j) sel = (li == j) ? r[j] : sel;
    double* dst = (double*)(ws + OFF_RS) + (size_t)(p * 128 + w * 32 + lg * 8 + (li & 7)) * 2 + (li >> 3);
    double dv = (double)sel;
    asm volatile("global_atomic_add_f64 %0, %1, off sc1" :: "v"(dst), "v"(dv) : "memory");
  }
  SCHED();

  PRIO_LO();

  unsigned* cnt = (unsigned*)(ws + OFF_CNT + p * 256);
  iter(IC<18>{}); iter(IC<19>{});
  step(IC<20>{}, IC<0>{}); sync(IC<20>{});
  if (tid == 0) { unsigned one = 1; asm volatile("global_atomic_add %0, %1, off sc1" :: "v"(cnt), "v"(one) : "memory"); }
  step(IC<20>{}, IC<1>{});
  iter(IC<21>{}); iter(IC<22>{}); iter(IC<23>{}); iter(IC<24>{}); iter(IC<25>{});
  unsigned seen;
  asm volatile("global_load_dword %0, %1, off sc1" : "=v"(seen) : "v"(cnt) : "memory");
  iter(IC<26>{});
  KA_ACC_DONE();
  asm volatile("s_waitcnt vmcnt(0)" : "+v"(seen) :: "memory");
  PRIO_HI();
  const unsigned seen_s = RFL(seen);
  const bool early = seen_s >= 32u;
  const char* sp = ws + OFF_RS + (size_t)(p * 128 + w * 32 + (lane & 31)) * 16;
  u32x4 st = {0, 0, 0, 0};
  asm volatile("s_cmp_lt_u32 %2, 32\n\ts_cbranch_scc1 1f\n\tglobal_load_dwordx4 %0, %1, off sc1\n1:" : "+v"(st) : "v"(sp), "s"(seen_s) : "memory", "scc");
  SCHED();
  u32x4 wt[8];
  if constexpr (FAST) {
    const int f0 = (sj * 4 + w) * 8, ks = (f0 >> 3) & 63, tp = f0 >> 9;
#pragma unroll
    for (int m = 0; m < 8; ++m) wt[m] = *(const u32x4*)(wsp + (size_t)prow(tp * 8 + m, li) * 2048 + ks * 32 + lg * 8);
  }
#pragma unroll
  for (int m = 0; m < 2; ++m) {
    char* zp = ws + OFF_Z1T + ((size_t)(p * 8 + w * 2 + m) * 16 + fb) * 4608;
    unsigned pk[C::NZ1][2];
#pragma unroll
    for (int n = 0; n < C::NZ1; ++n) {
      f32x2 a = gelu2((f32x2){acc[m][n][0], acc[m][n][1]}), b = gelu2((f32x2){acc[m][n][2], acc[m][n][3]});
      pk[n][0] = pk_bf16(a[0], a[1]); pk[n][1] = pk_bf16(b[0], b[1]);
    }
#pragma unroll
    for (int q = 0; q < 2; ++q) st16(zp + (FAST * 2 + q) * 1024 + vl16, (u32x4){pk[2 * q][0], pk[2 * q][1], pk[2 * q + 1][0], pk[2 * q + 1][1]});
    if constexpr (FAST) *(u32x2*)(zp + 4096 + lane * 8) = (u32x2){pk[4][0], pk[4][1]};
  }
  SCHED();
  if constexpr (FAST) {
    const int f0 = (sj * 4 + w) * 8;
#pragma unroll
    for (int m = 0; m < 8; ++m) st16(ws + OFF_WT + (size_t)(f0 + m) * 1024 + vl16, wt[m]);
    if (sj < 90) {
      const int ks = sj, base0 = wc_krow(ks, 0, 0), base1 = wc_krow(ks, 0, 4);
      u32x4 v[9];
#pragma unroll
      for (int k = 0; k < 9; ++k) {
        int c = tid + k * 256, r = c / 72, ch = c - r * 72, row = (r < 16 ? base0 : base1 - 16) + r;
        const u16* src = row < 2304 ? wout + (size_t)row * 576 : wo + (size_t)(row - 2304) * 576;
        v[k] = *(const u32x4*)(src + ch * 8);
      }
      LBAR();
#pragma unroll
      for (int k = 0; k < 9; ++k) { int c = tid + k * 256, r = c / 72, ch = c - r * 72; *(u32x4*)(smem + r * K0_PITCH + ch * 16) = v[k]; }
      LBAR();
      const unsigned a0 = ldsb + (4 * lg + (li >> 2)) * K0_PITCH + (li & 3) * 8;
#pragma unroll
      for (int e = 0; e < 10; ++e) {
        int f = w * 10 + e, ct = f / 5, n = f - ct * 5, d0 = (ct < 7 ? ct * 72 : 496) + n * 16;
        u32x4 o = tr_frag(a0 + d0 * 2, a0 + d0 * 2 + 16 * K0_PITCH);
        st16(ws + OFF_WC + (size_t)((ct * 90 + ks) * 5 + n) * 1024 + vl16, o);
      }
    }
  }
  if (!early) {
    while (RFL(seen) < 32u) {
      __builtin_amdgcn_s_sleep(KA_SLEEP);
      asm volatile("global_load_dword %0, %1, off sc1\n\ts_waitcnt vmcnt(0)" : "=v"(seen) : "v"(cnt) : "memory");
    }
  }
  asm volatile("s_cmp_ge_u32 %2, 32\n\ts_cbranch_scc1 1f\n\tglobal_load_dwordx4 %0, %1, off sc1\n1:\n\ts_waitcnt vmcnt(0)" : "+v"(st) : "v"(sp), "s"(seen_s) : "memory", "scc");
  {
    const double sm = __builtin_bit_cast(double, (u32x2){st[0], st[1]}), sq = __builtin_bit_cast(double, (u32x2){st[2], st[3]});
    const double mean = sm * (1.0 / 2304.0);
    const float var = fmaxf((float)(sq * (1.0 / 2304.0) - mean * mean), 0.f);
    const float mb = rbf((float)mean), rs = 1.f / rbf(sqrtf(rbf(rbf(var) + 1e-5f)));
    f32x2* sl = (f32x2*)(smem + KA_ATT + w * 256);
    if (lane < 32) sl[lane] = (f32x2){mb, rs};
    __builtin_amdgcn_s_waitcnt(0xC07F);
    __builtin_amdgcn_wave_barrier();
    f32x4 ms[4];
#pragma unroll
    for (int j = 0; j < 4; ++j) ms[j] = *(const f32x4*)((const char*)sl + lg * 64 + j * 16);
    char* zp = ws + OFF_Z2T + ((size_t)(fb * 64 + p * 4 + w) * 9 + (FAST ? 0 : 5)) * 1024 + vl16;
#pragma unroll
    for (int n = 0; n < C::NZ2; ++n) {
      u32x4 o;
#pragma unroll
      for (int j = 0; j < 4; ++j) o[j] = pk_bf16((bf_lo(zs[n][j]) - ms[j][0]) * ms[j][1], (bf_hi(zs[n][j]) - ms[j][2]) * ms[j][3]);
      st16(zp + n * 1024, o);
    }
  }
}
__global__ __launch_bounds__(256, 2) void kA(const char* __restrict__ ws_c, char* __restrict__ ws, const u16* __restrict__ wsp, const u16* __restrict__ wout, const u16* __restrict__ wo) {
  extern __shared__ __attribute__((aligned(16))) char smem[];
  const int b = blockIdx.x, xcd = b & 7, idx = b >> 3;
  const int p = (xcd & 1) * 8 + (idx & 7), q = idx >> 3, cg = (xcd >> 1) * 8 + (q & 3) * 2 + (q >> 2);
  if (idx < 32) ka_body<1>(smem, ws_c, ws, p, cg, b, wsp, wout, wo); else ka_body<0>(smem, ws_c, ws, p, cg, b, wsp, wout, wo);
}

constexpr int KB_STAGE = 36864;
constexpr int KB_LDS = 4 * KB_STAGE;
__global__ __launch_bounds__(512, 2) void kB(const char* __restrict__ ws_c, char* __restrict__ ws, const u16* __restrict__ bsp) {
  extern __shared__ __attribute__((aligned(16))) char smem[];
  const int tid = threadIdx.x, lane = tid & 63;
  const int w = RFL(tid >> 6), wm = w & 3, kh = w >> 2;
  const int b = blockIdx.x, xcd = b & 7, idx = b >> 3;
  const int tp = (xcd & 1) * 8 + (idx & 7), fb = (xcd >> 1) * 4 + (idx >> 3);
  const rsrc_t rZ = mk_rsrc(ws_c + OFF_Z2T + (size_t)fb * (64 * 9216), 64 * 9216);
  const rsrc_t rW = mk_rsrc(ws_c + OFF_WT + (size_t)tp * (64 * 8192), 64 * 8192);
  const unsigned ldsb = (unsigned)(size_t)(__attribute__((address_space(3))) char*)smem;
  const unsigned vl16 = lane * 16;
  const unsigned vW = vl16 + wm * 2048 + kh * 16384;
  const unsigned dmaF = w * 4096, dmaH = 32768 + w * 512;
  unsigned rd01 = ldsb + kh * 18432 + lane * 16;
  unsigned rd23 = rd01 + 2 * KB_STAGE;

  f32x4 acc[2][9];
#pragma unroll
  for (int m = 0; m < 2; ++m)
#pragma unroll
    for (int n = 0; n < 9; ++n) acc[m][n] = (f32x4){0.f, 0.f, 0.f, 0.f};
  bf16x8 Wr[4][4];
  bf16x8 ZA[9], ZB[9];

#define KB_DMA1(stage, slot, imm) do { \
    unsigned so_ = RFL((unsigned)(stage) * KB_STAGE + dmaF), lo_ = RFL(ldsb + (slot) * KB_STAGE + dmaF); DMA16(lo_, vl16, rZ, so_, imm); } while (0)
#define KB_DMAH(stage, slot) do { \
    unsigned so2_ = RFL((unsigned)(stage) * KB_STAGE + dmaH), lo2_ = RFL(ldsb + (slot) * KB_STAGE + dmaH); DMA16H(lo2_, vl16, rZ, so2_); } while (0)
#define KB_W1(stage, slot, e) do { unsigned sw_ = RFL((unsigned)(stage) * 32768u + ((e) >> 1) * 8192u); \
    if ((e) & 1) BLOAD16(Wr[slot][e], vW, rW, sw_, 1024); else BLOAD16(Wr[slot][e], vW, rW, sw_, 0); } while (0)
#define KB_ALL(stage, slot) do { KB_DMA1(stage, slot, 0); KB_DMA1(stage, slot, 1024); KB_DMA1(stage, slot, 2048); KB_DMA1(stage, slot, 3072); KB_DMAH(stage, slot); \
    KB_W1(stage, slot, 0); KB_W1(stage, slot, 1); KB_W1(stage, slot, 2); KB_W1(stage, slot, 3); } while (0)
#define KB_WAIT(slot, N) asm volatile("s_waitcnt vmcnt(" #N ") lgkmcnt(0)" : "+v"(Wr[slot][0]), "+v"(Wr[slot][1]), "+v"(Wr[slot][2]), "+v"(Wr[slot][3]) :: "memory")

  KB_ALL(0, 0);
  __builtin_amdgcn_s_barrier();
  KB_ALL(1, 1);
  KB_WAIT(0, 9);
  __builtin_amdgcn_s_barrier();

  const rsrc_t rZ1 = mk_rsrc(ws_c + OFF_Z1T + ((size_t)(tp * 8 + wm * 2) * 16 + fb) * 4608, 17 * 4608);
  const rsrc_t rB = mk_rsrc(bsp, 4096);
  u32x4 z1q[2][2]; u32x2 z1s[2]; unsigned biasu[2];
  const unsigned vz1 = lane * 16 + kh * 2048, vz8 = lane * 8 + 4096;
  const unsigned vb = (tp * 128 + wm * 32 + ((lane & 15) >> 2) * 8 + (lane & 3)) * 2;
#define KB_EPI_LD(e) do { \
    if ((e) == 0) BLOAD16(z1q[0][0], vz1, rZ1, 0, 0); \
    if ((e) == 1) BLOAD16(z1q[0][1], vz1, rZ1, 0, 1024); \
    if ((e) == 2) asm volatile("buffer_load_dwordx4 %0, %1, %2, %3 offen" : "=v"(z1q[1][0]) : "v"(vz1), "s"(rZ1), "s"(16 * 4608)); \
    if ((e) == 3) asm volatile("buffer_load_dwordx4 %0, %1, %2, %3 offen offset:1024" : "=v"(z1q[1][1]) : "v"(vz1), "s"(rZ1), "s"(16 * 4608)); \
    if ((e) == 4) asm volatile("buffer_load_dwordx2 %0, %1, %2, 0 offen" : "=v"(z1s[0]) : "v"(vz8), "s"(rZ1)); \
    if ((e) == 5) asm volatile("buffer_load_dwordx2 %0, %1, %2, %3 offen" : "=v"(z1s[1]) : "v"(vz8), "s"(rZ1), "s"(16 * 4608)); \
    if ((e) == 6) asm volatile("buffer_load_ushort %0, %1, %2, 0 offen" : "=v"(biasu[0]) : "v"(vb), "s"(rB)); \
    if ((e) == 7) asm volatile("buffer_load_ushort %0, %1, %2, 0 offen offset:8" : "=v"(biasu[1]) : "v"(vb), "s"(rB)); } while (0)

#define KB_ITER(i, j, MODE, WN) do { \
    _Pragma("unroll") for (int n = 0; n < 9; ++n) { \
      ADSRD(ZA[n], ((j) < 2 ? rd01 : rd23), ((j) & 1) * KB_STAGE + n * 1024); \
      if ((MODE) != 1) { AMFMA(acc[0][n], ZB[n], Wr[((j) + 3) & 3][2]); AMFMA(acc[1][n], ZB[n], Wr[((j) + 3) & 3][3]); } \
      if ((MODE) == 0) { \
        if (n == 0) KB_DMA1((i) + 3, ((j) + 3) & 3, 0); \
        if (n == 2) KB_DMA1((i) + 3, ((j) + 3) & 3, 1024); \
        if (n == 4) KB_W1((i) + 3, ((j) + 3) & 3, 0); \
        if (n == 6) KB_DMA1((i) + 3, ((j) + 3) & 3, 2048); \
        if (n == 8) KB_W1((i) + 3, ((j) + 3) & 3, 1); } \
      if ((MODE) == 2 && !(n & 1)) KB_EPI_LD(n >> 1); } \
    if ((MODE) == 1) { KB_ALL(2, 2); KB_DMA1(3, 3, 0); KB_DMA1(3, 3, 1024); KB_W1(3, 3, 0); KB_DMA1(3, 3, 2048); KB_W1(3, 3, 1); } \
    _Pragma("unroll") for (int n = 0; n < 9; ++n) { \
      if (n == 0) asm volatile("s_waitcnt lgkmcnt(6)" ::: "memory"); \
      if (n == 2) asm volatile("s_waitcnt lgkmcnt(5)" ::: "memory"); \
      if (n == 5) asm volatile("s_waitcnt lgkmcnt(5)" ::: "memory"); \
      ADSRD(ZB[n], ((j) < 2 ? rd01 : rd23), ((j) & 1) * KB_STAGE + 9216 + n * 1024); \
      AMFMA(acc[0][n], ZA[n], Wr[j][0]); AMFMA(acc[1][n], ZA[n], Wr[j][1]); \
      if ((MODE) < 2) { \
        if (n == 0) KB_DMA1((i) + 3, ((j) + 3) & 3, 3072); \
        if (n == 2) KB_W1((i) + 3, ((j) + 3) & 3, 2); \
        if (n == 4) KB_DMAH((i) + 3, ((j) + 3) & 3); \
        if (n == 6) KB_W1((i) + 3, ((j) + 3) & 3, 3); } \
      if ((MODE) == 2 && (n == 0 || n == 2 || n == 4)) KB_EPI_LD(5 + (n >> 1)); } \
    KB_WAIT(((j) + 1) & 3, WN); \
    __builtin_amdgcn_s_barrier(); \
    } while (0)

  KB_ITER(0, 0, 1, 18);
  KB_ITER(1, 1, 0, 18);
  KB_ITER(2, 2, 0, 18);
  KB_ITER(3, 3, 0, 18);
  for (int io = 4; io < 12; io += 4) {
    KB_ITER(io + 0, 0, 0, 18);
    KB_ITER(io + 1, 1, 0, 18);
    KB_ITER(io + 2, 2, 0, 18);
    KB_ITER(io + 3, 3, 0, 18);
  }
  KB_ITER(12, 0, 0, 18);
  KB_ITER(13, 1, 2, 17);
  KB_ITER(14, 2, 3, 8);
  {
#pragma unroll
    for (int n = 0; n < 9; ++n) { ADSRD(ZA[n], rd23, KB_STAGE + n * 1024); AMFMA(acc[0][n], ZB[n], Wr[2][2]); AMFMA(acc[1][n], ZB[n], Wr[2][3]); }
#pragma unroll
    for (int n = 0; n < 9; ++n) {
      if (n == 0) asm volatile("s_waitcnt lgkmcnt(6)" ::: "memory");
      if (n == 2) asm volatile("s_waitcnt lgkmcnt(5)" ::: "memory");
      if (n == 5) asm volatile("s_waitcnt lgkmcnt(5)" ::: "memory");
      ADSRD(ZB[n], rd23, KB_STAGE + 9216 + n * 1024);
      AMFMA(acc[0][n], ZA[n], Wr[3][0]); AMFMA(acc[1][n], ZA[n], Wr[3][1]); }
    asm volatile("s_waitcnt lgkmcnt(0)" ::: "memory");
#pragma unroll
    for (int n = 0; n < 9; ++n) { AMFMA(acc[0][n], ZB[n], Wr[3][2]); AMFMA(acc[1][n], ZB[n], Wr[3][3]); }
  }
  asm volatile("s_nop 15" : "+v"(acc[0][0]), "+v"(acc[0][1]), "+v"(acc[0][2]), "+v"(acc[0][3]), "+v"(acc[0][4]), "+v"(acc[0][5]), "+v"(acc[0][6]), "+v"(acc[0][7]), "+v"(acc[0][8]),
               "+v"(acc[1][0]), "+v"(acc[1][1]), "+v"(acc[1][2]), "+v"(acc[1][3]), "+v"(acc[1][4]), "+v"(acc[1][5]), "+v"(acc[1][6]), "+v"(acc[1][7]), "+v"(acc[1][8]) :: "memory");
  asm volatile("s_waitcnt vmcnt(0)" : "+v"(z1q[0][0]), "+v"(z1q[0][1]), "+v"(z1q[1][0]), "+v"(z1q[1][1]), "+v"(z1s[0]), "+v"(z1s[1]), "+v"(biasu[0]), "+v"(biasu[1]) :: "memory");
  SCHED();
  char* hp = ws + OFF_HT + ((size_t)(tp * 8 + wm * 2) * 16 + fb) * 4608;
  float bias[2] = {bf_lo(biasu[0]), bf_lo(biasu[1])};
  char* ex = smem + wm * 18432 + lane * 16;
  if (kh == 0) {
#pragma unroll
    for (int m = 0; m < 2; ++m)
#pragma unroll
      for (int n = 4; n < 8; ++n) *(f32x4*)(ex + (m * 9 + n) * 1024) = acc[m][n];
  } else {
#pragma unroll
    for (int m = 0; m < 2; ++m) {
#pragma unroll
      for (int n = 0; n < 4; ++n) *(f32x4*)(ex + (m * 9 + n) * 1024) = acc[m][n];
      *(f32x4*)(ex + (m * 9 + 8) * 1024) = acc[m][8];
    }
  }
  __syncthreads();
#pragma unroll
  for (int m = 0; m < 2; ++m) {
#pragma unroll
    for (int q = 0; q < 2; ++q) {
      f32x4 g0, g1;
      if (kh == 0) { g0 = acc[m][2 * q] + *(const f32x4*)(ex + (m * 9 + 2 * q) * 1024); g1 = acc[m][2 * q + 1] + *(const f32x4*)(ex + (m * 9 + 2 * q + 1) * 1024); }
      else { g0 = acc[m][4 + 2 * q] + *(const f32x4*)(ex + (m * 9 + 4 + 2 * q) * 1024); g1 = acc[m][5 + 2 * q] + *(const f32x4*)(ex + (m * 9 + 5 + 2 * q) * 1024); }
      u32x4 z = z1q[m][q], o;
      o[0] = pk_bf16(bf_lo(z[0]) * (g0[0] + bias[m]), bf_hi(z[0]) * (g0[1] + bias[m]));
      o[1] = pk_bf16(bf_lo(z[1]) * (g0[2] + bias[m]), bf_hi(z[1]) * (g0[3] + bias[m]));
      o[2] = pk_bf16(bf_lo(z[2]) * (g1[0] + bias[m]), bf_hi(z[2]) * (g1[1] + bias[m]));
      o[3] = pk_bf16(bf_lo(z[3]) * (g1[2] + bias[m]), bf_hi(z[3]) * (g1[3] + bias[m]));
      st16(hp + m * (16 * 4608) + (kh * 2 + q) * 1024 + lane * 16, o);
    }
    if (kh == 0) {
      f32x4 g0 = acc[m][8] + *(const f32x4*)(ex + (m * 9 + 8) * 1024);
      u32x2 z = z1s[m], o;
      o[0] = pk_bf16(bf_lo(z[0]) * (g0[0] + bias[m]), bf_hi(z[0]) * (g0[1] + bias[m]));
      o[1] = pk_bf16(bf_lo(z[1]) * (g0[2] + bias[m]), bf_hi(z[1]) * (g0[3] + bias[m]));
      *(u32x2*)(hp + m * (16 * 4608) + 4096 + lane * 8) = o;
    }
  }
}

constexpr int KC_PITCH = 336;
constexpr int KC_PART = 64 * KC_PITCH;
constexpr int KC_LDS = 5 * KC_PART;
template <bool ISO>
__device__ __forceinline__ void kc_main(f32x4 (&acc)[4][5], rsrc_t rA, rsrc_t rW, unsigned vA16, unsigned vA8, unsigned vW, unsigned sA, unsigned sW) {
  constexpr unsigned TS = ISO ? 18432u : 16u * 4608u;
  constexpr int PD = 3;
  bf16x8 A[PD + 1][4], W[PD + 1][5];
  auto load = [&](int u, int slot) {
#pragma unroll
    for (int m = 0; m < 4; ++m) {
      if (ISO || (u % 9) == 4) {
        unsigned o0, o1;
        if (ISO) { o0 = sA + m * TS + u * 1024; o1 = o0 + 512; }
        else { o0 = sA + m * TS + (u / 9) * 2 * 4608 + 4096; o1 = o0 + 4608; }
        u32x2 lo = __builtin_amdgcn_raw_buffer_load_b64(rA, vA8, o0, 0);
        u32x2 hi = __builtin_amdgcn_raw_buffer_load_b64(rA, vA8, o1, 0);
        u32x4 v = {lo[0], lo[1], hi[0], hi[1]};
        A[slot][m] = __builtin_bit_cast(bf16x8, v);
      } else {
        int i = u % 9;
        unsigned o = sA + m * TS + (u / 9) * 2 * 4608 + (i < 4 ? i * 1024 : 4608 + (i - 5) * 1024);
        A[slot][m] = __builtin_bit_cast(bf16x8, __builtin_amdgcn_raw_buffer_load_b128(rA, vA16, o, 0));
      }
    }
#pragma unroll
    for (int n = 0; n < 5; ++n) W[slot][n] = __builtin_bit_cast(bf16x8, __builtin_amdgcn_raw_buffer_load_b128(rW, vW, sW + (u * 5 + n) * 1024, 0));
  };
#pragma unroll
  for (int u = 0; u < PD; ++u) load(u, u);
  SCHED();
#pragma unroll
  for (int u = 0; u < 18; ++u) {
    if (u + PD < 18) load(u + PD, (u + PD) % (PD + 1));
    SCHED();
#pragma unroll
    for (int m = 0; m < 4; ++m)
#pragma unroll
      for (int n = 0; n < 5; ++n) acc[m][n] = MFMA(W[u % (PD + 1)][n], A[u % (PD + 1)][m], acc[m][n]);
    SCHED();
  }
}
__global__ __launch_bounds__(320, 2) void kC(const char* __restrict__ ws_c, const u16* __restrict__ x, u16* __restrict__ out) {
  extern __shared__ __attribute__((aligned(16))) char smem[];
  const int tid = threadIdx.x, lane = tid & 63;
  const int w = RFL(tid >> 6);
  const int b = blockIdx.x, xcd = b & 7, idx = b >> 3;
  const int rt = (xcd & 3) * 8 + (idx & 7), ct = (xcd >> 2) * 4 + (idx >> 3);
  f32x4 acc[4][5];
#pragma unroll
  for (int m = 0; m < 4; ++m)
#pragma unroll
    for (int n = 0; n < 5; ++n) acc[m][n] = (f32x4){0.f, 0.f, 0.f, 0.f};
  const rsrc_t rW = mk_rsrc(ws_c + OFF_WC, (int)SZ_WC);
  const unsigned sW = RFL((unsigned)((ct * 90 + w * 18) * 5 * 1024));
  const int dstart = ct < 7 ? ct * 72 : 496;
  if (w < 4) {
    const rsrc_t rA = mk_rsrc(ws_c + OFF_HT, (int)SZ_Z);
    kc_main<false>(acc, rA, rW, lane * 16, lane * 8, lane * 16, RFL((unsigned)((rt * 4 * 16 + w * 4) * 4608)), sW);
  } else {
    const rsrc_t rA = mk_rsrc(ws_c + OFF_OT, (int)SZ_OT);
    kc_main<true>(acc, rA, rW, lane * 16, lane * 8, lane * 16, RFL((unsigned)(rt * 4 * 18432)), sW);
  }
  {
    char* p = smem + w * KC_PART + (lane & 15) * KC_PITCH + (lane >> 4) * 16;
#pragma unroll
    for (int m = 0; m < 4; ++m)
#pragma unroll
      for (int n = 0; n < 5; ++n) *(f32x4*)(p + m * 16 * KC_PITCH + n * 64) = acc[m][n];
  }
  __syncthreads();
  const int own0 = ct * 72 - dstart;
  for (int c = tid; c < 576; c += 320) {
    int r = c / 9, ch = c - r * 9;
    const char* p = smem + r * KC_PITCH + (own0 + ch * 8) * 4;
    f32x4 s0 = *(const f32x4*)p, s1 = *(const f32x4*)(p + 16);
#pragma unroll
    for (int ww = 1; ww < 5; ++ww) { s0 += *(const f32x4*)(p + ww * KC_PART); s1 += *(const f32x4*)(p + ww * KC_PART + 16); }
    size_t go = (size_t)(rt * 64 + prow(r >> 4, r & 15)) * 576 + ct * 72 + ch * 8;
    u32x4 xv = *(const u32x4*)(x + go), o;
    o[0] = pk_bf16(s0[0] + bf_lo(xv[0]), s0[1] + bf_hi(xv[0]));
    o[1] = pk_bf16(s0[2] + bf_lo(xv[1]), s0[3] + bf_hi(xv[1]));
    o[2] = pk_bf16(s1[0] + bf_lo(xv[2]), s1[1] + bf_hi(xv[2]));
    o[3] = pk_bf16(s1[2] + bf_lo(xv[3]), s1[3] + bf_hi(xv[3]));
    st16(out + go, o);
  }
}

constexpr int K0_BLOCKS = 280;
__global__ __launch_bounds__(256) void k0(const u16* __restrict__ x, const u16* __restrict__ wqkv, const u16* __restrict__ win, char* __restrict__ ws) {
  __shared__ __attribute__((aligned(16))) char sm[32 * K0_PITCH];
  const int tid = threadIdx.x, lane = tid & 63, i = lane & 15, g = lane >> 4, w = tid >> 6, b = blockIdx.x;
  const unsigned smb = (unsigned)(size_t)(__attribute__((address_space(3))) char*)sm;
  if (b < 144) {
    const int ks = b >> 3, cb = b & 7;
    u32x4 v[9];
#pragma unroll
    for (int k = 0; k < 9; ++k) { int c = tid + k * 256, r = c / 72, ch = c - r * 72; v[k] = *(const u32x4*)(win + (size_t)(ks * 32 + r) * 4608 + cb * 576 + ch * 8); }
#pragma unroll
    for (int k = 0; k < 9; ++k) { int c = tid + k * 256, r = c / 72, ch = c - r * 72; *(u32x4*)(sm + r * K0_PITCH + ch * 16) = v[k]; }
    __syncthreads();
    const unsigned a0 = smb + (8 * g + (i >> 2)) * K0_PITCH + (i & 3) * 8;
#pragma unroll
    for (int e = 0; e < 9; ++e) {
      int nt = w * 9 + e;
      int gt = cb * 36 + nt;
      int z2 = gt >= 144, t = gt - z2 * 144, fb = t / 9, n = t - fb * 9;
      int fast = z2 ? n < 5 : n >= 4, nz = 4 + fast;
      int fr = fb * 468 + (fast ? 0 : 252) + 72 + (z2 ? ks * nz + n - (fast ? 0 : 5) : 18 * nz + ks * nz + n - (fast ? 4 : 0));
      u32x4 o = tr_frag(a0 + nt * 32, a0 + nt * 32 + 4 * K0_PITCH);
      st16(ws + OFF_W1 + (size_t)fr * 1024 + lane * 16, o);
    }
  } else if (b < 216) {
    const int u = b - 144, ks = u >> 2, hq = u & 3;
    u32x4 v[7];
#pragma unroll
    for (int k = 0; k < 7; ++k) { int c = tid + k * 256, r = c / 54, e = c - r * 54, part = e / 18, ch = e - part * 18;
      if (c < 32 * 54) v[k] = *(const u32x4*)(wqkv + (size_t)(ks * 32 + r) * 1728 + part * 576 + hq * 144 + ch * 8); }
#pragma unroll
    for (int k = 0; k < 7; ++k) { int c = tid + k * 256, r = c / 54, e = c - r * 54; if (c < 32 * 54) *(u32x4*)(sm + r * K0_PITCH + e * 16) = v[k]; }
    __syncthreads();
#pragma unroll
    for (int e = 0; e < 8; ++e) {
      int f = w * 8 + e, hl = f >> 2, n = f & 3, c = n * 16 + i, col; bool ok = true;
      if (c < 18) col = 144 + hl * 18 + c; else if (c >= 20 && c < 38) col = 288 + hl * 18 + c - 20; else if (c >= 40 && c < 58) col = hl * 18 + c - 40; else { col = 0; ok = false; }
      unsigned h[8];
#pragma unroll
      for (int j = 0; j < 8; ++j) h[j] = ok ? *(const u16*)(sm + (8 * g + j) * K0_PITCH + col * 2) : 0u;
      st16(ws + OFF_W1 + (size_t)(((hq * 8 + hl) >> 1) * 468 + (hl & 1) * 252 + ks * 4 + n) * 1024 + lane * 16, (u32x4){h[0] | (h[1] << 16), h[2] | (h[3] << 16), h[4] | (h[5] << 16), h[6] | (h[7] << 16)});
    }
  } else {
    if (b == 216) for (int c = tid; c < (2048 * 16 + 16 * 256) / 16; c += 256) *(u32x4*)(ws + OFF_RS + c * 16) = (u32x4){0, 0, 0, 0};
    const int u = (b - 216) * 4 + w, tt = u >> 1, slot = (u & 1) * 8 + (lane >> 3), c0 = lane & 7;
    const u16* xr = x + (size_t)prow(tt, slot) * 576 + c0 * 8;
    u32x4 v[9];
#pragma unroll
    for (int k = 0; k < 9; ++k) v[k] = *(const u32x4*)(xr + k * 64);
    float s = 0.f;
#pragma unroll
    for (int k = 0; k < 9; ++k)
#pragma unroll
      for (int j = 0; j < 4; ++j) s += bf_lo(v[k][j]) + bf_hi(v[k][j]);
    s += __shfl_xor(s, 1); s += __shfl_xor(s, 2); s += __shfl_xor(s, 4);
    const float mean = s * (1.f / 576.f);
    float q = 0.f;
#pragma unroll
    for (int k = 0; k < 9; ++k)
#pragma unroll
      for (int j = 0; j < 4; ++j) { float a = bf_lo(v[k][j]) - mean, c = bf_hi(v[k][j]) - mean; q += a * a + c * c; }
    q += __shfl_xor(q, 1); q += __shfl_xor(q, 2); q += __shfl_xor(q, 4);
    const float mb = rbf(mean), sd = rbf(sqrtf(rbf(rbf(q * (1.f / 576.f)) + 1e-5f))), rs = 1.f / sd;
    char* dst = ws + OFF_XT + (size_t)tt * 18432 + slot * 16;
#pragma unroll
    for (int k = 0; k < 9; ++k) {
      u32x4 o;
#pragma unroll
      for (int j = 0; j < 4; ++j) o[j] = pk_bf16(rbf(bf_lo(v[k][j]) - mb) * rs, rbf(bf_hi(v[k][j]) - mb) * rs);
      int c = c0 + 8 * k;
      st16(dst + (c >> 2) * 1024 + (c & 3) * 256, o);
    }
  }
}

extern "C" void kernel_launch(void* const* d_in, const int* in_sizes, int n_in, void* d_out, int out_size, void* d_ws, size_t ws_size, hipStream_t stream) {
  (void)in_sizes; (void)n_in; (void)out_size; (void)ws_size;
  const u16* x = (const u16*)d_in[0];
  char* ws = (char*)d_ws;
  (void)hipFuncSetAttribute((const void*)kA, hipFuncAttributeMaxDynamicSharedMemorySize, KA_LDS);
  (void)hipFuncSetAttribute((const void*)kB, hipFuncAttributeMaxDynamicSharedMemorySize, KB_LDS);
  (void)hipFuncSetAttribute((const void*)kC, hipFuncAttributeMaxDynamicSharedMemorySize, KC_LDS);
  k0<<<K0_BLOCKS, 256, 0, stream>>>(x, (const u16*)d_in[1], (const u16*)d_in[4], ws);
  kA<<<512, 256, KA_LDS, stream>>>(ws, ws, (const u16*)d_in[5], (const u16*)d_in[7], (const u16*)d_in[2]);
  kB<<<256, 512, KB_LDS, stream>>>(ws, ws, (const u16*)d_in[6]);
  kC<<<256, 320, KC_LDS, stream>>>(ws, x, (u16*)d_out);
}
```

```cpp
#include <hip/hip_runtime.h>
#include <hip/hip_bf16.h>
#include <stdint.h>

typedef unsigned short u16;
typedef __attribute__((ext_vector_type(8))) short bf16x8;
typedef __attribute__((ext_vector_type(4))) short bf16x4;
typedef __attribute__((ext_vector_type(4))) float f32x4;
typedef __attribute__((ext_vector_type(4))) unsigned u32x4;
typedef __attribute__((ext_vector_type(2))) unsigned u32x2;
typedef __amdgpu_buffer_rsrc_t rsrc_t;

constexpr int S_ = 2048, D_ = 576, F_ = 4608, FH_ = 2304;
constexpr size_t OFF_Z2T = 0;
constexpr size_t SZ_Z = (size_t)16 * 64 * 9 * 1024;
constexpr size_t OFF_Z1T = OFF_Z2T + SZ_Z;
constexpr size_t OFF_HT = OFF_Z1T + SZ_Z;
constexpr size_t OFF_WT = OFF_HT + SZ_Z;
constexpr size_t SZ_WT = (size_t)16 * 64 * 8 * 1024;
constexpr size_t OFF_OT = OFF_WT + SZ_WT;
constexpr size_t SZ_OT = (size_t)128 * 144 * 128;
constexpr size_t OFF_WC = OFF_OT + SZ_OT;
constexpr size_t SZ_WC = (size_t)8 * 90 * 5 * 1024;
constexpr size_t OFF_XT = OFF_WC + SZ_WC;
constexpr size_t SZ_XT = (size_t)128 * 18 * 1024;
constexpr size_t OFF_W1 = OFF_XT + SZ_XT;
constexpr size_t SZ_W1 = (size_t)32 * 234 * 1024;
constexpr size_t OFF_RS = OFF_W1 + SZ_W1;
constexpr size_t OFF_CNT = OFF_RS + 2048 * 16;
constexpr size_t OFF_ST = OFF_CNT + 16 * 256;
constexpr size_t WS_END = OFF_ST + (1 << 20);

#define RFL(x) __builtin_amdgcn_readfirstlane(x)
#define SCHED() __builtin_amdgcn_sched_barrier(0)
__device__ __forceinline__ rsrc_t mk_rsrc(const void* p, int bytes) { return __builtin_amdgcn_make_buffer_rsrc((void*)p, (short)0, bytes, 0x00020000); }
__device__ __forceinline__ float bf_lo(unsigned w) { return __uint_as_float(w << 16); }
__device__ __forceinline__ float bf_hi(unsigned w) { return __uint_as_float(w & 0xffff0000u); }
__device__ __forceinline__ unsigned pk_bf16(float lo, float hi) {
  typedef __attribute__((ext_vector_type(2))) float f32x2; typedef __attribute__((ext_vector_type(2))) __bf16 bf2;
  f32x2 v = {lo, hi}; bf2 r = __builtin_convertvector(v, bf2); return __builtin_bit_cast(unsigned, r);
}
__device__ __forceinline__ float rbf(float f) { return bf_hi(pk_bf16(0.f, f)); }
__host__ __device__ __forceinline__ int prow(int tt, int i) { return 32 * (tt >> 1) + 8 * (i >> 2) + 4 * (tt & 1) + (i & 3); }
__device__ __forceinline__ void st16(void* p, u32x4 v) { asm volatile("global_store_dwordx4 %0, %1, off sc1\n\ts_nop 1" :: "v"(p), "v"(v) : "memory"); }
#define MFMA(a, b, c) __builtin_amdgcn_mfma_f32_16x16x32_bf16(a, b, c, 0, 0, 0)

#define DMA16(lds, voff, srd, soff, imm) asm volatile("s_mov_b32 m0, %0\n\ts_nop 0\n\tbuffer_load_dwordx4 %1, %2, %3 offen offset:" #imm " lds" :: "s"(lds), "v"(voff), "s"(srd), "s"(soff) : "memory")
#define DMA16H(lds, voff, srd, soff) asm volatile("s_mov_b32 m0, %0\n\ts_lshr_b64 exec, -1, 32\n\tbuffer_load_dwordx4 %1, %2, %3 offen lds\n\ts_mov_b64 exec, -1" :: "s"(lds), "v"(voff), "s"(srd), "s"(soff) : "memory")
typedef __attribute__((address_space(3))) char lds_char;
#define LDS_RD128(addr) (*(const __attribute__((address_space(3))) bf16x8*)(addr))
#define AMFMA(acc, a, b) asm volatile("v_mfma_f32_16x16x32_bf16 %0, %1, %2, %0" : "+v"(acc) : "v"(a), "v"(b))
#define ADSRD(dst, addr, off) asm volatile("ds_read_b128 %0, %1 offset:%2" : "=v"(dst) : "v"(addr), "i"(off))
#define OPAQUE(x) asm volatile("" : "+v"(x))
#define BLOAD16(dst, voff, srd, soff, imm) asm volatile("buffer_load_dwordx4 %0, %1, %2, %3 offen offset:" #imm : "=v"(dst) : "v"(voff), "s"(srd), "s"(soff))

constexpr int K0_PITCH = 1168;
__device__ __forceinline__ int wc_krow(int ks, int g, int j) {
  int r = j & 3, hi = j >> 2;
  if (ks >= 72) { int u = ks - 72; return 2304 + 4 * (8 * u + 4 * hi + g) + r; }
  int w = ks / 18, pi = (ks % 18) / 9, i = ks % 9, fb0 = 4 * w + 2 * pi;
  if (i == 4) return 144 * (fb0 + hi) + 128 + 4 * g + r;
  if (i < 4) return 144 * fb0 + 32 * i + 16 * hi + 4 * g + r;
  return 144 * (fb0 + 1) + 32 * (i - 5) + 16 * hi + 4 * g + r;
}
__device__ __forceinline__ u32x4 tr_frag(unsigned a0, unsigned a1) {
  u32x2 lo, hi;
  asm volatile("ds_read_b64_tr_b16 %0, %2\n\tds_read_b64_tr_b16 %1, %3\n\ts_waitcnt lgkmcnt(0)" : "=&v"(lo), "=&v"(hi) : "v"(a0), "v"(a1) : "memory");
  return (u32x4){lo[0], lo[1], hi[0], hi[1]};
}
#define KA_SLEEP 32
constexpr int KA_SLOT = 10240, KA_ATT = 6 * KA_SLOT, KA_PITCH = 144, KA_LDS = KA_ATT + 142 * KA_PITCH;
template <int FAST> struct KaCfg {
  static constexpr int NZ2 = 4 + FAST, NZ1 = 4 + FAST;
  static constexpr int nt(int s) { return s < 9 ? 4 : 4 + FAST; }
  static constexpr int goff(int s) { return s < 9 ? s * 8192 : 73728 + (s - 9) * (4 + FAST) * 2048; }
  static constexpr int ops(int s) { return s > 26 ? 0 : 2 + (nt(s) == 5 ? 1 : 0) + (s < 9 ? 4 + ((s & 1) ? 0 : 1) : 0); }
  static constexpr int wait(int s) { return s == 20 ? ops(23) + ops(24) : ops(s + 2) + ops(s + 3) + ops(s + 4) + ((s >= 9 && s <= 12) ? 6 : 0); }
};
template <int V> struct IC { static constexpr int v = V; };
#define BLD(dst, voff, srd, soff, IMM) asm volatile("buffer_load_dwordx4 %0, %1, %2, %3 offen offset:%4" : "=v"(dst) : "v"(voff), "s"(srd), "s"(soff), "i"(IMM))
#define AMFMA0(acc, a, b) asm volatile("v_mfma_f32_16x16x32_bf16 %0, %1, %2, 0" : "=&v"(acc) : "v"(a), "v"(b))
typedef __attribute__((ext_vector_type(2))) float f32x2;
typedef __attribute__((ext_vector_type(2))) __bf16 bf2_t;
__device__ __forceinline__ float dot2(unsigned a, unsigned b, float c) { return __builtin_amdgcn_fdot2_f32_bf16(__builtin_bit_cast(bf2_t, a), __builtin_bit_cast(bf2_t, b), c, false); }
__device__ __forceinline__ f32x2 gelu2(f32x2 x) {
  f32x2 t = x * x, p = t * (-0.10294324f) + (-2.3022082f), e = x * p;
  f32x2 d = {__builtin_amdgcn_exp2f(e[0]) + 1.f, __builtin_amdgcn_exp2f(e[1]) + 1.f};
  f32x2 r = {__builtin_amdgcn_rcpf(d[0]), __builtin_amdgcn_rcpf(d[1])};
  return x * r;
}
template <int CTRL> __device__ __forceinline__ float dpp_add(float v) {
  return v + __builtin_bit_cast(float, __builtin_amdgcn_update_dpp(0, __builtin_bit_cast(int, v), CTRL, 0xf, 0xf, true));
}

__device__ __forceinline__ float xmax16(float v) { auto r = __builtin_amdgcn_permlane16_swap(__float_as_uint(v), __float_as_uint(v), false, false); return fmaxf(__uint_as_float(r[0]), __uint_as_float(r[1])); }
__device__ __forceinline__ float xmax32(float v) { auto r = __builtin_amdgcn_permlane32_swap(__float_as_uint(v), __float_as_uint(v), false, false); return fmaxf(__uint_as_float(r[0]), __uint_as_float(r[1])); }
__device__ __forceinline__ float xadd16(float v) { auto r = __builtin_amdgcn_permlane16_swap(__float_as_uint(v), __float_as_uint(v), false, false); return __uint_as_float(r[0]) + __uint_as_float(r[1]); }
__device__ __forceinline__ float xadd32(float v) { auto r = __builtin_amdgcn_permlane32_swap(__float_as_uint(v), __float_as_uint(v), false, false); return __uint_as_float(r[0]) + __uint_as_float(r[1]); }
#define PRIO_HI()
#define PRIO_LO()
#define LBAR() asm volatile("s_waitcnt lgkmcnt(0)\n\ts_barrier" ::: "memory")
__device__ __forceinline__ float swap1(float v) { return __builtin_bit_cast(float, __builtin_amdgcn_update_dpp(0, __builtin_bit_cast(int, v), 0xB1, 0xf, 0xf, true)); }
template <int FAST>
__device__ __forceinline__ void ka_body(char* smem, const char* __restrict__ ws_c, char* __restrict__ ws, const int p, const int cg, const int sj, const u16* __restrict__ wsp, const u16* __restrict__ wout, const u16* __restrict__ wo) {
  typedef KaCfg<FAST> C;
  const int tid = threadIdx.x, lane = tid & 63, li = lane & 15, lg = lane >> 4;
  const int w = RFL(tid >> 6), fb = cg >> 1;
  const rsrc_t rW = mk_rsrc(ws_c + OFF_W1 + (size_t)fb * (468 * 1024) + (FAST ? 0 : 252 * 1024), (FAST ? 252 : 216) * 1024);
  const rsrc_t rX = mk_rsrc(ws_c + OFF_XT, (int)SZ_XT);
  const unsigned ldsb = (unsigned)(size_t)(__attribute__((address_space(3))) char*)smem;
  const unsigned vl16 = lane * 16;
  const unsigned vX = vl16 + (unsigned)(p * 8 + w * 2) * 18432u;
  const unsigned vH = p == 0 ? 0x80000000u : (unsigned)(p * 8 - 2 + (li >> 3)) * 18432u + (unsigned)(lg * 16 + (li | 8)) * 16u + (unsigned)w * 1024u;
  const unsigned sF = RFL((unsigned)w * 2048u), sH = RFL(8192u + (unsigned)w * 512u);
  const unsigned rd = ldsb + vl16;
  constexpr int KA_WARM = 0;
  bf16x8 X[2][18], HX[5], F[5];
  f32x4 acc[2][5], hacc[3];
#pragma unroll
  for (int n = 0; n < 3; ++n) hacc[n] = (f32x4){0.f, 0.f, 0.f, 0.f};

  bf16x8 (&X_)[2][18] = X; bf16x8 (&HX_)[5] = HX; bf16x8 (&F_)[5] = F; f32x4 (&acc_)[2][5] = acc; f32x4 (&hacc_)[3] = hacc;
  const rsrc_t rW_ = rW, rX_ = rX; const unsigned vl16_ = vl16, vX_ = vX, vH_ = vH, rd_ = rd;
#define KA_CAP bf16x8 (&X)[2][18] = X_; bf16x8 (&HX)[5] = HX_; bf16x8 (&F)[5] = F_; f32x4 (&acc)[2][5] = acc_; f32x4 (&hacc)[3] = hacc_; \
    const rsrc_t rW = rW_, rX = rX_; const unsigned vl16 = vl16_, vX = vX_, vH = vH_, rd = rd_; (void)X; (void)HX; (void)F; (void)acc; (void)hacc; (void)rW; (void)rX; (void)vl16; (void)vX; (void)vH; (void)rd;
  auto vop = [&](auto S_, auto K_) {
    KA_CAP
    constexpr int s = decltype(S_)::v, k = decltype(K_)::v;
    if constexpr (s <= 26) {
      constexpr int slot = s % 6, nd = 2 + (C::nt(s) == 5 ? 1 : 0);
      if constexpr (k == 0) { unsigned lo = ldsb + slot * KA_SLOT + sF, so = C::goff(s) + sF; DMA16(lo, vl16, rW, so, 0); }
      else if constexpr (k == 1) { unsigned lo = ldsb + slot * KA_SLOT + sF, so = C::goff(s) + sF; DMA16(lo, vl16, rW, so, 1024); }
      else if constexpr (k == 2 && nd == 3) { unsigned lo = ldsb + slot * KA_SLOT + sH, so = C::goff(s) + sH; DMA16H(lo, vl16, rW, so); }
      else if constexpr (s < 9 && k >= 2 && k < 6) {
        constexpr int m = (k - 2) & 1, ks = 2 * s + ((k - 2) >> 1);
        BLD(X[m][ks], vX, rX, m * 18432 + (ks & ~3) * 1024, (ks & 3) * 1024);
      } else if constexpr (s < 9 && k == 6 && !(s & 1)) { BLD(HX[s >> 1], vH, rX, (s >> 1) * 4096, 0); }
    }
  };
  auto group = [&](auto S_) { vop(S_, IC<0>{}); vop(S_, IC<1>{}); vop(S_, IC<2>{}); vop(S_, IC<3>{}); vop(S_, IC<4>{}); vop(S_, IC<5>{}); vop(S_, IC<6>{}); };
  auto sync = [&](auto S_) {
    KA_CAP
    constexpr int s = decltype(S_)::v;
    if constexpr (s < 26) {
      if constexpr (s + 1 < 9) {
        constexpr int k0 = 2 * (s + 1);
        asm volatile("s_waitcnt vmcnt(%5)" : "+v"(X[0][k0]), "+v"(X[1][k0]), "+v"(X[0][k0 + 1]), "+v"(X[1][k0 + 1]), "+v"(HX[(s + 1) >> 1]) : "i"(C::wait(s)) : "memory");
      } else asm volatile("s_waitcnt vmcnt(%0)" :: "i"(C::wait(s)) : "memory");
      __builtin_amdgcn_s_barrier();
    }
  };
  auto step = [&](auto S_, auto H_) {
    KA_CAP
    constexpr int s = decltype(S_)::v, h = decltype(H_)::v;
    constexpr int NT = C::nt(s), job = s / 9, ks = 2 * (s % 9) + h;
    constexpr int s2 = h ? s + 1 : s, h2 = h ^ 1, NT2 = s2 <= 26 ? C::nt(s2) : 0;
    constexpr int roff = (s2 % 6) * KA_SLOT + h2 * NT2 * 1024;
    if constexpr (ks == 0) asm volatile("s_waitcnt lgkmcnt(0)" ::: "memory");
    if constexpr (job == 0) {
      if (w == (ks & 3)) {
        asm volatile("s_waitcnt lgkmcnt(1)" ::: "memory");
#pragma unroll
        for (int n = 0; n < 3; ++n) AMFMA(hacc[n], F[n], HX[ks >> 2]);
      }
    }
#pragma unroll
    for (int n = 0; n < NT; ++n) {
      if constexpr (ks != 0) asm volatile("s_waitcnt lgkmcnt(%0)" :: "i"(NT - 1) : "memory");
      if constexpr (job == 1) {
        if constexpr (ks == 0) { AMFMA0(acc[0][n], X[0][ks], F[n]); AMFMA0(acc[1][n], X[1][ks], F[n]); }
        else { AMFMA(acc[0][n], X[0][ks], F[n]); AMFMA(acc[1][n], X[1][ks], F[n]); }
      } else {
        if constexpr (ks == 0) { AMFMA0(acc[0][n], F[n], X[0][ks]); AMFMA0(acc[1][n], F[n], X[1][ks]); }
        else { AMFMA(acc[0][n], F[n], X[0][ks]); AMFMA(acc[1][n], F[n], X[1][ks]); }
      }
      if (n < NT2) ADSRD(F[n], rd, roff + n * 1024);
      if constexpr (h == 1) {
        if (n == 0) { vop(IC<s + 5>{}, IC<0>{}); vop(IC<s + 5>{}, IC<2>{}); }
        if (n == 1) { vop(IC<s + 5>{}, IC<1>{}); vop(IC<s + 5>{}, IC<3>{}); }
        if (n == 2) { vop(IC<s + 5>{}, IC<4>{}); vop(IC<s + 5>{}, IC<5>{}); }
        if (n == 3) { vop(IC<s + 5>{}, IC<6>{}); }
      }
    }
    if constexpr (NT2 > NT) ADSRD(F[4], rd, roff + 4 * 1024);
  };
#define KA_ACC_DONE() do { asm volatile("s_nop 15" : "+v"(acc[0][0]), "+v"(acc[0][1]), "+v"(acc[0][2]), "+v"(acc[0][3]), "+v"(acc[1][0]), "+v"(acc[1][1]), "+v"(acc[1][2]), "+v"(acc[1][3]) :: "memory"); \
    if constexpr (FAST) asm volatile("" : "+v"(acc[0][4]), "+v"(acc[1][4]) :: "memory"); } while (0)
  auto iter = [&](auto S_) { step(S_, IC<0>{}); sync(S_); step(S_, IC<1>{}); };

  group(IC<0>{}); group(IC<1>{});
  asm volatile("s_waitcnt vmcnt(%5)" : "+v"(X[0][0]), "+v"(X[1][0]), "+v"(X[0][1]), "+v"(X[1][1]), "+v"(HX[0]) : "i"(C::ops(1) + KA_WARM) : "memory");
  __builtin_amdgcn_s_barrier();
#pragma unroll
  for (int n = 0; n < 4; ++n) ADSRD(F[n], rd, n * 1024);

  step(IC<0>{}, IC<0>{}); group(IC<2>{}); group(IC<3>{}); group(IC<4>{}); sync(IC<0>{}); step(IC<0>{}, IC<1>{});
  iter(IC<1>{}); iter(IC<2>{}); iter(IC<3>{}); iter(IC<4>{}); iter(IC<5>{}); iter(IC<6>{}); iter(IC<7>{}); iter(IC<8>{});
  asm volatile("s_nop 15" : "+v"(acc[0][0]), "+v"(acc[0][1]), "+v"(acc[0][2]), "+v"(acc[0][3]), "+v"(acc[1][0]), "+v"(acc[1][1]), "+v"(acc[1][2]), "+v"(acc[1][3]), "+v"(hacc[0]), "+v"(hacc[1]), "+v"(hacc[2]) :: "memory");

  SCHED();
  {
    char* att = smem + KA_ATT;
    char* hp = smem + 2 * KA_SLOT;
#pragma unroll
    for (int n = 0; n < 3; ++n) if (n != w) *(f32x4*)(hp + (n * 3 + (w > n ? w - 1 : w)) * 1024 + vl16) = hacc[n];
#pragma unroll
    for (int m = 0; m < 2; ++m) {
      char* rp = att + (14 + w * 32 + 8 * (li >> 2) + 4 * m + (li & 3)) * KA_PITCH + lg * 8;
#pragma unroll
      for (int n = 0; n < 4; ++n) *(u32x2*)(rp + n * 32) = (u32x2){pk_bf16(acc[m][n][0], acc[m][n][1]), pk_bf16(acc[m][n][2], acc[m][n][3])};
    }
    LBAR();
    if (w < 3) {
      f32x4 a = w == 0 ? hacc[0] : (w == 1 ? hacc[1] : hacc[2]);
#pragma unroll
      for (int q = 0; q < 3; ++q) a += *(const f32x4*)(hp + (w * 3 + q) * 1024 + vl16);
      int ho = (li < 8) ? 8 * (li >> 2) + (li & 3) : 8 * ((li - 8) >> 2) + 4 + (li & 3);
      if (ho >= 2) *(u32x2*)(att + (ho - 2) * KA_PITCH + w * 32 + lg * 8) = (u32x2){pk_bf16(a[0], a[1]), pk_bf16(a[2], a[3])};
    }
    LBAR();
    const unsigned attb = ldsb + KA_ATT;
    float mbias[8];
#pragma unroll
    for (int e = 0; e < 8; ++e) { int d = (e >> 2) * 16 + lg * 4 + (e & 3) - li; mbias[e] = (!(d & 1) && d >= 2 && d <= 16) ? 0.f : -__builtin_inff(); }
    bf16x8 Q[2], K[2][2]; u32x2 V[2][2][2];
#pragma unroll
    for (int jj = 0; jj < 2; ++jj) {
      const int j = w * 2 + jj;
      Q[jj] = *(const bf16x8*)(att + (14 + 16 * j + li) * KA_PITCH + 80 + (lg < 3 ? lg : 2) * 16);
#pragma unroll
      for (int kt = 0; kt < 2; ++kt) {
        int R = 16 * j - 2 + 16 * kt + li; R = R < 0 ? 0 : R;
        K[jj][kt] = *(const bf16x8*)(att + R * KA_PITCH + lg * 16);
        int Rv = 16 * j - 2 + 16 * kt + 4 * lg + (li >> 2); Rv = Rv < 0 ? 0 : Rv;
        const unsigned va = attb + Rv * KA_PITCH + 40 + (li & 3) * 8;
        asm volatile("ds_read_b64_tr_b16 %0, %2\n\tds_read_b64_tr_b16 %1, %2 offset:32" : "=&v"(V[jj][0][kt]), "=&v"(V[jj][1][kt]) : "v"(va) : "memory");
      }
    }
    f32x4 S[2][2];
#pragma unroll
    for (int jj = 0; jj < 2; ++jj) {
      if (lg == 3) Q[jj] = (bf16x8){0, 0, 0, 0, 0, 0, 0, 0};
#pragma unroll
      for (int kt = 0; kt < 2; ++kt) S[jj][kt] = MFMA(K[jj][kt], Q[jj], ((f32x4){0.f, 0.f, 0.f, 0.f}));
    }
    bf16x8 P[2];
#pragma unroll
    for (int jj = 0; jj < 2; ++jj) {
      float l[8];
#pragma unroll
      for (int e = 0; e < 8; ++e) l[e] = S[jj][e >> 2][e & 3] + mbias[e];
      if (p == 0 && w == 0 && jj == 0) {
#pragma unroll
        for (int e = 0; e < 4; ++e) l[e] = -__builtin_inff();
      }
      float mx = fmaxf(fmaxf(fmaxf(l[0], l[1]), fmaxf(l[2], l[3])), fmaxf(fmaxf(l[4], l[5]), fmaxf(l[6], l[7])));
      mx = xmax16(mx); mx = xmax32(mx);
      constexpr float CE = 0.23570226f * 1.44269504f;
      const float nm = -mx * CE;
      float sum = 0.f;
#pragma unroll
      for (int e = 0; e < 8; ++e) { l[e] = __builtin_amdgcn_exp2f(l[e] * CE + nm); sum += l[e]; }
      sum = xadd16(sum); sum = xadd32(sum);
      const float inv = __builtin_amdgcn_rcpf(sum);
      u32x4 pk = {pk_bf16(l[0] * inv, l[1] * inv), pk_bf16(l[2] * inv, l[3] * inv), pk_bf16(l[4] * inv, l[5] * inv), pk_bf16(l[6] * inv, l[7] * inv)};
      P[jj] = __builtin_bit_cast(bf16x8, pk);
    }
    asm volatile("s_waitcnt lgkmcnt(0)" : "+v"(V[0][0][0]), "+v"(V[0][0][1]), "+v"(V[0][1][0]), "+v"(V[0][1][1]), "+v"(V[1][0][0]), "+v"(V[1][0][1]), "+v"(V[1][1][0]), "+v"(V[1][1][1]) :: "memory");
#pragma unroll
    for (int jj = 0; jj < 2; ++jj) {
      f32x4 O[2];
#pragma unroll
      for (int dt = 0; dt < 2; ++dt) {
        u32x4 vv = {V[jj][dt][0][0], V[jj][dt][0][1], V[jj][dt][1][0], V[jj][dt][1][1]};
        O[dt] = MFMA(__builtin_bit_cast(bf16x8, vv), P[jj], ((f32x4){0.f, 0.f, 0.f, 0.f}));
      }
      const int tl = jj * 16 + li, tt = p * 8 + w * 2 + ((tl >> 2) & 1), slot = (tl >> 3) * 4 + (tl & 3);
      char* ob = ws + OFF_OT + (size_t)tt * 18432 + slot * 8;
      const int d0 = cg * 18 + lg * 4, d1 = d0 + 2, d2 = cg * 18 + 16;
      *(unsigned*)(ob + (d0 >> 2) * 128 + (d0 & 3) * 2) = pk_bf16(O[0][0], O[0][1]);
      *(unsigned*)(ob + (d1 >> 2) * 128 + (d1 & 3) * 2) = pk_bf16(O[0][2], O[0][3]);
      if (lg == 0) *(unsigned*)(ob + (d2 >> 2) * 128 + (d2 & 3) * 2) = pk_bf16(O[1][0], O[1][1]);
    }
  }
  SCHED();
  PRIO_LO();

  iter(IC<9>{}); iter(IC<10>{}); iter(IC<11>{}); iter(IC<12>{}); iter(IC<13>{}); iter(IC<14>{}); iter(IC<15>{}); iter(IC<16>{}); iter(IC<17>{});
  KA_ACC_DONE();

  SCHED();
  PRIO_HI();
  unsigned zs[C::NZ2][4];
  {
#pragma unroll
    for (int n = 0; n < C::NZ2; ++n) {
#pragma unroll
      for (int m = 0; m < 2; ++m) {
        f32x2 a = gelu2((f32x2){acc[m][n][0], acc[m][n][1]}), b = gelu2((f32x2){acc[m][n][2], acc[m][n][3]});
        zs[n][2 * m] = pk_bf16(a[0], a[1]); zs[n][2 * m + 1] = pk_bf16(b[0], b[1]);
      }
    }
    f32x2 s1[4], s2[4];
#pragma unroll
    for (int j = 0; j < 4; ++j) {
      s1[j] = (f32x2){0.f, 0.f}; s2[j] = s1[j];
#pragma unroll
      for (int n = 0; n < C::NZ2; ++n) { f32x2 v = {bf_lo(zs[n][j]), bf_hi(zs[n][j])}; s1[j] += v; s2[j] += v * v; }
    }
    float r[16];
#pragma unroll
    for (int j = 0; j < 4; ++j) { r[2 * j] = s1[j][0]; r[2 * j + 1] = s1[j][1]; r[8 + 2 * j] = s2[j][0]; r[9 + 2 * j] = s2[j][1]; }
#pragma unroll
    for (int j = 0; j < 16; ++j) { float v = r[j]; v = dpp_add<0x128>(v); v = dpp_add<0x124>(v); v = dpp_add<0x122>(v); v = dpp_add<0x121>(v); r[j] = v; }
    float sel = r[0];
#pragma unroll
    for (int j = 1; j < 16; ++j) sel = (li == j) ? r[j] : sel;
    double* dst = (double*)(ws + OFF_RS) + (size_t)(p * 128 + w * 32 + lg * 8 + (li & 7)) * 2 + (li >> 3);
    double dv = (double)sel;
    asm volatile("global_atomic_add_f64 %0, %1, off sc1" :: "v"(dst), "v"(dv) : "memory");
  }
  SCHED();

  PRIO_LO();

  unsigned* cnt = (unsigned*)(ws + OFF_CNT + p * 256);
  iter(IC<18>{}); iter(IC<19>{});
  step(IC<20>{}, IC<0>{}); sync(IC<20>{});
  if (tid == 0) { unsigned one = 1; asm volatile("global_atomic_add %0, %1, off sc1" :: "v"(cnt), "v"(one) : "memory"); }
  step(IC<20>{}, IC<1>{});
  iter(IC<21>{}); iter(IC<22>{}); iter(IC<23>{}); iter(IC<24>{}); iter(IC<25>{});
  unsigned seen;
  asm volatile("global_load_dword %0, %1, off sc1" : "=v"(seen) : "v"(cnt) : "memory");
  iter(IC<26>{});
  KA_ACC_DONE();
  asm volatile("s_waitcnt vmcnt(0)" : "+v"(seen) :: "memory");
  PRIO_HI();
  const unsigned seen_s = RFL(seen);
  const bool early = seen_s >= 32u;
  const char* sp = ws + OFF_RS + (size_t)(p * 128 + w * 32 + (lane & 31)) * 16;
  u32x4 st = {0, 0, 0, 0};
  asm volatile("s_cmp_lt_u32 %2, 32\n\ts_cbranch_scc1 1f\n\tglobal_load_dwordx4 %0, %1, off sc1\n1:" : "+v"(st) : "v"(sp), "s"(seen_s) : "memory", "scc");
  SCHED();
  u32x4 wt[8];
  if constexpr (FAST) {
    const int f0 = (sj * 4 + w) * 8, ks = (f0 >> 3) & 63, tp = f0 >> 9;
#pragma unroll
    for (int m = 0; m < 8; ++m) wt[m] = *(const u32x4*)(wsp + (size_t)prow(tp * 8 + m, li) * 2048 + ks * 32 + lg * 8);
  }
#pragma unroll
  for (int m = 0; m < 2; ++m) {
    char* zp = ws + OFF_Z1T + ((size_t)(p * 8 + w * 2 + m) * 16 + fb) * 4608;
    unsigned pk[C::NZ1][2];
#pragma unroll
    for (int n = 0; n < C::NZ1; ++n) {
      f32x2 a = gelu2((f32x2){acc[m][n][0], acc[m][n][1]}), b = gelu2((f32x2){acc[m][n][2], acc[m][n][3]});
      pk[n][0] = pk_bf16(a[0], a[1]); pk[n][1] = pk_bf16(b[0], b[1]);
    }
#pragma unroll
    for (int q = 0; q < 2; ++q) st16(zp + (FAST * 2 + q) * 1024 + vl16, (u32x4){pk[2 * q][0], pk[2 * q][1], pk[2 * q + 1][0], pk[2 * q + 1][1]});
    if constexpr (FAST) *(u32x2*)(zp + 4096 + lane * 8) = (u32x2){pk[4][0], pk[4][1]};
  }
  SCHED();
  if constexpr (FAST) {
    const int f0 = (sj * 4 + w) * 8;
#pragma unroll
    for (int m = 0; m < 8; ++m) st16(ws + OFF_WT + (size_t)(f0 + m) * 1024 + vl16, wt[m]);
    if (sj < 90) {
      const int ks = sj, base0 = wc_krow(ks, 0, 0), base1 = wc_krow(ks, 0, 4);
      u32x4 v[9];
#pragma unroll
      for (int k = 0; k < 9; ++k) {
        int c = tid + k * 256, r = c / 72, ch = c - r * 72, row = (r < 16 ? base0 : base1 - 16) + r;
        const u16* src = row < 2304 ? wout + (size_t)row * 576 : wo + (size_t)(row - 2304) * 576;
        v[k] = *(const u32x4*)(src + ch * 8);
      }
      LBAR();
#pragma unroll
      for (int k = 0; k < 9; ++k) { int c = tid + k * 256, r = c / 72, ch = c - r * 72; *(u32x4*)(smem + r * K0_PITCH + ch * 16) = v[k]; }
      LBAR();
      const unsigned a0 = ldsb + (4 * lg + (li >> 2)) * K0_PITCH + (li & 3) * 8;
#pragma unroll
      for (int e = 0; e < 10; ++e) {
        int f = w * 10 + e, ct = f / 5, n = f - ct * 5, d0 = (ct < 7 ? ct * 72 : 496) + n * 16;
        u32x4 o = tr_frag(a0 + d0 * 2, a0 + d0 * 2 + 16 * K0_PITCH);
        st16(ws + OFF_WC + (size_t)((ct * 90 + ks) * 5 + n) * 1024 + vl16, o);
      }
    }
  }
  if (!early) {
    while (RFL(seen) < 32u) {
      __builtin_amdgcn_s_sleep(KA_SLEEP);
      asm volatile("global_load_dword %0, %1, off sc1\n\ts_waitcnt vmcnt(0)" : "=v"(seen) : "v"(cnt) : "memory");
    }
  }
  asm volatile("s_cmp_ge_u32 %2, 32\n\ts_cbranch_scc1 1f\n\tglobal_load_dwordx4 %0, %1, off sc1\n1:\n\ts_waitcnt vmcnt(0)" : "+v"(st) : "v"(sp), "s"(seen_s) : "memory", "scc");
  {
    const double sm = __builtin_bit_cast(double, (u32x2){st[0], st[1]}), sq = __builtin_bit_cast(double, (u32x2){st[2], st[3]});
    const double mean = sm * (1.0 / 2304.0);
    const float var = fmaxf((float)(sq * (1.0 / 2304.0) - mean * mean), 0.f);
    const float mb = rbf((float)mean), rs = 1.f / rbf(sqrtf(rbf(rbf(var) + 1e-5f)));
    f32x2* sl = (f32x2*)(smem + KA_ATT + w * 256);
    if (lane < 32) sl[lane] = (f32x2){mb, rs};
    __builtin_amdgcn_s_waitcnt(0xC07F);
    __builtin_amdgcn_wave_barrier();
    f32x4 ms[4];
#pragma unroll
    for (int j = 0; j < 4; ++j) ms[j] = *(const f32x4*)((const char*)sl + lg * 64 + j * 16);
    char* zp = ws + OFF_Z2T + ((size_t)(fb * 64 + p * 4 + w) * 9 + (FAST ? 0 : 5)) * 1024 + vl16;
#pragma unroll
    for (int n = 0; n < C::NZ2; ++n) {
      u32x4 o;
#pragma unroll
      for (int j = 0; j < 4; ++j) o[j] = pk_bf16((bf_lo(zs[n][j]) - ms[j][0]) * ms[j][1], (bf_hi(zs[n][j]) - ms[j][2]) * ms[j][3]);
      st16(zp + n * 1024, o);
    }
  }
}
__global__ __launch_bounds__(256, 2) void kA(const char* __restrict__ ws_c, char* __restrict__ ws, const u16* __restrict__ wsp, const u16* __restrict__ wout, const u16* __restrict__ wo) {
  extern __shared__ __attribute__((aligned(16))) char smem[];
  const int b = blockIdx.x, xcd = b & 7, idx = b >> 3;
  const int p = (xcd & 1) * 8 + (idx & 7), q = idx >> 3, cg = (xcd >> 1) * 8 + (q & 3) * 2 + (q >> 2);
  if (idx < 32) ka_body<1>(smem, ws_c, ws, p, cg, b, wsp, wout, wo); else ka_body<0>(smem, ws_c, ws, p, cg, b, wsp, wout, wo);
}

constexpr int KB_STAGE = 36864;
constexpr int KB_LDS = 4 * KB_STAGE;
__global__ __launch_bounds__(512, 2) void kB(const char* __restrict__ ws_c, char* __restrict__ ws, const u16* __restrict__ bsp) {
  extern __shared__ __attribute__((aligned(16))) char smem[];
  const int tid = threadIdx.x, lane = tid & 63;
  const int w = RFL(tid >> 6), wm = w & 3, kh = w >> 2;
  const int b = blockIdx.x, xcd = b & 7, idx = b >> 3;
  const int tp = (xcd & 1) * 8 + (idx & 7), fb = (xcd >> 1) * 4 + (idx >> 3);
  const rsrc_t rZ = mk_rsrc(ws_c + OFF_Z2T + (size_t)fb * (64 * 9216), 64 * 9216);
  const rsrc_t rW = mk_rsrc(ws_c + OFF_WT + (size_t)tp * (64 * 8192), 64 * 8192);
  const unsigned ldsb = (unsigned)(size_t)(__attribute__((address_space(3))) char*)smem;
  const unsigned vl16 = lane * 16;
  const unsigned vW = vl16 + wm * 2048 + kh * 16384;
  const unsigned dmaF = w * 4096, dmaH = 32768 + w * 512;
  unsigned rd01 = ldsb + kh * 18432 + lane * 16;
  unsigned rd23 = rd01 + 2 * KB_STAGE;

  f32x4 acc[2][9];
#pragma unroll
  for (int m = 0; m < 2; ++m)
#pragma unroll
    for (int n = 0; n < 9; ++n) acc[m][n] = (f32x4){0.f, 0.f, 0.f, 0.f};
  bf16x8 Wr[4][4];
  bf16x8 ZA[9], ZB[9];

#define KB_DMA1(stage, slot, imm) do { \
    unsigned so_ = RFL((unsigned)(stage) * KB_STAGE + dmaF), lo_ = RFL(ldsb + (slot) * KB_STAGE + dmaF); DMA16(lo_, vl16, rZ, so_, imm); } while (0)
#define KB_DMAH(stage, slot) do { \
    unsigned so2_ = RFL((unsigned)(stage) * KB_STAGE + dmaH), lo2_ = RFL(ldsb + (slot) * KB_STAGE + dmaH); DMA16H(lo2_, vl16, rZ, so2_); } while (0)
#define KB_W1(stage, slot, e) do { unsigned sw_ = RFL((unsigned)(stage) * 32768u + ((e) >> 1) * 8192u); \
    if ((e) & 1) BLOAD16(Wr[slot][e], vW, rW, sw_, 1024); else BLOAD16(Wr[slot][e], vW, rW, sw_, 0); } while (0)
#define KB_ALL(stage, slot) do { KB_DMA1(stage, slot, 0); KB_DMA1(stage, slot, 1024); KB_DMA1(stage, slot, 2048); KB_DMA1(stage, slot, 3072); KB_DMAH(stage, slot); \
    KB_W1(stage, slot, 0); KB_W1(stage, slot, 1); KB_W1(stage, slot, 2); KB_W1(stage, slot, 3); } while (0)
#define KB_WAIT(slot, N) asm volatile("s_waitcnt vmcnt(" #N ") lgkmcnt(0)" : "+v"(Wr[slot][0]), "+v"(Wr[slot][1]), "+v"(Wr[slot][2]), "+v"(Wr[slot][3]) :: "memory")

  KB_ALL(0, 0);
  __builtin_amdgcn_s_barrier();
  KB_ALL(1, 1);
  KB_WAIT(0, 9);
  __builtin_amdgcn_s_barrier();

  const rsrc_t rZ1 = mk_rsrc(ws_c + OFF_Z1T + ((size_t)(tp * 8 + wm * 2) * 16 + fb) * 4608, 17 * 4608);
  const rsrc_t rB = mk_rsrc(bsp, 4096);
  u32x4 z1q[2][2]; u32x2 z1s[2]; unsigned biasu[2];
  const unsigned vz1 = lane * 16 + kh * 2048, vz8 = lane * 8 + 4096;
  const unsigned vb = (tp * 128 + wm * 32 + ((lane & 15) >> 2) * 8 + (lane & 3)) * 2;
#define KB_EPI_LD(e) do { \
    if ((e) == 0) BLOAD16(z1q[0][0], vz1, rZ1, 0, 0); \
    if ((e) == 1) BLOAD16(z1q[0][1], vz1, rZ1, 0, 1024); \
    if ((e) == 2) asm volatile("buffer_load_dwordx4 %0, %1, %2, %3 offen" : "=v"(z1q[1][0]) : "v"(vz1), "s"(rZ1), "s"(16 * 4608)); \
    if ((e) == 3) asm volatile("buffer_load_dwordx4 %0, %1, %2, %3 offen offset:1024" : "=v"(z1q[1][1]) : "v"(vz1), "s"(rZ1), "s"(16 * 4608)); \
    if ((e) == 4) asm volatile("buffer_load_dwordx2 %0, %1, %2, 0 offen" : "=v"(z1s[0]) : "v"(vz8), "s"(rZ1)); \
    if ((e) == 5) asm volatile("buffer_load_dwordx2 %0, %1, %2, %3 offen" : "=v"(z1s[1]) : "v"(vz8), "s"(rZ1), "s"(16 * 4608)); \
    if ((e) == 6) asm volatile("buffer_load_ushort %0, %1, %2, 0 offen" : "=v"(biasu[0]) : "v"(vb), "s"(rB)); \
    if ((e) == 7) asm volatile("buffer_load_ushort %0, %1, %2, 0 offen offset:8" : "=v"(biasu[1]) : "v"(vb), "s"(rB)); } while (0)

#define KB_ITER(i, j, MODE, WN) do { \
    _Pragma("unroll") for (int n = 0; n < 9; ++n) { \
      ADSRD(ZA[n], ((j) < 2 ? rd01 : rd23), ((j) & 1) * KB_STAGE + n * 1024); \
      if ((MODE) != 1) { AMFMA(acc[0][n], ZB[n], Wr[((j) + 3) & 3][2]); AMFMA(acc[1][n], ZB[n], Wr[((j) + 3) & 3][3]); } \
      if ((MODE) == 0) { \
        if (n == 0) KB_DMA1((i) + 3, ((j) + 3) & 3, 0); \
        if (n == 2) KB_DMA1((i) + 3, ((j) + 3) & 3, 1024); \
        if (n == 4) KB_W1((i) + 3, ((j) + 3) & 3, 0); \
        if (n == 6) KB_DMA1((i) + 3, ((j) + 3) & 3, 2048); \
        if (n == 8) KB_W1((i) + 3, ((j) + 3) & 3, 1); } \
      if ((MODE) == 2 && !(n & 1)) KB_EPI_LD(n >> 1); } \
    if ((MODE) == 1) { KB_ALL(2, 2); KB_DMA1(3, 3, 0); KB_DMA1(3, 3, 1024); KB_W1(3, 3, 0); KB_DMA1(3, 3, 2048); KB_W1(3, 3, 1); } \
    _Pragma("unroll") for (int n = 0; n < 9; ++n) { \
      if (n == 0) asm volatile("s_waitcnt lgkmcnt(6)" ::: "memory"); \
      if (n == 2) asm volatile("s_waitcnt lgkmcnt(5)" ::: "memory"); \
      if (n == 5) asm volatile("s_waitcnt lgkmcnt(5)" ::: "memory"); \
      ADSRD(ZB[n], ((j) < 2 ? rd01 : rd23), ((j) & 1) * KB_STAGE + 9216 + n * 1024); \
      AMFMA(acc[0][n], ZA[n], Wr[j][0]); AMFMA(acc[1][n], ZA[n], Wr[j][1]); \
      if ((MODE) < 2) { \
        if (n == 0) KB_DMA1((i) + 3, ((j) + 3) & 3, 3072); \
        if (n == 2) KB_W1((i) + 3, ((j) + 3) & 3, 2); \
        if (n == 4) KB_DMAH((i) + 3, ((j) + 3) & 3); \
        if (n == 6) KB_W1((i) + 3, ((j) + 3) & 3, 3); } \
      if ((MODE) == 2 && (n == 0 || n == 2 || n == 4)) KB_EPI_LD(5 + (n >> 1)); } \
    KB_WAIT(((j) + 1) & 3, WN); \
    __builtin_amdgcn_s_barrier(); \
    } while (0)

  KB_ITER(0, 0, 1, 18);
  KB_ITER(1, 1, 0, 18);
  KB_ITER(2, 2, 0, 18);
  KB_ITER(3, 3, 0, 18);
  for (int io = 4; io < 12; io += 4) {
    KB_ITER(io + 0, 0, 0, 18);
    KB_ITER(io + 1, 1, 0, 18);
    KB_ITER(io + 2, 2, 0, 18);
    KB_ITER(io + 3, 3, 0, 18);
  }
  KB_ITER(12, 0, 0, 18);
  KB_ITER(13, 1, 2, 17);
  KB_ITER(14, 2, 3, 8);
  {
#pragma unroll
    for (int n = 0; n < 9; ++n) { ADSRD(ZA[n], rd23, KB_STAGE + n * 1024); AMFMA(acc[0][n], ZB[n], Wr[2][2]); AMFMA(acc[1][n], ZB[n], Wr[2][3]); }
#pragma unroll
    for (int n = 0; n < 9; ++n) {
      if (n == 0) asm volatile("s_waitcnt lgkmcnt(6)" ::: "memory");
      if (n == 2) asm volatile("s_waitcnt lgkmcnt(5)" ::: "memory");
      if (n == 5) asm volatile("s_waitcnt lgkmcnt(5)" ::: "memory");
      ADSRD(ZB[n], rd23, KB_STAGE + 9216 + n * 1024);
      AMFMA(acc[0][n], ZA[n], Wr[3][0]); AMFMA(acc[1][n], ZA[n], Wr[3][1]); }
    asm volatile("s_waitcnt lgkmcnt(0)" ::: "memory");
#pragma unroll
    for (int n = 0; n < 9; ++n) { AMFMA(acc[0][n], ZB[n], Wr[3][2]); AMFMA(acc[1][n], ZB[n], Wr[3][3]); }
  }
  asm volatile("s_nop 15" : "+v"(acc[0][0]), "+v"(acc[0][1]), "+v"(acc[0][2]), "+v"(acc[0][3]), "+v"(acc[0][4]), "+v"(acc[0][5]), "+v"(acc[0][6]), "+v"(acc[0][7]), "+v"(acc[0][8]),
               "+v"(acc[1][0]), "+v"(acc[1][1]), "+v"(acc[1][2]), "+v"(acc[1][3]), "+v"(acc[1][4]), "+v"(acc[1][5]), "+v"(acc[1][6]), "+v"(acc[1][7]), "+v"(acc[1][8]) :: "memory");
  asm volatile("s_waitcnt vmcnt(0)" : "+v"(z1q[0][0]), "+v"(z1q[0][1]), "+v"(z1q[1][0]), "+v"(z1q[1][1]), "+v"(z1s[0]), "+v"(z1s[1]), "+v"(biasu[0]), "+v"(biasu[1]) :: "memory");
  SCHED();
  char* hp = ws + OFF_HT + ((size_t)(tp * 8 + wm * 2) * 16 + fb) * 4608;
  float bias[2] = {bf_lo(biasu[0]), bf_lo(biasu[1])};
  char* ex = smem + wm * 18432 + lane * 16;
  if (kh == 0) {
#pragma unroll
    for (int m = 0; m < 2; ++m)
#pragma unroll
      for (int n = 4; n < 8; ++n) *(f32x4*)(ex + (m * 9 + n) * 1024) = acc[m][n];
  } else {
#pragma unroll
    for (int m = 0; m < 2; ++m) {
#pragma unroll
      for (int n = 0; n < 4; ++n) *(f32x4*)(ex + (m * 9 + n) * 1024) = acc[m][n];
      *(f32x4*)(ex + (m * 9 + 8) * 1024) = acc[m][8];
    }
  }
  __syncthreads();
#pragma unroll
  for (int m = 0; m < 2; ++m) {
#pragma unroll
    for (int q = 0; q < 2; ++q) {
      f32x4 g0, g1;
      if (kh == 0) { g0 = acc[m][2 * q] + *(const f32x4*)(ex + (m * 9 + 2 * q) * 1024); g1 = acc[m][2 * q + 1] + *(const f32x4*)(ex + (m * 9 + 2 * q + 1) * 1024); }
      else { g0 = acc[m][4 + 2 * q] + *(const f32x4*)(ex + (m * 9 + 4 + 2 * q) * 1024); g1 = acc[m][5 + 2 * q] + *(const f32x4*)(ex + (m * 9 + 5 + 2 * q) * 1024); }
      u32x4 z = z1q[m][q], o;
      o[0] = pk_bf16(bf_lo(z[0]) * (g0[0] + bias[m]), bf_hi(z[0]) * (g0[1] + bias[m]));
      o[1] = pk_bf16(bf_lo(z[1]) * (g0[2] + bias[m]), bf_hi(z[1]) * (g0[3] + bias[m]));
      o[2] = pk_bf16(bf_lo(z[2]) * (g1[0] + bias[m]), bf_hi(z[2]) * (g1[1] + bias[m]));
      o[3] = pk_bf16(bf_lo(z[3]) * (g1[2] + bias[m]), bf_hi(z[3]) * (g1[3] + bias[m]));
      st16(hp + m * (16 * 4608) + (kh * 2 + q) * 1024 + lane * 16, o);
    }
    if (kh == 0) {
      f32x4 g0 = acc[m][8] + *(const f32x4*)(ex + (m * 9 + 8) * 1024);
      u32x2 z = z1s[m], o;
      o[0] = pk_bf16(bf_lo(z[0]) * (g0[0] + bias[m]), bf_hi(z[0]) * (g0[1] + bias[m]));
      o[1] = pk_bf16(bf_lo(z[1]) * (g0[2] + bias[m]), bf_hi(z[1]) * (g0[3] + bias[m]));
      *(u32x2*)(hp + m * (16 * 4608) + 4096 + lane * 8) = o;
    }
  }
}

constexpr int KC_PITCH = 336;
constexpr int KC_PART = 64 * KC_PITCH;
constexpr int KC_LDS = 5 * KC_PART;
template <bool ISO>
__device__ __forceinline__ void kc_main(f32x4 (&acc)[4][5], rsrc_t rA, rsrc_t rW, unsigned vA16, unsigned vA8, unsigned vW, unsigned sA, unsigned sW) {
  constexpr unsigned TS = ISO ? 18432u : 16u * 4608u;
  constexpr int PD = 3;
  bf16x8 A[PD + 1][4], W[PD + 1][5];
  auto load = [&](int u, int slot) {
#pragma unroll
    for (int m = 0; m < 4; ++m) {
      if (ISO || (u % 9) == 4) {
        unsigned o0, o1;
        if (ISO) { o0 = sA + m * TS + u * 1024; o1 = o0 + 512; }
        else { o0 = sA + m * TS + (u / 9) * 2 * 4608 + 4096; o1 = o0 + 4608; }
        u32x2 lo = __builtin_amdgcn_raw_buffer_load_b64(rA, vA8, o0, 0);
        u32x2 hi = __builtin_amdgcn_raw_buffer_load_b64(rA, vA8, o1, 0);
        u32x4 v = {lo[0], lo[1], hi[0], hi[1]};
        A[slot][m] = __builtin_bit_cast(bf16x8, v);
      } else {
        int i = u % 9;
        unsigned o = sA + m * TS + (u / 9) * 2 * 4608 + (i < 4 ? i * 1024 : 4608 + (i - 5) * 1024);
        A[slot][m] = __builtin_bit_cast(bf16x8, __builtin_amdgcn_raw_buffer_load_b128(rA, vA16, o, 0));
      }
    }
#pragma unroll
    for (int n = 0; n < 5; ++n) W[slot][n] = __builtin_bit_cast(bf16x8, __builtin_amdgcn_raw_buffer_load_b128(rW, vW, sW + (u * 5 + n) * 1024, 0));
  };
#pragma unroll
  for (int u = 0; u < PD; ++u) load(u, u);
  SCHED();
#pragma unroll
  for (int u = 0; u < 18; ++u) {
    if (u + PD < 18) load(u + PD, (u + PD) % (PD + 1));
    SCHED();
#pragma unroll
    for (int m = 0; m < 4; ++m)
#pragma unroll
      for (int n = 0; n < 5; ++n) acc[m][n] = MFMA(W[u % (PD + 1)][n], A[u % (PD + 1)][m], acc[m][n]);
    SCHED();
  }
}
__global__ __launch_bounds__(320, 2) void kC(const char* __restrict__ ws_c, const u16* __restrict__ x, u16* __restrict__ out) {
  extern __shared__ __attribute__((aligned(16))) char smem[];
  const int tid = threadIdx.x, lane = tid & 63;
  const int w = RFL(tid >> 6);
  const int b = blockIdx.x, xcd = b & 7, idx = b >> 3;
  const int rt = (xcd & 3) * 8 + (idx & 7), ct = (xcd >> 2) * 4 + (idx >> 3);
  f32x4 acc[4][5];
#pragma unroll
  for (int m = 0; m < 4; ++m)
#pragma unroll
    for (int n = 0; n < 5; ++n) acc[m][n] = (f32x4){0.f, 0.f, 0.f, 0.f};
  const rsrc_t rW = mk_rsrc(ws_c + OFF_WC, (int)SZ_WC);
  const unsigned sW = RFL((unsigned)((ct * 90 + w * 18) * 5 * 1024));
  const int dstart = ct < 7 ? ct * 72 : 496;
  if (w < 4) {
    const rsrc_t rA = mk_rsrc(ws_c + OFF_HT, (int)SZ_Z);
    kc_main<false>(acc, rA, rW, lane * 16, lane * 8, lane * 16, RFL((unsigned)((rt * 4 * 16 + w * 4) * 4608)), sW);
  } else {
    const rsrc_t rA = mk_rsrc(ws_c + OFF_OT, (int)SZ_OT);
    kc_main<true>(acc, rA, rW, lane * 16, lane * 8, lane * 16, RFL((unsigned)(rt * 4 * 18432)), sW);
  }
  {
    char* p = smem + w * KC_PART + (lane & 15) * KC_PITCH + (lane >> 4) * 16;
#pragma unroll
    for (int m = 0; m < 4; ++m)
#pragma unroll
      for (int n = 0; n < 5; ++n) *(f32x4*)(p + m * 16 * KC_PITCH + n * 64) = acc[m][n];
  }
  __syncthreads();
  const int own0 = ct * 72 - dstart;
  for (int c = tid; c < 576; c += 320) {
    int r = c / 9, ch = c - r * 9;
    const char* p = smem + r * KC_PITCH + (own0 + ch * 8) * 4;
    f32x4 s0 = *(const f32x4*)p, s1 = *(const f32x4*)(p + 16);
#pragma unroll
    for (int ww = 1; ww < 5; ++ww) { s0 += *(const f32x4*)(p + ww * KC_PART); s1 += *(const f32x4*)(p + ww * KC_PART + 16); }
    size_t go = (size_t)(rt * 64 + prow(r >> 4, r & 15)) * 576 + ct * 72 + ch * 8;
    u32x4 xv = *(const u32x4*)(x + go), o;
    o[0] = pk_bf16(s0[0] + bf_lo(xv[0]), s0[1] + bf_hi(xv[0]));
    o[1] = pk_bf16(s0[2] + bf_lo(xv[1]), s0[3] + bf_hi(xv[1]));
    o[2] = pk_bf16(s1[0] + bf_lo(xv[2]), s1[1] + bf_hi(xv[2]));
    o[3] = pk_bf16(s1[2] + bf_lo(xv[3]), s1[3] + bf_hi(xv[3]));
    st16(out + go, o);
  }
}

constexpr int K0_BLOCKS = 280;
constexpr int PFA_BYTES = 36864, PFB_BYTES = 9216, PFC_BYTES = 13312;
#define PF_TLB_N 16
#define PF_TLB_STRIDE 65536
__global__ __launch_bounds__(256) void k0(const u16* __restrict__ x, const u16* __restrict__ wqkv, const u16* __restrict__ win, char* __restrict__ ws, const u16* __restrict__ wsp, const u16* __restrict__ wout, const u16* __restrict__ wo) {
  __shared__ __attribute__((aligned(16))) char sm[32 * K0_PITCH];
  const int tid = threadIdx.x, lane = tid & 63, i = lane & 15, g = lane >> 4, w = tid >> 6, b = blockIdx.x;
  const unsigned smb = (unsigned)(size_t)(__attribute__((address_space(3))) char*)sm;
  unsigned pf0 = 0, pf1 = 0, pf2 = 0;
  if (b < 144) {
    const int ks = b >> 3, cb = b & 7;
    u32x4 v[9];
#pragma unroll
    for (int k = 0; k < 9; ++k) { int c = tid + k * 256, r = c / 72, ch = c - r * 72; v[k] = *(const u32x4*)(win + (size_t)(ks * 32 + r) * 4608 + cb * 576 + ch * 8); }
#pragma unroll
    for (int k = 0; k < 9; ++k) { int c = tid + k * 256, r = c / 72, ch = c - r * 72; *(u32x4*)(sm + r * K0_PITCH + ch * 16) = v[k]; }
    __syncthreads();
    const unsigned a0 = smb + (8 * g + (i >> 2)) * K0_PITCH + (i & 3) * 8;
#pragma unroll
    for (int e = 0; e < 9; ++e) {
      int nt = w * 9 + e;
      int gt = cb * 36 + nt;
      int z2 = gt >= 144, t = gt - z2 * 144, fb = t / 9, n = t - fb * 9;
      int fast = z2 ? n < 5 : n >= 4, nz = 4 + fast;
      int fr = fb * 468 + (fast ? 0 : 252) + 72 + (z2 ? ks * nz + n - (fast ? 0 : 5) : 18 * nz + ks * nz + n - (fast ? 4 : 0));
      u32x4 o = tr_frag(a0 + nt * 32, a0 + nt * 32 + 4 * K0_PITCH);
      st16(ws + OFF_W1 + (size_t)fr * 1024 + lane * 16, o);
    }
  } else if (b < 216) {
    const int u = b - 144, ks = u >> 2, hq = u & 3;
    u32x4 v[7];
#pragma unroll
    for (int k = 0; k < 7; ++k) { int c = tid + k * 256, r = c / 54, e = c - r * 54, part = e / 18, ch = e - part * 18;
      if (c < 32 * 54) v[k] = *(const u32x4*)(wqkv + (size_t)(ks * 32 + r) * 1728 + part * 576 + hq * 144 + ch * 8); }
#pragma unroll
    for (int k = 0; k < 7; ++k) { int c = tid + k * 256, r = c / 54, e = c - r * 54; if (c < 32 * 54) *(u32x4*)(sm + r * K0_PITCH + e * 16) = v[k]; }
    __syncthreads();
#pragma unroll
    for (int e = 0; e < 8; ++e) {
      int f = w * 8 + e, hl = f >> 2, n = f & 3, c = n * 16 + i, col; bool ok = true;
      if (c < 18) col = 144 + hl * 18 + c; else if (c >= 20 && c < 38) col = 288 + hl * 18 + c - 20; else if (c >= 40 && c < 58) col = hl * 18 + c - 40; else { col = 0; ok = false; }
      unsigned h[8];
#pragma unroll
      for (int j = 0; j < 8; ++j) h[j] = ok ? *(const u16*)(sm + (8 * g + j) * K0_PITCH + col * 2) : 0u;
      st16(ws + OFF_W1 + (size_t)(((hq * 8 + hl) >> 1) * 468 + (hl & 1) * 252 + ks * 4 + n) * 1024 + lane * 16, (u32x4){h[0] | (h[1] << 16), h[2] | (h[3] << 16), h[4] | (h[5] << 16), h[6] | (h[7] << 16)});
    }
  } else {
    if (b == 216) for (int c = tid; c < (2048 * 16 + 16 * 256) / 16; c += 256) *(u32x4*)(ws + OFF_RS + c * 16) = (u32x4){0, 0, 0, 0};
    const int u = (b - 216) * 4 + w, tt = u >> 1, slot = (u & 1) * 8 + (lane >> 3), c0 = lane & 7;
    const u16* xr = x + (size_t)prow(tt, slot) * 576 + c0 * 8;
    u32x4 v[9];
#pragma unroll
    for (int k = 0; k < 9; ++k) v[k] = *(const u32x4*)(xr + k * 64);
    float s = 0.f;
#pragma unroll
    for (int k = 0; k < 9; ++k)
#pragma unroll
      for (int j = 0; j < 4; ++j) s += bf_lo(v[k][j]) + bf_hi(v[k][j]);
    s += __shfl_xor(s, 1); s += __shfl_xor(s, 2); s += __shfl_xor(s, 4);
    const float mean = s * (1.f / 576.f);
    float q = 0.f;
#pragma unroll
    for (int k = 0; k < 9; ++k)
#pragma unroll
      for (int j = 0; j < 4; ++j) { float a = bf_lo(v[k][j]) - mean, c = bf_hi(v[k][j]) - mean; q += a * a + c * c; }
    q += __shfl_xor(q, 1); q += __shfl_xor(q, 2); q += __shfl_xor(q, 4);
    const float mb = rbf(mean), sd = rbf(sqrtf(rbf(rbf(q * (1.f / 576.f)) + 1e-5f))), rs = 1.f / sd;
    char* dst = ws + OFF_XT + (size_t)tt * 18432 + slot * 16;
#pragma unroll
    for (int k = 0; k < 9; ++k) {
      u32x4 o;
#pragma unroll
      for (int j = 0; j < 4; ++j) o[j] = pk_bf16(rbf(bf_lo(v[k][j]) - mb) * rs, rbf(bf_hi(v[k][j]) - mb) * rs);
      int c = c0 + 8 * k;
      st16(dst + (c >> 2) * 1024 + (c & 3) * 256, o);
    }
  }
  asm volatile("s_waitcnt vmcnt(0)" : "+v"(pf0), "+v"(pf1), "+v"(pf2) :: "memory");
}

extern "C" void kernel_launch(void* const* d_in, const int* in_sizes, int n_in, void* d_out, int out_size, void* d_ws, size_t ws_size, hipStream_t stream) {
  (void)in_sizes; (void)n_in; (void)out_size; (void)ws_size;
  const u16* x = (const u16*)d_in[0];
  char* ws = (char*)d_ws;
  (void)hipFuncSetAttribute((const void*)kA, hipFuncAttributeMaxDynamicSharedMemorySize, KA_LDS);
  (void)hipFuncSetAttribute((const void*)kB, hipFuncAttributeMaxDynamicSharedMemorySize, KB_LDS);
  (void)hipFuncSetAttribute((const void*)kC, hipFuncAttributeMaxDynamicSharedMemorySize, KC_LDS);
  k0<<<K0_BLOCKS, 256, 0, stream>>>(x, (const u16*)d_in[1], (const u16*)d_in[4], ws, (const u16*)d_in[5], (const u16*)d_in[7], (const u16*)d_in[2]);
  kA<<<512, 256, KA_LDS, stream>>>(ws, ws, (const u16*)d_in[5], (const u16*)d_in[7], (const u16*)d_in[2]);
  kB<<<256, 512, KB_LDS, stream>>>(ws, ws, (const u16*)d_in[6]);
  kC<<<256, 320, KC_LDS, stream>>>(ws, x, (u16*)d_out);
}
```

```cpp
#define PF_W
#include <hip/hip_runtime.h>
#include <hip/hip_bf16.h>
#include <stdint.h>

typedef unsigned short u16;
typedef __attribute__((ext_vector_type(8))) short bf16x8;
typedef __attribute__((ext_vector_type(4))) short bf16x4;
typedef __attribute__((ext_vector_type(4))) float f32x4;
typedef __attribute__((ext_vector_type(4))) unsigned u32x4;
typedef __attribute__((ext_vector_type(2))) unsigned u32x2;
typedef __amdgpu_buffer_rsrc_t rsrc_t;

constexpr int S_ = 2048, D_ = 576, F_ = 4608, FH_ = 2304;
constexpr size_t OFF_Z2T = 0;
constexpr size_t SZ_Z = (size_t)16 * 64 * 9 * 1024;
constexpr size_t OFF_Z1T = OFF_Z2T + SZ_Z;
constexpr size_t OFF_HT = OFF_Z1T + SZ_Z;
constexpr size_t OFF_WT = OFF_HT + SZ_Z;
constexpr size_t SZ_WT = (size_t)16 * 64 * 8 * 1024;
constexpr size_t OFF_OT = OFF_WT + SZ_WT;
constexpr size_t SZ_OT = (size_t)128 * 144 * 128;
constexpr size_t OFF_WC = OFF_OT + SZ_OT;
constexpr size_t SZ_WC = (size_t)8 * 90 * 5 * 1024;
constexpr size_t OFF_XT = OFF_WC + SZ_WC;
constexpr size_t SZ_XT = (size_t)128 * 18 * 1024;
constexpr size_t OFF_W1 = OFF_XT + SZ_XT;
constexpr size_t SZ_W1 = (size_t)32 * 234 * 1024;
constexpr size_t OFF_RS = OFF_W1 + SZ_W1;
constexpr size_t OFF_CNT = OFF_RS + 2048 * 16;
constexpr size_t OFF_ST = OFF_CNT + 16 * 256;
constexpr size_t WS_END = OFF_ST + (1 << 20);

#define RFL(x) __builtin_amdgcn_readfirstlane(x)
#define SCHED() __builtin_amdgcn_sched_barrier(0)
__device__ __forceinline__ rsrc_t mk_rsrc(const void* p, int bytes) { return __builtin_amdgcn_make_buffer_rsrc((void*)p, (short)0, bytes, 0x00020000); }
__device__ __forceinline__ float bf_lo(unsigned w) { return __uint_as_float(w << 16); }
__device__ __forceinline__ float bf_hi(unsigned w) { return __uint_as_float(w & 0xffff0000u); }
__device__ __forceinline__ unsigned pk_bf16(float lo, float hi) {
  typedef __attribute__((ext_vector_type(2))) float f32x2; typedef __attribute__((ext_vector_type(2))) __bf16 bf2;
  f32x2 v = {lo, hi}; bf2 r = __builtin_convertvector(v, bf2); return __builtin_bit_cast(unsigned, r);
}
__device__ __forceinline__ float rbf(float f) { return bf_hi(pk_bf16(0.f, f)); }
__host__ __device__ __forceinline__ int prow(int tt, int i) { return 32 * (tt >> 1) + 8 * (i >> 2) + 4 * (tt & 1) + (i & 3); }
__device__ __forceinline__ void st16(void* p, u32x4 v) { asm volatile("global_store_dwordx4 %0, %1, off sc1\n\ts_nop 1" :: "v"(p), "v"(v) : "memory"); }
#define MFMA(a, b, c) __builtin_amdgcn_mfma_f32_16x16x32_bf16(a, b, c, 0, 0, 0)

#define DMA16(lds, voff, srd, soff, imm) asm volatile("s_mov_b32 m0, %0\n\ts_nop 0\n\tbuffer_load_dwordx4 %1, %2, %3 offen offset:" #imm " lds" :: "s"(lds), "v"(voff), "s"(srd), "s"(soff) : "memory")
#define DMA16H(lds, voff, srd, soff) asm volatile("s_mov_b32 m0, %0\n\ts_lshr_b64 exec, -1, 32\n\tbuffer_load_dwordx4 %1, %2, %3 offen lds\n\ts_mov_b64 exec, -1" :: "s"(lds), "v"(voff), "s"(srd), "s"(soff) : "memory")
typedef __attribute__((address_space(3))) char lds_char;
#define LDS_RD128(addr) (*(const __attribute__((address_space(3))) bf16x8*)(addr))
#define AMFMA(acc, a, b) asm volatile("v_mfma_f32_16x16x32_bf16 %0, %1, %2, %0" : "+v"(acc) : "v"(a), "v"(b))
#define ADSRD(dst, addr, off) asm volatile("ds_read_b128 %0, %1 offset:%2" : "=v"(dst) : "v"(addr), "i"(off))
#define OPAQUE(x) asm volatile("" : "+v"(x))
#define BLOAD16(dst, voff, srd, soff, imm) asm volatile("buffer_load_dwordx4 %0, %1, %2, %3 offen offset:" #imm : "=v"(dst) : "v"(voff), "s"(srd), "s"(soff))

constexpr int K0_PITCH = 1168;
__device__ __forceinline__ int wc_krow(int ks, int g, int j) {
  int r = j & 3, hi = j >> 2;
  if (ks >= 72) { int u = ks - 72; return 2304 + 4 * (8 * u + 4 * hi + g) + r; }
  int w = ks / 18, pi = (ks % 18) / 9, i = ks % 9, fb0 = 4 * w + 2 * pi;
  if (i == 4) return 144 * (fb0 + hi) + 128 + 4 * g + r;
  if (i < 4) return 144 * fb0 + 32 * i + 16 * hi + 4 * g + r;
  return 144 * (fb0 + 1) + 32 * (i - 5) + 16 * hi + 4 * g + r;
}
__device__ __forceinline__ u32x4 tr_frag(unsigned a0, unsigned a1) {
  u32x2 lo, hi;
  asm volatile("ds_read_b64_tr_b16 %0, %2\n\tds_read_b64_tr_b16 %1, %3\n\ts_waitcnt lgkmcnt(0)" : "=&v"(lo), "=&v"(hi) : "v"(a0), "v"(a1) : "memory");
  return (u32x4){lo[0], lo[1], hi[0], hi[1]};
}
#define KA_SLEEP 32
constexpr int KA_SLOT = 10240, KA_ATT = 6 * KA_SLOT, KA_PITCH = 144, KA_LDS = KA_ATT + 142 * KA_PITCH;
template <int FAST> struct KaCfg {
  static constexpr int NZ2 = 4 + FAST, NZ1 = 4 + FAST;
  static constexpr int nt(int s) { return s < 9 ? 4 : 4 + FAST; }
  static constexpr int goff(int s) { return s < 9 ? s * 8192 : 73728 + (s - 9) * (4 + FAST) * 2048; }
  static constexpr int ops(int s) { return s > 26 ? 0 : 2 + (nt(s) == 5 ? 1 : 0) + (s < 9 ? 4 + ((s & 1) ? 0 : 1) : 0); }
  static constexpr int wait(int s) { return s == 20 ? ops(23) + ops(24) : ops(s + 2) + ops(s + 3) + ops(s + 4) + ((s >= 9 && s <= 12) ? 6 : 0); }
};
template <int V> struct IC { static constexpr int v = V; };
#define BLD(dst, voff, srd, soff, IMM) asm volatile("buffer_load_dwordx4 %0, %1, %2, %3 offen offset:%4" : "=v"(dst) : "v"(voff), "s"(srd), "s"(soff), "i"(IMM))
#define AMFMA0(acc, a, b) asm volatile("v_mfma_f32_16x16x32_bf16 %0, %1, %2, 0" : "=&v"(acc) : "v"(a), "v"(b))
typedef __attribute__((ext_vector_type(2))) float f32x2;
typedef __attribute__((ext_vector_type(2))) __bf16 bf2_t;
__device__ __forceinline__ float dot2(unsigned a, unsigned b, float c) { return __builtin_amdgcn_fdot2_f32_bf16(__builtin_bit_cast(bf2_t, a), __builtin_bit_cast(bf2_t, b), c, false); }
__device__ __forceinline__ f32x2 gelu2(f32x2 x) {
  f32x2 t = x * x, p = t * (-0.10294324f) + (-2.3022082f), e = x * p;
  f32x2 d = {__builtin_amdgcn_exp2f(e[0]) + 1.f, __builtin_amdgcn_exp2f(e[1]) + 1.f};
  f32x2 r = {__builtin_amdgcn_rcpf(d[0]), __builtin_amdgcn_rcpf(d[1])};
  return x * r;
}
template <int CTRL> __device__ __forceinline__ float dpp_add(float v) {
  return v + __builtin_bit_cast(float, __builtin_amdgcn_update_dpp(0, __builtin_bit_cast(int, v), CTRL, 0xf, 0xf, true));
}

__device__ __forceinline__ float xmax16(float v) { auto r = __builtin_amdgcn_permlane16_swap(__float_as_uint(v), __float_as_uint(v), false, false); return fmaxf(__uint_as_float(r[0]), __uint_as_float(r[1])); }
__device__ __forceinline__ float xmax32(float v) { auto r = __builtin_amdgcn_permlane32_swap(__float_as_uint(v), __float_as_uint(v), false, false); return fmaxf(__uint_as_float(r[0]), __uint_as_float(r[1])); }
__device__ __forceinline__ float xadd16(float v) { auto r = __builtin_amdgcn_permlane16_swap(__float_as_uint(v), __float_as_uint(v), false, false); return __uint_as_float(r[0]) + __uint_as_float(r[1]); }
__device__ __forceinline__ float xadd32(float v) { auto r = __builtin_amdgcn_permlane32_swap(__float_as_uint(v), __float_as_uint(v), false, false); return __uint_as_float(r[0]) + __uint_as_float(r[1]); }
#define PRIO_HI()
#define PRIO_LO()
#define LBAR() asm volatile("s_waitcnt lgkmcnt(0)\n\ts_barrier" ::: "memory")
__device__ __forceinline__ float swap1(float v) { return __builtin_bit_cast(float, __builtin_amdgcn_update_dpp(0, __builtin_bit_cast(int, v), 0xB1, 0xf, 0xf, true)); }
template <int FAST>
__device__ __forceinline__ void ka_body(char* smem, const char* __restrict__ ws_c, char* __restrict__ ws, const int p, const int cg, const int sj, const u16* __restrict__ wsp, const u16* __restrict__ wout, const u16* __restrict__ wo) {
  typedef KaCfg<FAST> C;
  const int tid = threadIdx.x, lane = tid & 63, li = lane & 15, lg = lane >> 4;
  const int w = RFL(tid >> 6), fb = cg >> 1;
  const rsrc_t rW = mk_rsrc(ws_c + OFF_W1 + (size_t)fb * (468 * 1024) + (FAST ? 0 : 252 * 1024), (FAST ? 252 : 216) * 1024);
  const rsrc_t rX = mk_rsrc(ws_c + OFF_XT, (int)SZ_XT);
  const unsigned ldsb = (unsigned)(size_t)(__attribute__((address_space(3))) char*)smem;
  const unsigned vl16 = lane * 16;
  const unsigned vX = vl16 + (unsigned)(p * 8 + w * 2) * 18432u;
  const unsigned vH = p == 0 ? 0x80000000u : (unsigned)(p * 8 - 2 + (li >> 3)) * 18432u + (unsigned)(lg * 16 + (li | 8)) * 16u + (unsigned)w * 1024u;
  const unsigned sF = RFL((unsigned)w * 2048u), sH = RFL(8192u + (unsigned)w * 512u);
  const unsigned rd = ldsb + vl16;
  constexpr int KA_WARM = 0;
  bf16x8 X[2][18], HX[5], F[5];
  f32x4 acc[2][5], hacc[3];
#pragma unroll
  for (int n = 0; n < 3; ++n) hacc[n] = (f32x4){0.f, 0.f, 0.f, 0.f};

  bf16x8 (&X_)[2][18] = X; bf16x8 (&HX_)[5] = HX; bf16x8 (&F_)[5] = F; f32x4 (&acc_)[2][5] = acc; f32x4 (&hacc_)[3] = hacc;
  const rsrc_t rW_ = rW, rX_ = rX; const unsigned vl16_ = vl16, vX_ = vX, vH_ = vH, rd_ = rd;
#define KA_CAP bf16x8 (&X)[2][18] = X_; bf16x8 (&HX)[5] = HX_; bf16x8 (&F)[5] = F_; f32x4 (&acc)[2][5] = acc_; f32x4 (&hacc)[3] = hacc_; \
    const rsrc_t rW = rW_, rX = rX_; const unsigned vl16 = vl16_, vX = vX_, vH = vH_, rd = rd_; (void)X; (void)HX; (void)F; (void)acc; (void)hacc; (void)rW; (void)rX; (void)vl16; (void)vX; (void)vH; (void)rd;
  auto vop = [&](auto S_, auto K_) {
    KA_CAP
    constexpr int s = decltype(S_)::v, k = decltype(K_)::v;
    if constexpr (s <= 26) {
      constexpr int slot = s % 6, nd = 2 + (C::nt(s) == 5 ? 1 : 0);
      if constexpr (k == 0) { unsigned lo = ldsb + slot * KA_SLOT + sF, so = C::goff(s) + sF; DMA16(lo, vl16, rW, so, 0); }
      else if constexpr (k == 1) { unsigned lo = ldsb + slot * KA_SLOT + sF, so = C::goff(s) + sF; DMA16(lo, vl16, rW, so, 1024); }
      else if constexpr (k == 2 && nd == 3) { unsigned lo = ldsb + slot * KA_SLOT + sH, so = C::goff(s) + sH; DMA16H(lo, vl16, rW, so); }
      else if constexpr (s < 9 && k >= 2 && k < 6) {
        constexpr int m = (k - 2) & 1, ks = 2 * s + ((k - 2) >> 1);
        BLD(X[m][ks], vX, rX, m * 18432 + (ks & ~3) * 1024, (ks & 3) * 1024);
      } else if constexpr (s < 9 && k == 6 && !(s & 1)) { BLD(HX[s >> 1], vH, rX, (s >> 1) * 4096, 0); }
    }
  };
  auto group = [&](auto S_) { vop(S_, IC<0>{}); vop(S_, IC<1>{}); vop(S_, IC<2>{}); vop(S_, IC<3>{}); vop(S_, IC<4>{}); vop(S_, IC<5>{}); vop(S_, IC<6>{}); };
  auto sync = [&](auto S_) {
    KA_CAP
    constexpr int s = decltype(S_)::v;
    if constexpr (s < 26) {
      if constexpr (s + 1 < 9) {
        constexpr int k0 = 2 * (s + 1);
        asm volatile("s_waitcnt vmcnt(%5)" : "+v"(X[0][k0]), "+v"(X[1][k0]), "+v"(X[0][k0 + 1]), "+v"(X[1][k0 + 1]), "+v"(HX[(s + 1) >> 1]) : "i"(C::wait(s)) : "memory");
      } else asm volatile("s_waitcnt vmcnt(%0)" :: "i"(C::wait(s)) : "memory");
      __builtin_amdgcn_s_barrier();
    }
  };
  auto step = [&](auto S_, auto H_) {
    KA_CAP
    constexpr int s = decltype(S_)::v, h = decltype(H_)::v;
    constexpr int NT = C::nt(s), job = s / 9, ks = 2 * (s % 9) + h;
    constexpr int s2 = h ? s + 1 : s, h2 = h ^ 1, NT2 = s2 <= 26 ? C::nt(s2) : 0;
    constexpr int roff = (s2 % 6) * KA_SLOT + h2 * NT2 * 1024;
    if constexpr (ks == 0) asm volatile("s_waitcnt lgkmcnt(0)" ::: "memory");
    if constexpr (job == 0) {
      if (w == (ks & 3)) {
        asm volatile("s_waitcnt lgkmcnt(1)" ::: "memory");
#pragma unroll
        for (int n = 0; n < 3; ++n) AMFMA(hacc[n], F[n], HX[ks >> 2]);
      }
    }
#pragma unroll
    for (int n = 0; n < NT; ++n) {
      if constexpr (ks != 0) asm volatile("s_waitcnt lgkmcnt(%0)" :: "i"(NT - 1) : "memory");
      if constexpr (job == 1) {
        if constexpr (ks == 0) { AMFMA0(acc[0][n], X[0][ks], F[n]); AMFMA0(acc[1][n], X[1][ks], F[n]); }
        else { AMFMA(acc[0][n], X[0][ks], F[n]); AMFMA(acc[1][n], X[1][ks], F[n]); }
      } else {
        if constexpr (ks == 0) { AMFMA0(acc[0][n], F[n], X[0][ks]); AMFMA0(acc[1][n], F[n], X[1][ks]); }
        else { AMFMA(acc[0][n], F[n], X[0][ks]); AMFMA(acc[1][n], F[n], X[1][ks]); }
      }
      if (n < NT2) ADSRD(F[n], rd, roff + n * 1024);
      if constexpr (h == 1) {
        if (n == 0) { vop(IC<s + 5>{}, IC<0>{}); vop(IC<s + 5>{}, IC<2>{}); }
        if (n == 1) { vop(IC<s + 5>{}, IC<1>{}); vop(IC<s + 5>{}, IC<3>{}); }
        if (n == 2) { vop(IC<s + 5>{}, IC<4>{}); vop(IC<s + 5>{}, IC<5>{}); }
        if (n == 3) { vop(IC<s + 5>{}, IC<6>{}); }
      }
    }
    if constexpr (NT2 > NT) ADSRD(F[4], rd, roff + 4 * 1024);
  };
#define KA_ACC_DONE() do { asm volatile("s_nop 15" : "+v"(acc[0][0]), "+v"(acc[0][1]), "+v"(acc[0][2]), "+v"(acc[0][3]), "+v"(acc[1][0]), "+v"(acc[1][1]), "+v"(acc[1][2]), "+v"(acc[1][3]) :: "memory"); \
    if constexpr (FAST) asm volatile("" : "+v"(acc[0][4]), "+v"(acc[1][4]) :: "memory"); } while (0)
  auto iter = [&](auto S_) { step(S_, IC<0>{}); sync(S_); step(S_, IC<1>{}); };

  group(IC<0>{}); group(IC<1>{});
  asm volatile("s_waitcnt vmcnt(%5)" : "+v"(X[0][0]), "+v"(X[1][0]), "+v"(X[0][1]), "+v"(X[1][1]), "+v"(HX[0]) : "i"(C::ops(1) + KA_WARM) : "memory");
  __builtin_amdgcn_s_barrier();
#pragma unroll
  for (int n = 0; n < 4; ++n) ADSRD(F[n], rd, n * 1024);

  step(IC<0>{}, IC<0>{}); group(IC<2>{}); group(IC<3>{}); group(IC<4>{}); sync(IC<0>{}); step(IC<0>{}, IC<1>{});
  iter(IC<1>{}); iter(IC<2>{}); iter(IC<3>{}); iter(IC<4>{}); iter(IC<5>{}); iter(IC<6>{}); iter(IC<7>{}); iter(IC<8>{});
  asm volatile("s_nop 15" : "+v"(acc[0][0]), "+v"(acc[0][1]), "+v"(acc[0][2]), "+v"(acc[0][3]), "+v"(acc[1][0]), "+v"(acc[1][1]), "+v"(acc[1][2]), "+v"(acc[1][3]), "+v"(hacc[0]), "+v"(hacc[1]), "+v"(hacc[2]) :: "memory");

  SCHED();
  {
    char* att = smem + KA_ATT;
    char* hp = smem + 2 * KA_SLOT;
#pragma unroll
    for (int n = 0; n < 3; ++n) if (n != w) *(f32x4*)(hp + (n * 3 + (w > n ? w - 1 : w)) * 1024 + vl16) = hacc[n];
#pragma unroll
    for (int m = 0; m < 2; ++m) {
      char* rp = att + (14 + w * 32 + 8 * (li >> 2) + 4 * m + (li & 3)) * KA_PITCH + lg * 8;
#pragma unroll
      for (int n = 0; n < 4; ++n) *(u32x2*)(rp + n * 32) = (u32x2){pk_bf16(acc[m][n][0], acc[m][n][1]), pk_bf16(acc[m][n][2], acc[m][n][3])};
    }
    LBAR();
    if (w < 3) {
      f32x4 a = w == 0 ? hacc[0] : (w == 1 ? hacc[1] : hacc[2]);
#pragma unroll
      for (int q = 0; q < 3; ++q) a += *(const f32x4*)(hp + (w * 3 + q) * 1024 + vl16);
      int ho = (li < 8) ? 8 * (li >> 2) + (li & 3) : 8 * ((li - 8) >> 2) + 4 + (li & 3);
      if (ho >= 2) *(u32x2*)(att + (ho - 2) * KA_PITCH + w * 32 + lg * 8) = (u32x2){pk_bf16(a[0], a[1]), pk_bf16(a[2], a[3])};
    }
    LBAR();
    const unsigned attb = ldsb + KA_ATT;
    float mbias[8];
#pragma unroll
    for (int e = 0; e < 8; ++e) { int d = (e >> 2) * 16 + lg * 4 + (e & 3) - li; mbias[e] = (!(d & 1) && d >= 2 && d <= 16) ? 0.f : -__builtin_inff(); }
    bf16x8 Q[2], K[2][2]; u32x2 V[2][2][2];
#pragma unroll
    for (int jj = 0; jj < 2; ++jj) {
      const int j = w * 2 + jj;
      Q[jj] = *(const bf16x8*)(att + (14 + 16 * j + li) * KA_PITCH + 80 + (lg < 3 ? lg : 2) * 16);
#pragma unroll
      for (int kt = 0; kt < 2; ++kt) {
        int R = 16 * j - 2 + 16 * kt + li; R = R < 0 ? 0 : R;
        K[jj][kt] = *(const bf16x8*)(att + R * KA_PITCH + lg * 16);
        int Rv = 16 * j - 2 + 16 * kt + 4 * lg + (li >> 2); Rv = Rv < 0 ? 0 : Rv;
        const unsigned va = attb + Rv * KA_PITCH + 40 + (li & 3) * 8;
        asm volatile("ds_read_b64_tr_b16 %0, %2\n\tds_read_b64_tr_b16 %1, %2 offset:32" : "=&v"(V[jj][0][kt]), "=&v"(V[jj][1][kt]) : "v"(va) : "memory");
      }
    }
    f32x4 S[2][2];
#pragma unroll
    for (int jj = 0; jj < 2; ++jj) {
      if (lg == 3) Q[jj] = (bf16x8){0, 0, 0, 0, 0, 0, 0, 0};
#pragma unroll
      for (int kt = 0; kt < 2; ++kt) S[jj][kt] = MFMA(K[jj][kt], Q[jj], ((f32x4){0.f, 0.f, 0.f, 0.f}));
    }
    bf16x8 P[2];
#pragma unroll
    for (int jj = 0; jj < 2; ++jj) {
      float l[8];
#pragma unroll
      for (int e = 0; e < 8; ++e) l[e] = S[jj][e >> 2][e & 3] + mbias[e];
      if (p == 0 && w == 0 && jj == 0) {
#pragma unroll
        for (int e = 0; e < 4; ++e) l[e] = -__builtin_inff();
      }
      float mx = fmaxf(fmaxf(fmaxf(l[0], l[1]), fmaxf(l[2], l[3])), fmaxf(fmaxf(l[4], l[5]), fmaxf(l[6], l[7])));
      mx = xmax16(mx); mx = xmax32(mx);
      constexpr float CE = 0.23570226f * 1.44269504f;
      const float nm = -mx * CE;
      float sum = 0.f;
#pragma unroll
      for (int e = 0; e < 8; ++e) { l[e] = __builtin_amdgcn_exp2f(l[e] * CE + nm); sum += l[e]; }
      sum = xadd16(sum); sum = xadd32(sum);
      const float inv = __builtin_amdgcn_rcpf(sum);
      u32x4 pk = {pk_bf16(l[0] * inv, l[1] * inv), pk_bf16(l[2] * inv, l[3] * inv), pk_bf16(l[4] * inv, l[5] * inv), pk_bf16(l[6] * inv, l[7] * inv)};
      P[jj] = __builtin_bit_cast(bf16x8, pk);
    }
    asm volatile("s_waitcnt lgkmcnt(0)" : "+v"(V[0][0][0]), "+v"(V[0][0][1]), "+v"(V[0][1][0]), "+v"(V[0][1][1]), "+v"(V[1][0][0]), "+v"(V[1][0][1]), "+v"(V[1][1][0]), "+v"(V[1][1][1]) :: "memory");
#pragma unroll
    for (int jj = 0; jj < 2; ++jj) {
      f32x4 O[2];
#pragma unroll
      for (int dt = 0; dt < 2; ++dt) {
        u32x4 vv = {V[jj][dt][0][0], V[jj][dt][0][1], V[jj][dt][1][0], V[jj][dt][1][1]};
        O[dt] = MFMA(__builtin_bit_cast(bf16x8, vv), P[jj], ((f32x4){0.f, 0.f, 0.f, 0.f}));
      }
      const int tl = jj * 16 + li, tt = p * 8 + w * 2 + ((tl >> 2) & 1), slot = (tl >> 3) * 4 + (tl & 3);
      char* ob = ws + OFF_OT + (size_t)tt * 18432 + slot * 8;
      const int d0 = cg * 18 + lg * 4, d1 = d0 + 2, d2 = cg * 18 + 16;
      *(unsigned*)(ob + (d0 >> 2) * 128 + (d0 & 3) * 2) = pk_bf16(O[0][0], O[0][1]);
      *(unsigned*)(ob + (d1 >> 2) * 128 + (d1 & 3) * 2) = pk_bf16(O[0][2], O[0][3]);
      if (lg == 0) *(unsigned*)(ob + (d2 >> 2) * 128 + (d2 & 3) * 2) = pk_bf16(O[1][0], O[1][1]);
    }
  }
  SCHED();
  PRIO_LO();

  iter(IC<9>{}); iter(IC<10>{}); iter(IC<11>{}); iter(IC<12>{}); iter(IC<13>{}); iter(IC<14>{}); iter(IC<15>{}); iter(IC<16>{}); iter(IC<17>{});
  KA_ACC_DONE();

  SCHED();
  PRIO_HI();
  unsigned zs[C::NZ2][4];
  {
#pragma unroll
    for (int n = 0; n < C::NZ2; ++n) {
#pragma unroll
      for (int m = 0; m < 2; ++m) {
        f32x2 a = gelu2((f32x2){acc[m][n][0], acc[m][n][1]}), b = gelu2((f32x2){acc[m][n][2], acc[m][n][3]});
        zs[n][2 * m] = pk_bf16(a[0], a[1]); zs[n][2 * m + 1] = pk_bf16(b[0], b[1]);
      }
    }
    f32x2 s1[4], s2[4];
#pragma unroll
    for (int j = 0; j < 4; ++j) {
      s1[j] = (f32x2){0.f, 0.f}; s2[j] = s1[j];
#pragma unroll
      for (int n = 0; n < C::NZ2; ++n) { f32x2 v = {bf_lo(zs[n][j]), bf_hi(zs[n][j])}; s1[j] += v; s2[j] += v * v; }
    }
    float r[16];
#pragma unroll
    for (int j = 0; j < 4; ++j) { r[2 * j] = s1[j][0]; r[2 * j + 1] = s1[j][1]; r[8 + 2 * j] = s2[j][0]; r[9 + 2 * j] = s2[j][1]; }
#pragma unroll
    for (int j = 0; j < 16; ++j) { float v = r[j]; v = dpp_add<0x128>(v); v = dpp_add<0x124>(v); v = dpp_add<0x122>(v); v = dpp_add<0x121>(v); r[j] = v; }
    float sel = r[0];
#pragma unroll
    for (int j = 1; j < 16; ++j) sel = (li == j) ? r[j] : sel;
    double* dst = (double*)(ws + OFF_RS) + (size_t)(p * 128 + w * 32 + lg * 8 + (li & 7)) * 2 + (li >> 3);
    double dv = (double)sel;
    asm volatile("global_atomic_add_f64 %0, %1, off sc1" :: "v"(dst), "v"(dv) : "memory");
  }
  SCHED();

  PRIO_LO();

  unsigned* cnt = (unsigned*)(ws + OFF_CNT + p * 256);
  iter(IC<18>{}); iter(IC<19>{});
  step(IC<20>{}, IC<0>{}); sync(IC<20>{});
  if (tid == 0) { unsigned one = 1; asm volatile("global_atomic_add %0, %1, off sc1" :: "v"(cnt), "v"(one) : "memory"); }
  step(IC<20>{}, IC<1>{});
  iter(IC<21>{}); iter(IC<22>{}); iter(IC<23>{}); iter(IC<24>{}); iter(IC<25>{});
  unsigned seen;
  asm volatile("global_load_dword %0, %1, off sc1" : "=v"(seen) : "v"(cnt) : "memory");
  iter(IC<26>{});
  KA_ACC_DONE();
  asm volatile("s_waitcnt vmcnt(0)" : "+v"(seen) :: "memory");
  PRIO_HI();
  const unsigned seen_s = RFL(seen);
  const bool early = seen_s >= 32u;
  const char* sp = ws + OFF_RS + (size_t)(p * 128 + w * 32 + (lane & 31)) * 16;
  u32x4 st = {0, 0, 0, 0};
  asm volatile("s_cmp_lt_u32 %2, 32\n\ts_cbranch_scc1 1f\n\tglobal_load_dwordx4 %0, %1, off sc1\n1:" : "+v"(st) : "v"(sp), "s"(seen_s) : "memory", "scc");
  SCHED();
  u32x4 wt[8];
  if constexpr (FAST) {
    const int f0 = (sj * 4 + w) * 8, ks = (f0 >> 3) & 63, tp = f0 >> 9;
#pragma unroll
    for (int m = 0; m < 8; ++m) wt[m] = *(const u32x4*)(wsp + (size_t)prow(tp * 8 + m, li) * 2048 + ks * 32 + lg * 8);
  }
#pragma unroll
  for (int m = 0; m < 2; ++m) {
    char* zp = ws + OFF_Z1T + ((size_t)(p * 8 + w * 2 + m) * 16 + fb) * 4608;
    unsigned pk[C::NZ1][2];
#pragma unroll
    for (int n = 0; n < C::NZ1; ++n) {
      f32x2 a = gelu2((f32x2){acc[m][n][0], acc[m][n][1]}), b = gelu2((f32x2){acc[m][n][2], acc[m][n][3]});
      pk[n][0] = pk_bf16(a[0], a[1]); pk[n][1] = pk_bf16(b[0], b[1]);
    }
#pragma unroll
    for (int q = 0; q < 2; ++q) st16(zp + (FAST * 2 + q) * 1024 + vl16, (u32x4){pk[2 * q][0], pk[2 * q][1], pk[2 * q + 1][0], pk[2 * q + 1][1]});
    if constexpr (FAST) *(u32x2*)(zp + 4096 + lane * 8) = (u32x2){pk[4][0], pk[4][1]};
  }
  SCHED();
  if constexpr (FAST) {
    const int f0 = (sj * 4 + w) * 8;
#pragma unroll
    for (int m = 0; m < 8; ++m) st16(ws + OFF_WT + (size_t)(f0 + m) * 1024 + vl16, wt[m]);
    if (sj < 90) {
      const int ks = sj, base0 = wc_krow(ks, 0, 0), base1 = wc_krow(ks, 0, 4);
      u32x4 v[9];
#pragma unroll
      for (int k = 0; k < 9; ++k) {
        int c = tid + k * 256, r = c / 72, ch = c - r * 72, row = (r < 16 ? base0 : base1 - 16) + r;
        const u16* src = row < 2304 ? wout + (size_t)row * 576 : wo + (size_t)(row - 2304) * 576;
        v[k] = *(const u32x4*)(src + ch * 8);
      }
      LBAR();
#pragma unroll
      for (int k = 0; k < 9; ++k) { int c = tid + k * 256, r = c / 72, ch = c - r * 72; *(u32x4*)(smem + r * K0_PITCH + ch * 16) = v[k]; }
      LBAR();
      const unsigned a0 = ldsb + (4 * lg + (li >> 2)) * K0_PITCH + (li & 3) * 8;
#pragma unroll
      for (int e = 0; e < 10; ++e) {
        int f = w * 10 + e, ct = f / 5, n = f - ct * 5, d0 = (ct < 7 ? ct * 72 : 496) + n * 16;
        u32x4 o = tr_frag(a0 + d0 * 2, a0 + d0 * 2 + 16 * K0_PITCH);
        st16(ws + OFF_WC + (size_t)((ct * 90 + ks) * 5 + n) * 1024 + vl16, o);
      }
    }
  }
  if (!early) {
    while (RFL(seen) < 32u) {
      __builtin_amdgcn_s_sleep(KA_SLEEP);
      asm volatile("global_load_dword %0, %1, off sc1\n\ts_waitcnt vmcnt(0)" : "=v"(seen) : "v"(cnt) : "memory");
    }
  }
  asm volatile("s_cmp_ge_u32 %2, 32\n\ts_cbranch_scc1 1f\n\tglobal_load_dwordx4 %0, %1, off sc1\n1:\n\ts_waitcnt vmcnt(0)" : "+v"(st) : "v"(sp), "s"(seen_s) : "memory", "scc");
  {
    const double sm = __builtin_bit_cast(double, (u32x2){st[0], st[1]}), sq = __builtin_bit_cast(double, (u32x2){st[2], st[3]});
    const double mean = sm * (1.0 / 2304.0);
    const float var = fmaxf((float)(sq * (1.0 / 2304.0) - mean * mean), 0.f);
    const float mb = rbf((float)mean), rs = 1.f / rbf(sqrtf(rbf(rbf(var) + 1e-5f)));
    f32x2* sl = (f32x2*)(smem + KA_ATT + w * 256);
    if (lane < 32) sl[lane] = (f32x2){mb, rs};
    __builtin_amdgcn_s_waitcnt(0xC07F);
    __builtin_amdgcn_wave_barrier();
    f32x4 ms[4];
#pragma unroll
    for (int j = 0; j < 4; ++j) ms[j] = *(const f32x4*)((const char*)sl + lg * 64 + j * 16);
    char* zp = ws + OFF_Z2T + ((size_t)(fb * 64 + p * 4 + w) * 9 + (FAST ? 0 : 5)) * 1024 + vl16;
#pragma unroll
    for (int n = 0; n < C::NZ2; ++n) {
      u32x4 o;
#pragma unroll
      for (int j = 0; j < 4; ++j) o[j] = pk_bf16((bf_lo(zs[n][j]) - ms[j][0]) * ms[j][1], (bf_hi(zs[n][j]) - ms[j][2]) * ms[j][3]);
      st16(zp + n * 1024, o);
    }
  }
}
__global__ __launch_bounds__(256, 2) void kA(const char* __restrict__ ws_c, char* __restrict__ ws, const u16* __restrict__ wsp, const u16* __restrict__ wout, const u16* __restrict__ wo) {
  extern __shared__ __attribute__((aligned(16))) char smem[];
  const int b = blockIdx.x, xcd = b & 7, idx = b >> 3;
  const int p = (xcd & 1) * 8 + (idx & 7), q = idx >> 3, cg = (xcd >> 1) * 8 + (q & 3) * 2 + (q >> 2);
  if (idx < 32) ka_body<1>(smem, ws_c, ws, p, cg, b, wsp, wout, wo); else ka_body<0>(smem, ws_c, ws, p, cg, b, wsp, wout, wo);
}

constexpr int KB_STAGE = 36864;
constexpr int KB_LDS = 4 * KB_STAGE;
__global__ __launch_bounds__(512, 2) void kB(const char* __restrict__ ws_c, char* __restrict__ ws, const u16* __restrict__ bsp) {
  extern __shared__ __attribute__((aligned(16))) char smem[];
  const int tid = threadIdx.x, lane = tid & 63;
  const int w = RFL(tid >> 6), wm = w & 3, kh = w >> 2;
  const int b = blockIdx.x, xcd = b & 7, idx = b >> 3;
  const int tp = (xcd & 1) * 8 + (idx & 7), fb = (xcd >> 1) * 4 + (idx >> 3);
  const rsrc_t rZ = mk_rsrc(ws_c + OFF_Z2T + (size_t)fb * (64 * 9216), 64 * 9216);
  const rsrc_t rW = mk_rsrc(ws_c + OFF_WT + (size_t)tp * (64 * 8192), 64 * 8192);
  const unsigned ldsb = (unsigned)(size_t)(__attribute__((address_space(3))) char*)smem;
  const unsigned vl16 = lane * 16;
  const unsigned vW = vl16 + wm * 2048 + kh * 16384;
  const unsigned dmaF = w * 4096, dmaH = 32768 + w * 512;
  unsigned rd01 = ldsb + kh * 18432 + lane * 16;
  unsigned rd23 = rd01 + 2 * KB_STAGE;

  f32x4 acc[2][9];
#pragma unroll
  for (int m = 0; m < 2; ++m)
#pragma unroll
    for (int n = 0; n < 9; ++n) acc[m][n] = (f32x4){0.f, 0.f, 0.f, 0.f};
  bf16x8 Wr[4][4];
  bf16x8 ZA[9], ZB[9];

#define KB_DMA1(stage, slot, imm) do { \
    unsigned so_ = RFL((unsigned)(stage) * KB_STAGE + dmaF), lo_ = RFL(ldsb + (slot) * KB_STAGE + dmaF); DMA16(lo_, vl16, rZ, so_, imm); } while (0)
#define KB_DMAH(stage, slot) do { \
    unsigned so2_ = RFL((unsigned)(stage) * KB_STAGE + dmaH), lo2_ = RFL(ldsb + (slot) * KB_STAGE + dmaH); DMA16H(lo2_, vl16, rZ, so2_); } while (0)
#define KB_W1(stage, slot, e) do { unsigned sw_ = RFL((unsigned)(stage) * 32768u + ((e) >> 1) * 8192u); \
    if ((e) & 1) BLOAD16(Wr[slot][e], vW, rW, sw_, 1024); else BLOAD16(Wr[slot][e], vW, rW, sw_, 0); } while (0)
#define KB_ALL(stage, slot) do { KB_DMA1(stage, slot, 0); KB_DMA1(stage, slot, 1024); KB_DMA1(stage, slot, 2048); KB_DMA1(stage, slot, 3072); KB_DMAH(stage, slot); \
    KB_W1(stage, slot, 0); KB_W1(stage, slot, 1); KB_W1(stage, slot, 2); KB_W1(stage, slot, 3); } while (0)
#define KB_WAIT(slot, N) asm volatile("s_waitcnt vmcnt(" #N ") lgkmcnt(0)" : "+v"(Wr[slot][0]), "+v"(Wr[slot][1]), "+v"(Wr[slot][2]), "+v"(Wr[slot][3]) :: "memory")

  KB_ALL(0, 0);
  __builtin_amdgcn_s_barrier();
  KB_ALL(1, 1);
  KB_WAIT(0, 9);
  __builtin_amdgcn_s_barrier();

  const rsrc_t rZ1 = mk_rsrc(ws_c + OFF_Z1T + ((size_t)(tp * 8 + wm * 2) * 16 + fb) * 4608, 17 * 4608);
  const rsrc_t rB = mk_rsrc(bsp, 4096);
  u32x4 z1q[2][2]; u32x2 z1s[2]; unsigned biasu[2];
  const unsigned vz1 = lane * 16 + kh * 2048, vz8 = lane * 8 + 4096;
  const unsigned vb = (tp * 128 + wm * 32 + ((lane & 15) >> 2) * 8 + (lane & 3)) * 2;
#define KB_EPI_LD(e) do { \
    if ((e) == 0) BLOAD16(z1q[0][0], vz1, rZ1, 0, 0); \
    if ((e) == 1) BLOAD16(z1q[0][1], vz1, rZ1, 0, 1024); \
    if ((e) == 2) asm volatile("buffer_load_dwordx4 %0, %1, %2, %3 offen" : "=v"(z1q[1][0]) : "v"(vz1), "s"(rZ1), "s"(16 * 4608)); \
    if ((e) == 3) asm volatile("buffer_load_dwordx4 %0, %1, %2, %3 offen offset:1024" : "=v"(z1q[1][1]) : "v"(vz1), "s"(rZ1), "s"(16 * 4608)); \
    if ((e) == 4) asm volatile("buffer_load_dwordx2 %0, %1, %2, 0 offen" : "=v"(z1s[0]) : "v"(vz8), "s"(rZ1)); \
    if ((e) == 5) asm volatile("buffer_load_dwordx2 %0, %1, %2, %3 offen" : "=v"(z1s[1]) : "v"(vz8), "s"(rZ1), "s"(16 * 4608)); \
    if ((e) == 6) asm volatile("buffer_load_ushort %0, %1, %2, 0 offen" : "=v"(biasu[0]) : "v"(vb), "s"(rB)); \
    if ((e) == 7) asm volatile("buffer_load_ushort %0, %1, %2, 0 offen offset:8" : "=v"(biasu[1]) : "v"(vb), "s"(rB)); } while (0)

#define KB_ITER(i, j, MODE, WN) do { \
    _Pragma("unroll") for (int n = 0; n < 9; ++n) { \
      ADSRD(ZA[n], ((j) < 2 ? rd01 : rd23), ((j) & 1) * KB_STAGE + n * 1024); \
      if ((MODE) != 1) { AMFMA(acc[0][n], ZB[n], Wr[((j) + 3) & 3][2]); AMFMA(acc[1][n], ZB[n], Wr[((j) + 3) & 3][3]); } \
      if ((MODE) == 0) { \
        if (n == 0) KB_DMA1((i) + 3, ((j) + 3) & 3, 0); \
        if (n == 2) KB_DMA1((i) + 3, ((j) + 3) & 3, 1024); \
        if (n == 4) KB_W1((i) + 3, ((j) + 3) & 3, 0); \
        if (n == 6) KB_DMA1((i) + 3, ((j) + 3) & 3, 2048); \
        if (n == 8) KB_W1((i) + 3, ((j) + 3) & 3, 1); } \
      if ((MODE) == 2 && !(n & 1)) KB_EPI_LD(n >> 1); } \
    if ((MODE) == 1) { KB_ALL(2, 2); KB_DMA1(3, 3, 0); KB_DMA1(3, 3, 1024); KB_W1(3, 3, 0); KB_DMA1(3, 3, 2048); KB_W1(3, 3, 1); } \
    _Pragma("unroll") for (int n = 0; n < 9; ++n) { \
      if (n == 0) asm volatile("s_waitcnt lgkmcnt(6)" ::: "memory"); \
      if (n == 2) asm volatile("s_waitcnt lgkmcnt(5)" ::: "memory"); \
      if (n == 5) asm volatile("s_waitcnt lgkmcnt(5)" ::: "memory"); \
      ADSRD(ZB[n], ((j) < 2 ? rd01 : rd23), ((j) & 1) * KB_STAGE + 9216 + n * 1024); \
      AMFMA(acc[0][n], ZA[n], Wr[j][0]); AMFMA(acc[1][n], ZA[n], Wr[j][1]); \
      if ((MODE) < 2) { \
        if (n == 0) KB_DMA1((i) + 3, ((j) + 3) & 3, 3072); \
        if (n == 2) KB_W1((i) + 3, ((j) + 3) & 3, 2); \
        if (n == 4) KB_DMAH((i) + 3, ((j) + 3) & 3); \
        if (n == 6) KB_W1((i) + 3, ((j) + 3) & 3, 3); } \
      if ((MODE) == 2 && (n == 0 || n == 2 || n == 4)) KB_EPI_LD(5 + (n >> 1)); } \
    KB_WAIT(((j) + 1) & 3, WN); \
    __builtin_amdgcn_s_barrier(); \
    } while (0)

  KB_ITER(0, 0, 1, 18);
  KB_ITER(1, 1, 0, 18);
  KB_ITER(2, 2, 0, 18);
  KB_ITER(3, 3, 0, 18);
  for (int io = 4; io < 12; io += 4) {
    KB_ITER(io + 0, 0, 0, 18);
    KB_ITER(io + 1, 1, 0, 18);
    KB_ITER(io + 2, 2, 0, 18);
    KB_ITER(io + 3, 3, 0, 18);
  }
  KB_ITER(12, 0, 0, 18);
  KB_ITER(13, 1, 2, 17);
  KB_ITER(14, 2, 3, 8);
  {
#pragma unroll
    for (int n = 0; n < 9; ++n) { ADSRD(ZA[n], rd23, KB_STAGE + n * 1024); AMFMA(acc[0][n], ZB[n], Wr[2][2]); AMFMA(acc[1][n], ZB[n], Wr[2][3]); }
#pragma unroll
    for (int n = 0; n < 9; ++n) {
      if (n == 0) asm volatile("s_waitcnt lgkmcnt(6)" ::: "memory");
      if (n == 2) asm volatile("s_waitcnt lgkmcnt(5)" ::: "memory");
      if (n == 5) asm volatile("s_waitcnt lgkmcnt(5)" ::: "memory");
      ADSRD(ZB[n], rd23, KB_STAGE + 9216 + n * 1024);
      AMFMA(acc[0][n], ZA[n], Wr[3][0]); AMFMA(acc[1][n], ZA[n], Wr[3][1]); }
    asm volatile("s_waitcnt lgkmcnt(0)" ::: "memory");
#pragma unroll
    for (int n = 0; n < 9; ++n) { AMFMA(acc[0][n], ZB[n], Wr[3][2]); AMFMA(acc[1][n], ZB[n], Wr[3][3]); }
  }
  asm volatile("s_nop 15" : "+v"(acc[0][0]), "+v"(acc[0][1]), "+v"(acc[0][2]), "+v"(acc[0][3]), "+v"(acc[0][4]), "+v"(acc[0][5]), "+v"(acc[0][6]), "+v"(acc[0][7]), "+v"(acc[0][8]),
               "+v"(acc[1][0]), "+v"(acc[1][1]), "+v"(acc[1][2]), "+v"(acc[1][3]), "+v"(acc[1][4]), "+v"(acc[1][5]), "+v"(acc[1][6]), "+v"(acc[1][7]), "+v"(acc[1][8]) :: "memory");
  asm volatile("s_waitcnt vmcnt(0)" : "+v"(z1q[0][0]), "+v"(z1q[0][1]), "+v"(z1q[1][0]), "+v"(z1q[1][1]), "+v"(z1s[0]), "+v"(z1s[1]), "+v"(biasu[0]), "+v"(biasu[1]) :: "memory");
  SCHED();
  char* hp = ws + OFF_HT + ((size_t)(tp * 8 + wm * 2) * 16 + fb) * 4608;
  float bias[2] = {bf_lo(biasu[0]), bf_lo(biasu[1])};
  char* ex = smem + wm * 18432 + lane * 16;
  if (kh == 0) {
#pragma unroll
    for (int m = 0; m < 2; ++m)
#pragma unroll
      for (int n = 4; n < 8; ++n) *(f32x4*)(ex + (m * 9 + n) * 1024) = acc[m][n];
  } else {
#pragma unroll
    for (int m = 0; m < 2; ++m) {
#pragma unroll
      for (int n = 0; n < 4; ++n) *(f32x4*)(ex + (m * 9 + n) * 1024) = acc[m][n];
      *(f32x4*)(ex + (m * 9 + 8) * 1024) = acc[m][8];
    }
  }
  __syncthreads();
#pragma unroll
  for (int m = 0; m < 2; ++m) {
#pragma unroll
    for (int q = 0; q < 2; ++q) {
      f32x4 g0, g1;
      if (kh == 0) { g0 = acc[m][2 * q] + *(const f32x4*)(ex + (m * 9 + 2 * q) * 1024); g1 = acc[m][2 * q + 1] + *(const f32x4*)(ex + (m * 9 + 2 * q + 1) * 1024); }
      else { g0 = acc[m][4 + 2 * q] + *(const f32x4*)(ex + (m * 9 + 4 + 2 * q) * 1024); g1 = acc[m][5 + 2 * q] + *(const f32x4*)(ex + (m * 9 + 5 + 2 * q) * 1024); }
      u32x4 z = z1q[m][q], o;
      o[0] = pk_bf16(bf_lo(z[0]) * (g0[0] + bias[m]), bf_hi(z[0]) * (g0[1] + bias[m]));
      o[1] = pk_bf16(bf_lo(z[1]) * (g0[2] + bias[m]), bf_hi(z[1]) * (g0[3] + bias[m]));
      o[2] = pk_bf16(bf_lo(z[2]) * (g1[0] + bias[m]), bf_hi(z[2]) * (g1[1] + bias[m]));
      o[3] = pk_bf16(bf_lo(z[3]) * (g1[2] + bias[m]), bf_hi(z[3]) * (g1[3] + bias[m]));
      st16(hp + m * (16 * 4608) + (kh * 2 + q) * 1024 + lane * 16, o);
    }
    if (kh == 0) {
      f32x4 g0 = acc[m][8] + *(const f32x4*)(ex + (m * 9 + 8) * 1024);
      u32x2 z = z1s[m], o;
      o[0] = pk_bf16(bf_lo(z[0]) * (g0[0] + bias[m]), bf_hi(z[0]) * (g0[1] + bias[m]));
      o[1] = pk_bf16(bf_lo(z[1]) * (g0[2] + bias[m]), bf_hi(z[1]) * (g0[3] + bias[m]));
      *(u32x2*)(hp + m * (16 * 4608) + 4096 + lane * 8) = o;
    }
  }
}

constexpr int KC_PITCH = 336;
constexpr int KC_PART = 64 * KC_PITCH;
constexpr int KC_LDS = 5 * KC_PART;
template <bool ISO>
__device__ __forceinline__ void kc_main(f32x4 (&acc)[4][5], rsrc_t rA, rsrc_t rW, unsigned vA16, unsigned vA8, unsigned vW, unsigned sA, unsigned sW) {
  constexpr unsigned TS = ISO ? 18432u : 16u * 4608u;
  constexpr int PD = 3;
  bf16x8 A[PD + 1][4], W[PD + 1][5];
  auto load = [&](int u, int slot) {
#pragma unroll
    for (int m = 0; m < 4; ++m) {
      if (ISO || (u % 9) == 4) {
        unsigned o0, o1;
        if (ISO) { o0 = sA + m * TS + u * 1024; o1 = o0 + 512; }
        else { o0 = sA + m * TS + (u / 9) * 2 * 4608 + 4096; o1 = o0 + 4608; }
        u32x2 lo = __builtin_amdgcn_raw_buffer_load_b64(rA, vA8, o0, 0);
        u32x2 hi = __builtin_amdgcn_raw_buffer_load_b64(rA, vA8, o1, 0);
        u32x4 v = {lo[0], lo[1], hi[0], hi[1]};
        A[slot][m] = __builtin_bit_cast(bf16x8, v);
      } else {
        int i = u % 9;
        unsigned o = sA + m * TS + (u / 9) * 2 * 4608 + (i < 4 ? i * 1024 : 4608 + (i - 5) * 1024);
        A[slot][m] = __builtin_bit_cast(bf16x8, __builtin_amdgcn_raw_buffer_load_b128(rA, vA16, o, 0));
      }
    }
#pragma unroll
    for (int n = 0; n < 5; ++n) W[slot][n] = __builtin_bit_cast(bf16x8, __builtin_amdgcn_raw_buffer_load_b128(rW, vW, sW + (u * 5 + n) * 1024, 0));
  };
#pragma unroll
  for (int u = 0; u < PD; ++u) load(u, u);
  SCHED();
#pragma unroll
  for (int u = 0; u < 18; ++u) {
    if (u + PD < 18) load(u + PD, (u + PD) % (PD + 1));
    SCHED();
#pragma unroll
    for (int m = 0; m < 4; ++m)
#pragma unroll
      for (int n = 0; n < 5; ++n) acc[m][n] = MFMA(W[u % (PD + 1)][n], A[u % (PD + 1)][m], acc[m][n]);
    SCHED();
  }
}
__global__ __launch_bounds__(320, 2) void kC(const char* __restrict__ ws_c, const u16* __restrict__ x, u16* __restrict__ out) {
  extern __shared__ __attribute__((aligned(16))) char smem[];
  const int tid = threadIdx.x, lane = tid & 63;
  const int w = RFL(tid >> 6);
  const int b = blockIdx.x, xcd = b & 7, idx = b >> 3;
  const int rt = (xcd & 3) * 8 + (idx & 7), ct = (xcd >> 2) * 4 + (idx >> 3);
  f32x4 acc[4][5];
#pragma unroll
  for (int m = 0; m < 4; ++m)
#pragma unroll
    for (int n = 0; n < 5; ++n) acc[m][n] = (f32x4){0.f, 0.f, 0.f, 0.f};
  const rsrc_t rW = mk_rsrc(ws_c + OFF_WC, (int)SZ_WC);
  const unsigned sW = RFL((unsigned)((ct * 90 + w * 18) * 5 * 1024));
  const int dstart = ct < 7 ? ct * 72 : 496;
  if (w < 4) {
    const rsrc_t rA = mk_rsrc(ws_c + OFF_HT, (int)SZ_Z);
    kc_main<false>(acc, rA, rW, lane * 16, lane * 8, lane * 16, RFL((unsigned)((rt * 4 * 16 + w * 4) * 4608)), sW);
  } else {
    const rsrc_t rA = mk_rsrc(ws_c + OFF_OT, (int)SZ_OT);
    kc_main<true>(acc, rA, rW, lane * 16, lane * 8, lane * 16, RFL((unsigned)(rt * 4 * 18432)), sW);
  }
  {
    char* p = smem + w * KC_PART + (lane & 15) * KC_PITCH + (lane >> 4) * 16;
#pragma unroll
    for (int m = 0; m < 4; ++m)
#pragma unroll
      for (int n = 0; n < 5; ++n) *(f32x4*)(p + m * 16 * KC_PITCH + n * 64) = acc[m][n];
  }
  __syncthreads();
  const int own0 = ct * 72 - dstart;
  for (int c = tid; c < 576; c += 320) {
    int r = c / 9, ch = c - r * 9;
    const char* p = smem + r * KC_PITCH + (own0 + ch * 8) * 4;
    f32x4 s0 = *(const f32x4*)p, s1 = *(const f32x4*)(p + 16);
#pragma unroll
    for (int ww = 1; ww < 5; ++ww) { s0 += *(const f32x4*)(p + ww * KC_PART); s1 += *(const f32x4*)(p + ww * KC_PART + 16); }
    size_t go = (size_t)(rt * 64 + prow(r >> 4, r & 15)) * 576 + ct * 72 + ch * 8;
    u32x4 xv = *(const u32x4*)(x + go), o;
    o[0] = pk_bf16(s0[0] + bf_lo(xv[0]), s0[1] + bf_hi(xv[0]));
    o[1] = pk_bf16(s0[2] + bf_lo(xv[1]), s0[3] + bf_hi(xv[1]));
    o[2] = pk_bf16(s1[0] + bf_lo(xv[2]), s1[1] + bf_hi(xv[2]));
    o[3] = pk_bf16(s1[2] + bf_lo(xv[3]), s1[3] + bf_hi(xv[3]));
    st16(out + go, o);
  }
}

constexpr int PF_WB = 64;
constexpr int K0_BLOCKS = 280 + PF_WB;
constexpr int PFA_BYTES = 36864, PFB_BYTES = 9216, PFC_BYTES = 13312;
#define PF_TLB_N 16
#define PF_TLB_STRIDE 65536
__global__ __launch_bounds__(256) void k0(const u16* __restrict__ x, const u16* __restrict__ wqkv, const u16* __restrict__ win, char* __restrict__ ws, const u16* __restrict__ wsp, const u16* __restrict__ wout, const u16* __restrict__ wo) {
  __shared__ __attribute__((aligned(16))) char sm[32 * K0_PITCH];
  const int tid = threadIdx.x, lane = tid & 63, i = lane & 15, g = lane >> 4, w = tid >> 6, b = blockIdx.x;
  const unsigned smb = (unsigned)(size_t)(__attribute__((address_space(3))) char*)sm;
  unsigned pf0 = 0, pf1 = 0, pf2 = 0;
  if (b >= K0_BLOCKS - PF_WB) {
    const int u = (b - (K0_BLOCKS - PF_WB)) * 256 + tid;
    for (int c = u; c < 65536 + 20736 + 5184; c += PF_WB * 256) {
      const char* a = c < 65536 ? (const char*)wsp + (size_t)c * 128 : (c < 65536 + 20736 ? (const char*)wout + (size_t)(c - 65536) * 128 : (const char*)wo + (size_t)(c - 65536 - 20736) * 128);
      asm volatile("global_load_dword %0, %1, off" : "+v"(pf2) : "v"(a));
    }
    asm volatile("s_waitcnt vmcnt(0)" : "+v"(pf2) :: "memory");
    return;
  }
  if (b < 144) {
    const int ks = b >> 3, cb = b & 7;
    u32x4 v[9];
#pragma unroll
    for (int k = 0; k < 9; ++k) { int c = tid + k * 256, r = c / 72, ch = c - r * 72; v[k] = *(const u32x4*)(win + (size_t)(ks * 32 + r) * 4608 + cb * 576 + ch * 8); }
#pragma unroll
    for (int k = 0; k < 9; ++k) { int c = tid + k * 256, r = c / 72, ch = c - r * 72; *(u32x4*)(sm + r * K0_PITCH + ch * 16) = v[k]; }
    __syncthreads();
    const unsigned a0 = smb + (8 * g + (i >> 2)) * K0_PITCH + (i & 3) * 8;
#pragma unroll
    for (int e = 0; e < 9; ++e) {
      int nt = w * 9 + e;
      int gt = cb * 36 + nt;
      int z2 = gt >= 144, t = gt - z2 * 144, fb = t / 9, n = t - fb * 9;
      int fast = z2 ? n < 5 : n >= 4, nz = 4 + fast;
      int fr = fb * 468 + (fast ? 0 : 252) + 72 + (z2 ? ks * nz + n - (fast ? 0 : 5) : 18 * nz + ks * nz + n - (fast ? 4 : 0));
      u32x4 o = tr_frag(a0 + nt * 32, a0 + nt * 32 + 4 * K0_PITCH);
      st16(ws + OFF_W1 + (size_t)fr * 1024 + lane * 16, o);
    }
  } else if (b < 216) {
    const int u = b - 144, ks = u >> 2, hq = u & 3;
    u32x4 v[7];
#pragma unroll
    for (int k = 0; k < 7; ++k) { int c = tid + k * 256, r = c / 54, e = c - r * 54, part = e / 18, ch = e - part * 18;
      if (c < 32 * 54) v[k] = *(const u32x4*)(wqkv + (size_t)(ks * 32 + r) * 1728 + part * 576 + hq * 144 + ch * 8); }
#pragma unroll
    for (int k = 0; k < 7; ++k) { int c = tid + k * 256, r = c / 54, e = c - r * 54; if (c < 32 * 54) *(u32x4*)(sm + r * K0_PITCH + e * 16) = v[k]; }
    __syncthreads();
#pragma unroll
    for (int e = 0; e < 8; ++e) {
      int f = w * 8 + e, hl = f >> 2, n = f & 3, c = n * 16 + i, col; bool ok = true;
      if (c < 18) col = 144 + hl * 18 + c; else if (c >= 20 && c < 38) col = 288 + hl * 18 + c - 20; else if (c >= 40 && c < 58) col = hl * 18 + c - 40; else { col = 0; ok = false; }
      unsigned h[8];
#pragma unroll
      for (int j = 0; j < 8; ++j) h[j] = ok ? *(const u16*)(sm + (8 * g + j) * K0_PITCH + col * 2) : 0u;
      st16(ws + OFF_W1 + (size_t)(((hq * 8 + hl) >> 1) * 468 + (hl & 1) * 252 + ks * 4 + n) * 1024 + lane * 16, (u32x4){h[0] | (h[1] << 16), h[2] | (h[3] << 16), h[4] | (h[5] << 16), h[6] | (h[7] << 16)});
    }
  } else {
    if (b == 216) for (int c = tid; c < (2048 * 16 + 16 * 256) / 16; c += 256) *(u32x4*)(ws + OFF_RS + c * 16) = (u32x4){0, 0, 0, 0};
    const int u = (b - 216) * 4 + w, tt = u >> 1, slot = (u & 1) * 8 + (lane >> 3), c0 = lane & 7;
    const u16* xr = x + (size_t)prow(tt, slot) * 576 + c0 * 8;
    u32x4 v[9];
#pragma unroll
    for (int k = 0; k < 9; ++k) v[k] = *(const u32x4*)(xr + k * 64);
    float s = 0.f;
#pragma unroll
    for (int k = 0; k < 9; ++k)
#pragma unroll
      for (int j = 0; j < 4; ++j) s += bf_lo(v[k][j]) + bf_hi(v[k][j]);
    s += __shfl_xor(s, 1); s += __shfl_xor(s, 2); s += __shfl_xor(s, 4);
    const float mean = s * (1.f / 576.f);
    float q = 0.f;
#pragma unroll
    for (int k = 0; k < 9; ++k)
#pragma unroll
      for (int j = 0; j < 4; ++j) { float a = bf_lo(v[k][j]) - mean, c = bf_hi(v[k][j]) - mean; q += a * a + c * c; }
    q += __shfl_xor(q, 1); q += __shfl_xor(q, 2); q += __shfl_xor(q, 4);
    const float mb = rbf(mean), sd = rbf(sqrtf(rbf(rbf(q * (1.f / 576.f)) + 1e-5f))), rs = 1.f / sd;
    char* dst = ws + OFF_XT + (size_t)tt * 18432 + slot * 16;
#pragma unroll
    for (int k = 0; k < 9; ++k) {
      u32x4 o;
#pragma unroll
      for (int j = 0; j < 4; ++j) o[j] = pk_bf16(rbf(bf_lo(v[k][j]) - mb) * rs, rbf(bf_hi(v[k][j]) - mb) * rs);
      int c = c0 + 8 * k;
      st16(dst + (c >> 2) * 1024 + (c & 3) * 256, o);
    }
  }
  asm volatile("s_waitcnt vmcnt(0)" : "+v"(pf0), "+v"(pf1), "+v"(pf2) :: "memory");
}

extern "C" void kernel_launch(void* const* d_in, const int* in_sizes, int n_in, void* d_out, int out_size, void* d_ws, size_t ws_size, hipStream_t stream) {
  (void)in_sizes; (void)n_in; (void)out_size; (void)ws_size;
  const u16* x = (const u16*)d_in[0];
  char* ws = (char*)d_ws;
  (void)hipFuncSetAttribute((const void*)kA, hipFuncAttributeMaxDynamicSharedMemorySize, KA_LDS);
  (void)hipFuncSetAttribute((const void*)kB, hipFuncAttributeMaxDynamicSharedMemorySize, KB_LDS);
  (void)hipFuncSetAttribute((const void*)kC, hipFuncAttributeMaxDynamicSharedMemorySize, KC_LDS);
  k0<<<K0_BLOCKS, 256, 0, stream>>>(x, (const u16*)d_in[1], (const u16*)d_in[4], ws, (const u16*)d_in[5], (const u16*)d_in[7], (const u16*)d_in[2]);
  kA<<<512, 256, KA_LDS, stream>>>(ws, ws, (const u16*)d_in[5], (const u16*)d_in[7], (const u16*)d_in[2]);
  kB<<<256, 512, KB_LDS, stream>>>(ws, ws, (const u16*)d_in[6]);
  kC<<<256, 320, KC_LDS, stream>>>(ws, x, (u16*)d_out);
}
```

```cpp
#define PF_B
#define PF_A
#include <hip/hip_runtime.h>
#include <hip/hip_bf16.h>
#include <stdint.h>

typedef unsigned short u16;
typedef __attribute__((ext_vector_type(8))) short bf16x8;
typedef __attribute__((ext_vector_type(4))) short bf16x4;
typedef __attribute__((ext_vector_type(4))) float f32x4;
typedef __attribute__((ext_vector_type(4))) unsigned u32x4;
typedef __attribute__((ext_vector_type(2))) unsigned u32x2;
typedef __amdgpu_buffer_rsrc_t rsrc_t;

constexpr int S_ = 2048, D_ = 576, F_ = 4608, FH_ = 2304;
constexpr size_t OFF_Z2T = 0;
constexpr size_t SZ_Z = (size_t)16 * 64 * 9 * 1024;
constexpr size_t OFF_Z1T = OFF_Z2T + SZ_Z;
constexpr size_t OFF_HT = OFF_Z1T + SZ_Z;
constexpr size_t OFF_WT = OFF_HT + SZ_Z;
constexpr size_t SZ_WT = (size_t)16 * 64 * 8 * 1024;
constexpr size_t OFF_OT = OFF_WT + SZ_WT;
constexpr size_t SZ_OT = (size_t)128 * 144 * 128;
constexpr size_t OFF_WC = OFF_OT + SZ_OT;
constexpr size_t SZ_WC = (size_t)8 * 90 * 5 * 1024;
constexpr size_t OFF_XT = OFF_WC + SZ_WC;
constexpr size_t SZ_XT = (size_t)128 * 18 * 1024;
constexpr size_t OFF_W1 = OFF_XT + SZ_XT;
constexpr size_t SZ_W1 = (size_t)32 * 234 * 1024;
constexpr size_t OFF_RS = OFF_W1 + SZ_W1;
constexpr size_t OFF_CNT = OFF_RS + 2048 * 16;
constexpr size_t OFF_ST = OFF_CNT + 16 * 256;
constexpr size_t WS_END = OFF_ST + (1 << 20);

#define RFL(x) __builtin_amdgcn_readfirstlane(x)
#define SCHED() __builtin_amdgcn_sched_barrier(0)
__device__ __forceinline__ rsrc_t mk_rsrc(const void* p, int bytes) { return __builtin_amdgcn_make_buffer_rsrc((void*)p, (short)0, bytes, 0x00020000); }
__device__ __forceinline__ float bf_lo(unsigned w) { return __uint_as_float(w << 16); }
__device__ __forceinline__ float bf_hi(unsigned w) { return __uint_as_float(w & 0xffff0000u); }
__device__ __forceinline__ unsigned pk_bf16(float lo, float hi) {
  typedef __attribute__((ext_vector_type(2))) float f32x2; typedef __attribute__((ext_vector_type(2))) __bf16 bf2;
  f32x2 v = {lo, hi}; bf2 r = __builtin_convertvector(v, bf2); return __builtin_bit_cast(unsigned, r);
}
__device__ __forceinline__ float rbf(float f) { return bf_hi(pk_bf16(0.f, f)); }
__host__ __device__ __forceinline__ int prow(int tt, int i) { return 32 * (tt >> 1) + 8 * (i >> 2) + 4 * (tt & 1) + (i & 3); }
__device__ __forceinline__ void st16(void* p, u32x4 v) { asm volatile("global_store_dwordx4 %0, %1, off sc1\n\ts_nop 1" :: "v"(p), "v"(v) : "memory"); }
#define MFMA(a, b, c) __builtin_amdgcn_mfma_f32_16x16x32_bf16(a, b, c, 0, 0, 0)

#define DMA16(lds, voff, srd, soff, imm) asm volatile("s_mov_b32 m0, %0\n\ts_nop 0\n\tbuffer_load_dwordx4 %1, %2, %3 offen offset:" #imm " lds" :: "s"(lds), "v"(voff), "s"(srd), "s"(soff) : "memory")
#define DMA16H(lds, voff, srd, soff) asm volatile("s_mov_b32 m0, %0\n\ts_lshr_b64 exec, -1, 32\n\tbuffer_load_dwordx4 %1, %2, %3 offen lds\n\ts_mov_b64 exec, -1" :: "s"(lds), "v"(voff), "s"(srd), "s"(soff) : "memory")
typedef __attribute__((address_space(3))) char lds_char;
#define LDS_RD128(addr) (*(const __attribute__((address_space(3))) bf16x8*)(addr))
#define AMFMA(acc, a, b) asm volatile("v_mfma_f32_16x16x32_bf16 %0, %1, %2, %0" : "+v"(acc) : "v"(a), "v"(b))
#define ADSRD(dst, addr, off) asm volatile("ds_read_b128 %0, %1 offset:%2" : "=v"(dst) : "v"(addr), "i"(off))
#define OPAQUE(x) asm volatile("" : "+v"(x))
#define BLOAD16(dst, voff, srd, soff, imm) asm volatile("buffer_load_dwordx4 %0, %1, %2, %3 offen offset:" #imm : "=v"(dst) : "v"(voff), "s"(srd), "s"(soff))

constexpr int K0_PITCH = 1168;
__device__ __forceinline__ int wc_krow(int ks, int g, int j) {
  int r = j & 3, hi = j >> 2;
  if (ks >= 72) { int u = ks - 72; return 2304 + 4 * (8 * u + 4 * hi + g) + r; }
  int w = ks / 18, pi = (ks % 18) / 9, i = ks % 9, fb0 = 4 * w + 2 * pi;
  if (i == 4) return 144 * (fb0 + hi) + 128 + 4 * g + r;
  if (i < 4) return 144 * fb0 + 32 * i + 16 * hi + 4 * g + r;
  return 144 * (fb0 + 1) + 32 * (i - 5) + 16 * hi + 4 * g + r;
}
__device__ __forceinline__ u32x4 tr_frag(unsigned a0, unsigned a1) {
  u32x2 lo, hi;
  asm volatile("ds_read_b64_tr_b16 %0, %2\n\tds_read_b64_tr_b16 %1, %3\n\ts_waitcnt lgkmcnt(0)" : "=&v"(lo), "=&v"(hi) : "v"(a0), "v"(a1) : "memory");
  return (u32x4){lo[0], lo[1], hi[0], hi[1]};
}
#define KA_SLEEP 32
constexpr int KA_SLOT = 10240, KA_ATT = 6 * KA_SLOT, KA_PITCH = 144, KA_LDS = KA_ATT + 142 * KA_PITCH;
template <int FAST> struct KaCfg {
  static constexpr int NZ2 = 4 + FAST, NZ1 = 4 + FAST;
  static constexpr int nt(int s) { return s < 9 ? 4 : 4 + FAST; }
  static constexpr int goff(int s) { return s < 9 ? s * 8192 : 73728 + (s - 9) * (4 + FAST) * 2048; }
  static constexpr int ops(int s) { return s > 26 ? 0 : 2 + (nt(s) == 5 ? 1 : 0) + (s < 9 ? 4 + ((s & 1) ? 0 : 1) : 0); }
  static constexpr int wait(int s) { return s == 20 ? ops(23) + ops(24) : ops(s + 2) + ops(s + 3) + ops(s + 4) + ((s >= 9 && s <= 12) ? 6 : 0); }
};
template <int V> struct IC { static constexpr int v = V; };
#define BLD(dst, voff, srd, soff, IMM) asm volatile("buffer_load_dwordx4 %0, %1, %2, %3 offen offset:%4" : "=v"(dst) : "v"(voff), "s"(srd), "s"(soff), "i"(IMM))
#define AMFMA0(acc, a, b) asm volatile("v_mfma_f32_16x16x32_bf16 %0, %1, %2, 0" : "=&v"(acc) : "v"(a), "v"(b))
typedef __attribute__((ext_vector_type(2))) float f32x2;
typedef __attribute__((ext_vector_type(2))) __bf16 bf2_t;
__device__ __forceinline__ float dot2(unsigned a, unsigned b, float c) { return __builtin_amdgcn_fdot2_f32_bf16(__builtin_bit_cast(bf2_t, a), __builtin_bit_cast(bf2_t, b), c, false); }
__device__ __forceinline__ f32x2 gelu2(f32x2 x) {
  f32x2 t = x * x, p = t * (-0.10294324f) + (-2.3022082f), e = x * p;
  f32x2 d = {__builtin_amdgcn_exp2f(e[0]) + 1.f, __builtin_amdgcn_exp2f(e[1]) + 1.f};
  f32x2 r = {__builtin_amdgcn_rcpf(d[0]), __builtin_amdgcn_rcpf(d[1])};
  return x * r;
}
template <int CTRL> __device__ __forceinline__ float dpp_add(float v) {
  return v + __builtin_bit_cast(float, __builtin_amdgcn_update_dpp(0, __builtin_bit_cast(int, v), CTRL, 0xf, 0xf, true));
}

__device__ __forceinline__ float xmax16(float v) { auto r = __builtin_amdgcn_permlane16_swap(__float_as_uint(v), __float_as_uint(v), false, false); return fmaxf(__uint_as_float(r[0]), __uint_as_float(r[1])); }
__device__ __forceinline__ float xmax32(float v) { auto r = __builtin_amdgcn_permlane32_swap(__float_as_uint(v), __float_as_uint(v), false, false); return fmaxf(__uint_as_float(r[0]), __uint_as_float(r[1])); }
__device__ __forceinline__ float xadd16(float v) { auto r = __builtin_amdgcn_permlane16_swap(__float_as_uint(v), __float_as_uint(v), false, false); return __uint_as_float(r[0]) + __uint_as_float(r[1]); }
__device__ __forceinline__ float xadd32(float v) { auto r = __builtin_amdgcn_permlane32_swap(__float_as_uint(v), __float_as_uint(v), false, false); return __uint_as_float(r[0]) + __uint_as_float(r[1]); }
#define PRIO_HI()
#define PRIO_LO()
#define LBAR() asm volatile("s_waitcnt lgkmcnt(0)\n\ts_barrier" ::: "memory")
__device__ __forceinline__ float swap1(float v) { return __builtin_bit_cast(float, __builtin_amdgcn_update_dpp(0, __builtin_bit_cast(int, v), 0xB1, 0xf, 0xf, true)); }
template <int FAST>
__device__ __forceinline__ void ka_body(char* smem, const char* __restrict__ ws_c, char* __restrict__ ws, const int p, const int cg, const int sj, const u16* __restrict__ wsp, const u16* __restrict__ wout, const u16* __restrict__ wo) {
  typedef KaCfg<FAST> C;
  const int tid = threadIdx.x, lane = tid & 63, li = lane & 15, lg = lane >> 4;
  const int w = RFL(tid >> 6), fb = cg >> 1;
  const rsrc_t rW = mk_rsrc(ws_c + OFF_W1 + (size_t)fb * (468 * 1024) + (FAST ? 0 : 252 * 1024), (FAST ? 252 : 216) * 1024);
  const rsrc_t rX = mk_rsrc(ws_c + OFF_XT, (int)SZ_XT);
  const unsigned ldsb = (unsigned)(size_t)(__attribute__((address_space(3))) char*)smem;
  const unsigned vl16 = lane * 16;
  const unsigned vX = vl16 + (unsigned)(p * 8 + w * 2) * 18432u;
  const unsigned vH = p == 0 ? 0x80000000u : (unsigned)(p * 8 - 2 + (li >> 3)) * 18432u + (unsigned)(lg * 16 + (li | 8)) * 16u + (unsigned)w * 1024u;
  const unsigned sF = RFL((unsigned)w * 2048u), sH = RFL(8192u + (unsigned)w * 512u);
  const unsigned rd = ldsb + vl16;
  constexpr int KA_WARM = 0;
  bf16x8 X[2][18], HX[5], F[5];
  f32x4 acc[2][5], hacc[3];
#pragma unroll
  for (int n = 0; n < 3; ++n) hacc[n] = (f32x4){0.f, 0.f, 0.f, 0.f};

  bf16x8 (&X_)[2][18] = X; bf16x8 (&HX_)[5] = HX; bf16x8 (&F_)[5] = F; f32x4 (&acc_)[2][5] = acc; f32x4 (&hacc_)[3] = hacc;
  const rsrc_t rW_ = rW, rX_ = rX; const unsigned vl16_ = vl16, vX_ = vX, vH_ = vH, rd_ = rd;
#define KA_CAP bf16x8 (&X)[2][18] = X_; bf16x8 (&HX)[5] = HX_; bf16x8 (&F)[5] = F_; f32x4 (&acc)[2][5] = acc_; f32x4 (&hacc)[3] = hacc_; \
    const rsrc_t rW = rW_, rX = rX_; const unsigned vl16 = vl16_, vX = vX_, vH = vH_, rd = rd_; (void)X; (void)HX; (void)F; (void)acc; (void)hacc; (void)rW; (void)rX; (void)vl16; (void)vX; (void)vH; (void)rd;
  auto vop = [&](auto S_, auto K_) {
    KA_CAP
    constexpr int s = decltype(S_)::v, k = decltype(K_)::v;
    if constexpr (s <= 26) {
      constexpr int slot = s % 6, nd = 2 + (C::nt(s) == 5 ? 1 : 0);
      if constexpr (k == 0) { unsigned lo = ldsb + slot * KA_SLOT + sF, so = C::goff(s) + sF; DMA16(lo, vl16, rW, so, 0); }
      else if constexpr (k == 1) { unsigned lo = ldsb + slot * KA_SLOT + sF, so = C::goff(s) + sF; DMA16(lo, vl16, rW, so, 1024); }
      else if constexpr (k == 2 && nd == 3) { unsigned lo = ldsb + slot * KA_SLOT + sH, so = C::goff(s) + sH; DMA16H(lo, vl16, rW, so); }
      else if constexpr (s < 9 && k >= 2 && k < 6) {
        constexpr int m = (k - 2) & 1, ks = 2 * s + ((k - 2) >> 1);
        BLD(X[m][ks], vX, rX, m * 18432 + (ks & ~3) * 1024, (ks & 3) * 1024);
      } else if constexpr (s < 9 && k == 6 && !(s & 1)) { BLD(HX[s >> 1], vH, rX, (s >> 1) * 4096, 0); }
    }
  };
  auto group = [&](auto S_) { vop(S_, IC<0>{}); vop(S_, IC<1>{}); vop(S_, IC<2>{}); vop(S_, IC<3>{}); vop(S_, IC<4>{}); vop(S_, IC<5>{}); vop(S_, IC<6>{}); };
  auto sync = [&](auto S_) {
    KA_CAP
    constexpr int s = decltype(S_)::v;
    if constexpr (s < 26) {
      if constexpr (s + 1 < 9) {
        constexpr int k0 = 2 * (s + 1);
        asm volatile("s_waitcnt vmcnt(%5)" : "+v"(X[0][k0]), "+v"(X[1][k0]), "+v"(X[0][k0 + 1]), "+v"(X[1][k0 + 1]), "+v"(HX[(s + 1) >> 1]) : "i"(C::wait(s)) : "memory");
      } else asm volatile("s_waitcnt vmcnt(%0)" :: "i"(C::wait(s)) : "memory");
      __builtin_amdgcn_s_barrier();
    }
  };
  auto step = [&](auto S_, auto H_) {
    KA_CAP
    constexpr int s = decltype(S_)::v, h = decltype(H_)::v;
    constexpr int NT = C::nt(s), job = s / 9, ks = 2 * (s % 9) + h;
    constexpr int s2 = h ? s + 1 : s, h2 = h ^ 1, NT2 = s2 <= 26 ? C::nt(s2) : 0;
    constexpr int roff = (s2 % 6) * KA_SLOT + h2 * NT2 * 1024;
    if constexpr (ks == 0) asm volatile("s_waitcnt lgkmcnt(0)" ::: "memory");
    if constexpr (job == 0) {
      if (w == (ks & 3)) {
        asm volatile("s_waitcnt lgkmcnt(1)" ::: "memory");
#pragma unroll
        for (int n = 0; n < 3; ++n) AMFMA(hacc[n], F[n], HX[ks >> 2]);
      }
    }
#pragma unroll
    for (int n = 0; n < NT; ++n) {
      if constexpr (ks != 0) asm volatile("s_waitcnt lgkmcnt(%0)" :: "i"(NT - 1) : "memory");
      if constexpr (job == 1) {
        if constexpr (ks == 0) { AMFMA0(acc[0][n], X[0][ks], F[n]); AMFMA0(acc[1][n], X[1][ks], F[n]); }
        else { AMFMA(acc[0][n], X[0][ks], F[n]); AMFMA(acc[1][n], X[1][ks], F[n]); }
      } else {
        if constexpr (ks == 0) { AMFMA0(acc[0][n], F[n], X[0][ks]); AMFMA0(acc[1][n], F[n], X[1][ks]); }
        else { AMFMA(acc[0][n], F[n], X[0][ks]); AMFMA(acc[1][n], F[n], X[1][ks]); }
      }
      if (n < NT2) ADSRD(F[n], rd, roff + n * 1024);
      if constexpr (h == 1) {
        if (n == 0) { vop(IC<s + 5>{}, IC<0>{}); vop(IC<s + 5>{}, IC<2>{}); }
        if (n == 1) { vop(IC<s + 5>{}, IC<1>{}); vop(IC<s + 5>{}, IC<3>{}); }
        if (n == 2) { vop(IC<s + 5>{}, IC<4>{}); vop(IC<s + 5>{}, IC<5>{}); }
        if (n == 3) { vop(IC<s + 5>{}, IC<6>{}); }
      }
    }
    if constexpr (NT2 > NT) ADSRD(F[4], rd, roff + 4 * 1024);
  };
#define KA_ACC_DONE() do { asm volatile("s_nop 15" : "+v"(acc[0][0]), "+v"(acc[0][1]), "+v"(acc[0][2]), "+v"(acc[0][3]), "+v"(acc[1][0]), "+v"(acc[1][1]), "+v"(acc[1][2]), "+v"(acc[1][3]) :: "memory"); \
    if constexpr (FAST) asm volatile("" : "+v"(acc[0][4]), "+v"(acc[1][4]) :: "memory"); } while (0)
  auto iter = [&](auto S_) { step(S_, IC<0>{}); sync(S_); step(S_, IC<1>{}); };

  group(IC<0>{}); group(IC<1>{});
  asm volatile("s_waitcnt vmcnt(%5)" : "+v"(X[0][0]), "+v"(X[1][0]), "+v"(X[0][1]), "+v"(X[1][1]), "+v"(HX[0]) : "i"(C::ops(1) + KA_WARM) : "memory");
  __builtin_amdgcn_s_barrier();
#pragma unroll
  for (int n = 0; n < 4; ++n) ADSRD(F[n], rd, n * 1024);

  step(IC<0>{}, IC<0>{}); group(IC<2>{}); group(IC<3>{}); group(IC<4>{}); sync(IC<0>{}); step(IC<0>{}, IC<1>{});
  iter(IC<1>{}); iter(IC<2>{}); iter(IC<3>{}); iter(IC<4>{}); iter(IC<5>{}); iter(IC<6>{}); iter(IC<7>{}); iter(IC<8>{});
  asm volatile("s_nop 15" : "+v"(acc[0][0]), "+v"(acc[0][1]), "+v"(acc[0][2]), "+v"(acc[0][3]), "+v"(acc[1][0]), "+v"(acc[1][1]), "+v"(acc[1][2]), "+v"(acc[1][3]), "+v"(hacc[0]), "+v"(hacc[1]), "+v"(hacc[2]) :: "memory");

  SCHED();
  {
    char* att = smem + KA_ATT;
    char* hp = smem + 2 * KA_SLOT;
#pragma unroll
    for (int n = 0; n < 3; ++n) if (n != w) *(f32x4*)(hp + (n * 3 + (w > n ? w - 1 : w)) * 1024 + vl16) = hacc[n];
#pragma unroll
    for (int m = 0; m < 2; ++m) {
      char* rp = att + (14 + w * 32 + 8 * (li >> 2) + 4 * m + (li & 3)) * KA_PITCH + lg * 8;
#pragma unroll
      for (int n = 0; n < 4; ++n) *(u32x2*)(rp + n * 32) = (u32x2){pk_bf16(acc[m][n][0], acc[m][n][1]), pk_bf16(acc[m][n][2], acc[m][n][3])};
    }
    LBAR();
    if (w < 3) {
      f32x4 a = w == 0 ? hacc[0] : (w == 1 ? hacc[1] : hacc[2]);
#pragma unroll
      for (int q = 0; q < 3; ++q) a += *(const f32x4*)(hp + (w * 3 + q) * 1024 + vl16);
      int ho = (li < 8) ? 8 * (li >> 2) + (li & 3) : 8 * ((li - 8) >> 2) + 4 + (li & 3);
      if (ho >= 2) *(u32x2*)(att + (ho - 2) * KA_PITCH + w * 32 + lg * 8) = (u32x2){pk_bf16(a[0], a[1]), pk_bf16(a[2], a[3])};
    }
    LBAR();
    const unsigned attb = ldsb + KA_ATT;
    float mbias[8];
#pragma unroll
    for (int e = 0; e < 8; ++e) { int d = (e >> 2) * 16 + lg * 4 + (e & 3) - li; mbias[e] = (!(d & 1) && d >= 2 && d <= 16) ? 0.f : -__builtin_inff(); }
    bf16x8 Q[2], K[2][2]; u32x2 V[2][2][2];
#pragma unroll
    for (int jj = 0; jj < 2; ++jj) {
      const int j = w * 2 + jj;
      Q[jj] = *(const bf16x8*)(att + (14 + 16 * j + li) * KA_PITCH + 80 + (lg < 3 ? lg : 2) * 16);
#pragma unroll
      for (int kt = 0; kt < 2; ++kt) {
        int R = 16 * j - 2 + 16 * kt + li; R = R < 0 ? 0 : R;
        K[jj][kt] = *(const bf16x8*)(att + R * KA_PITCH + lg * 16);
        int Rv = 16 * j - 2 + 16 * kt + 4 * lg + (li >> 2); Rv = Rv < 0 ? 0 : Rv;
        const unsigned va = attb + Rv * KA_PITCH + 40 + (li & 3) * 8;
        asm volatile("ds_read_b64_tr_b16 %0, %2\n\tds_read_b64_tr_b16 %1, %2 offset:32" : "=&v"(V[jj][0][kt]), "=&v"(V[jj][1][kt]) : "v"(va) : "memory");
      }
    }
    f32x4 S[2][2];
#pragma unroll
    for (int jj = 0; jj < 2; ++jj) {
      if (lg == 3) Q[jj] = (bf16x8){0, 0, 0, 0, 0, 0, 0, 0};
#pragma unroll
      for (int kt = 0; kt < 2; ++kt) S[jj][kt] = MFMA(K[jj][kt], Q[jj], ((f32x4){0.f, 0.f, 0.f, 0.f}));
    }
    bf16x8 P[2];
#pragma unroll
    for (int jj = 0; jj < 2; ++jj) {
      float l[8];
#pragma unroll
      for (int e = 0; e < 8; ++e) l[e] = S[jj][e >> 2][e & 3] + mbias[e];
      if (p == 0 && w == 0 && jj == 0) {
#pragma unroll
        for (int e = 0; e < 4; ++e) l[e] = -__builtin_inff();
      }
      float mx = fmaxf(fmaxf(fmaxf(l[0], l[1]), fmaxf(l[2], l[3])), fmaxf(fmaxf(l[4], l[5]), fmaxf(l[6], l[7])));
      mx = xmax16(mx); mx = xmax32(mx);
      constexpr float CE = 0.23570226f * 1.44269504f;
      const float nm = -mx * CE;
      float sum = 0.f;
#pragma unroll
      for (int e = 0; e < 8; ++e) { l[e] = __builtin_amdgcn_exp2f(l[e] * CE + nm); sum += l[e]; }
      sum = xadd16(sum); sum = xadd32(sum);
      const float inv = __builtin_amdgcn_rcpf(sum);
      u32x4 pk = {pk_bf16(l[0] * inv, l[1] * inv), pk_bf16(l[2] * inv, l[3] * inv), pk_bf16(l[4] * inv, l[5] * inv), pk_bf16(l[6] * inv, l[7] * inv)};
      P[jj] = __builtin_bit_cast(bf16x8, pk);
    }
    asm volatile("s_waitcnt lgkmcnt(0)" : "+v"(V[0][0][0]), "+v"(V[0][0][1]), "+v"(V[0][1][0]), "+v"(V[0][1][1]), "+v"(V[1][0][0]), "+v"(V[1][0][1]), "+v"(V[1][1][0]), "+v"(V[1][1][1]) :: "memory");
#pragma unroll
    for (int jj = 0; jj < 2; ++jj) {
      f32x4 O[2];
#pragma unroll
      for (int dt = 0; dt < 2; ++dt) {
        u32x4 vv = {V[jj][dt][0][0], V[jj][dt][0][1], V[jj][dt][1][0], V[jj][dt][1][1]};
        O[dt] = MFMA(__builtin_bit_cast(bf16x8, vv), P[jj], ((f32x4){0.f, 0.f, 0.f, 0.f}));
      }
      const int tl = jj * 16 + li, tt = p * 8 + w * 2 + ((tl >> 2) & 1), slot = (tl >> 3) * 4 + (tl & 3);
      char* ob = ws + OFF_OT + (size_t)tt * 18432 + slot * 8;
      const int d0 = cg * 18 + lg * 4, d1 = d0 + 2, d2 = cg * 18 + 16;
      *(unsigned*)(ob + (d0 >> 2) * 128 + (d0 & 3) * 2) = pk_bf16(O[0][0], O[0][1]);
      *(unsigned*)(ob + (d1 >> 2) * 128 + (d1 & 3) * 2) = pk_bf16(O[0][2], O[0][3]);
      if (lg == 0) *(unsigned*)(ob + (d2 >> 2) * 128 + (d2 & 3) * 2) = pk_bf16(O[1][0], O[1][1]);
    }
  }
  SCHED();
  PRIO_LO();

  iter(IC<9>{}); iter(IC<10>{}); iter(IC<11>{}); iter(IC<12>{}); iter(IC<13>{}); iter(IC<14>{}); iter(IC<15>{}); iter(IC<16>{}); iter(IC<17>{});
  KA_ACC_DONE();

  SCHED();
  if constexpr (FAST) {
    const unsigned dump = RFL(ldsb + KA_ATT + 2048 + (unsigned)w * 256u);
    {
      const rsrc_t rS = mk_rsrc(wsp, 2048 * 4096);
      const int ks = (sj * 4 + w) & 63, tp = (sj * 4 + w) >> 6, r = (w & 1) * 64 + lane;
      const unsigned vo = (unsigned)prow(tp * 8 + (r >> 4), r & 15) * 4096u + (unsigned)(ks >> 1) * 128u;
      asm volatile("s_mov_b32 m0, %0\n\ts_nop 0\n\tbuffer_load_dword %1, %2, 0 offen lds" :: "s"(dump), "v"(vo), "s"(rS) : "memory");
    }
    if (sj < 90) {
      const int base0 = wc_krow(sj, 0, 0), base1 = wc_krow(sj, 0, 4);
      const rsrc_t rS = sj < 72 ? mk_rsrc(wout, 2304 * 1152) : mk_rsrc(wo - (size_t)2304 * 576, 2880 * 1152);
      const int c = tid, r = c / 9, l = c - r * 9;
      const unsigned vo = (unsigned)((r < 16 ? base0 : base1 - 16) + r) * 1152u + (unsigned)l * 128u;
      asm volatile("s_mov_b32 m0, %0\n\ts_nop 0\n\tbuffer_load_dword %1, %2, 0 offen lds" :: "s"(dump), "v"(vo), "s"(rS) : "memory");
      if (w == 0) {
        const int c2 = 256 + (lane & 31), r2 = c2 / 9, l2 = c2 - r2 * 9;
        const unsigned vo2 = (unsigned)(base1 - 16 + r2) * 1152u + (unsigned)l2 * 128u;
        asm volatile("s_mov_b32 m0, %0\n\ts_nop 0\n\tbuffer_load_dword %1, %2, 0 offen lds" :: "s"(dump), "v"(vo2), "s"(rS) : "memory");
      }
    }
  }
  SCHED();
  PRIO_HI();
  unsigned zs[C::NZ2][4];
  {
#pragma unroll
    for (int n = 0; n < C::NZ2; ++n) {
#pragma unroll
      for (int m = 0; m < 2; ++m) {
        f32x2 a = gelu2((f32x2){acc[m][n][0], acc[m][n][1]}), b = gelu2((f32x2){acc[m][n][2], acc[m][n][3]});
        zs[n][2 * m] = pk_bf16(a[0], a[1]); zs[n][2 * m + 1] = pk_bf16(b[0], b[1]);
      }
    }
    f32x2 s1[4], s2[4];
#pragma unroll
    for (int j = 0; j < 4; ++j) {
      s1[j] = (f32x2){0.f, 0.f}; s2[j] = s1[j];
#pragma unroll
      for (int n = 0; n < C::NZ2; ++n) { f32x2 v = {bf_lo(zs[n][j]), bf_hi(zs[n][j])}; s1[j] += v; s2[j] += v * v; }
    }
    float r[16];
#pragma unroll
    for (int j = 0; j < 4; ++j) { r[2 * j] = s1[j][0]; r[2 * j + 1] = s1[j][1]; r[8 + 2 * j] = s2[j][0]; r[9 + 2 * j] = s2[j][1]; }
#pragma unroll
    for (int j = 0; j < 16; ++j) { float v = r[j]; v = dpp_add<0x128>(v); v = dpp_add<0x124>(v); v = dpp_add<0x122>(v); v = dpp_add<0x121>(v); r[j] = v; }
    float sel = r[0];
#pragma unroll
    for (int j = 1; j < 16; ++j) sel = (li == j) ? r[j] : sel;
    double* dst = (double*)(ws + OFF_RS) + (size_t)(p * 128 + w * 32 + lg * 8 + (li & 7)) * 2 + (li >> 3);
    double dv = (double)sel;
    asm volatile("global_atomic_add_f64 %0, %1, off sc1" :: "v"(dst), "v"(dv) : "memory");
  }
  SCHED();

  PRIO_LO();

  unsigned* cnt = (unsigned*)(ws + OFF_CNT + p * 256);
  iter(IC<18>{}); iter(IC<19>{});
  step(IC<20>{}, IC<0>{}); sync(IC<20>{});
  if (tid == 0) { unsigned one = 1; asm volatile("global_atomic_add %0, %1, off sc1" :: "v"(cnt), "v"(one) : "memory"); }
  step(IC<20>{}, IC<1>{});
  iter(IC<21>{}); iter(IC<22>{}); iter(IC<23>{}); iter(IC<24>{}); iter(IC<25>{});
  unsigned seen;
  asm volatile("global_load_dword %0, %1, off sc1" : "=v"(seen) : "v"(cnt) : "memory");
  iter(IC<26>{});
  KA_ACC_DONE();
  asm volatile("s_waitcnt vmcnt(0)" : "+v"(seen) :: "memory");
  PRIO_HI();
  const unsigned seen_s = RFL(seen);
  const bool early = seen_s >= 32u;
  const char* sp = ws + OFF_RS + (size_t)(p * 128 + w * 32 + (lane & 31)) * 16;
  u32x4 st = {0, 0, 0, 0};
  asm volatile("s_cmp_lt_u32 %2, 32\n\ts_cbranch_scc1 1f\n\tglobal_load_dwordx4 %0, %1, off sc1\n1:" : "+v"(st) : "v"(sp), "s"(seen_s) : "memory", "scc");
  SCHED();
  u32x4 wt[8];
  if constexpr (FAST) {
    const int f0 = (sj * 4 + w) * 8, ks = (f0 >> 3) & 63, tp = f0 >> 9;
#pragma unroll
    for (int m = 0; m < 8; ++m) wt[m] = *(const u32x4*)(wsp + (size_t)prow(tp * 8 + m, li) * 2048 + ks * 32 + lg * 8);
  }
#pragma unroll
  for (int m = 0; m < 2; ++m) {
    char* zp = ws + OFF_Z1T + ((size_t)(p * 8 + w * 2 + m) * 16 + fb) * 4608;
    unsigned pk[C::NZ1][2];
#pragma unroll
    for (int n = 0; n < C::NZ1; ++n) {
      f32x2 a = gelu2((f32x2){acc[m][n][0], acc[m][n][1]}), b = gelu2((f32x2){acc[m][n][2], acc[m][n][3]});
      pk[n][0] = pk_bf16(a[0], a[1]); pk[n][1] = pk_bf16(b[0], b[1]);
    }
#pragma unroll
    for (int q = 0; q < 2; ++q) st16(zp + (FAST * 2 + q) * 1024 + vl16, (u32x4){pk[2 * q][0], pk[2 * q][1], pk[2 * q + 1][0], pk[2 * q + 1][1]});
    if constexpr (FAST) *(u32x2*)(zp + 4096 + lane * 8) = (u32x2){pk[4][0], pk[4][1]};
  }
  SCHED();
  if constexpr (FAST) {
    const int f0 = (sj * 4 + w) * 8;
#pragma unroll
    for (int m = 0; m < 8; ++m) st16(ws + OFF_WT + (size_t)(f0 + m) * 1024 + vl16, wt[m]);
    if (sj < 90) {
      const int ks = sj, base0 = wc_krow(ks, 0, 0), base1 = wc_krow(ks, 0, 4);
      u32x4 v[9];
#pragma unroll
      for (int k = 0; k < 9; ++k) {
        int c = tid + k * 256, r = c / 72, ch = c - r * 72, row = (r < 16 ? base0 : base1 - 16) + r;
        const u16* src = row < 2304 ? wout + (size_t)row * 576 : wo + (size_t)(row - 2304) * 576;
        v[k] = *(const u32x4*)(src + ch * 8);
      }
      LBAR();
#pragma unroll
      for (int k = 0; k < 9; ++k) { int c = tid + k * 256, r = c / 72, ch = c - r * 72; *(u32x4*)(smem + r * K0_PITCH + ch * 16) = v[k]; }
      LBAR();
      const unsigned a0 = ldsb + (4 * lg + (li >> 2)) * K0_PITCH + (li & 3) * 8;
#pragma unroll
      for (int e = 0; e < 10; ++e) {
        int f = w * 10 + e, ct = f / 5, n = f - ct * 5, d0 = (ct < 7 ? ct * 72 : 496) + n * 16;
        u32x4 o = tr_frag(a0 + d0 * 2, a0 + d0 * 2 + 16 * K0_PITCH);
        st16(ws + OFF_WC + (size_t)((ct * 90 + ks) * 5 + n) * 1024 + vl16, o);
      }
    }
  }
  if (!early) {
    while (RFL(seen) < 32u) {
      __builtin_amdgcn_s_sleep(KA_SLEEP);
      asm volatile("global_load_dword %0, %1, off sc1\n\ts_waitcnt vmcnt(0)" : "=v"(seen) : "v"(cnt) : "memory");
    }
  }
  asm volatile("s_cmp_ge_u32 %2, 32\n\ts_cbranch_scc1 1f\n\tglobal_load_dwordx4 %0, %1, off sc1\n1:\n\ts_waitcnt vmcnt(0)" : "+v"(st) : "v"(sp), "s"(seen_s) : "memory", "scc");
  {
    const double sm = __builtin_bit_cast(double, (u32x2){st[0], st[1]}), sq = __builtin_bit_cast(double, (u32x2){st[2], st[3]});
    const double mean = sm * (1.0 / 2304.0);
    const float var = fmaxf((float)(sq * (1.0 / 2304.0) - mean * mean), 0.f);
    const float mb = rbf((float)mean), rs = 1.f / rbf(sqrtf(rbf(rbf(var) + 1e-5f)));
    f32x2* sl = (f32x2*)(smem + KA_ATT + w * 256);
    if (lane < 32) sl[lane] = (f32x2){mb, rs};
    __builtin_amdgcn_s_waitcnt(0xC07F);
    __builtin_amdgcn_wave_barrier();
    f32x4 ms[4];
#pragma unroll
    for (int j = 0; j < 4; ++j) ms[j] = *(const f32x4*)((const char*)sl + lg * 64 + j * 16);
    char* zp = ws + OFF_Z2T + ((size_t)(fb * 64 + p * 4 + w) * 9 + (FAST ? 0 : 5)) * 1024 + vl16;
#pragma unroll
    for (int n = 0; n < C::NZ2; ++n) {
      u32x4 o;
#pragma unroll
      for (int j = 0; j < 4; ++j) o[j] = pk_bf16((bf_lo(zs[n][j]) - ms[j][0]) * ms[j][1], (bf_hi(zs[n][j]) - ms[j][2]) * ms[j][3]);
      st16(zp + n * 1024, o);
    }
  }
}
__global__ __launch_bounds__(256, 2) void kA(const char* __restrict__ ws_c, char* __restrict__ ws, const u16* __restrict__ wsp, const u16* __restrict__ wout, const u16* __restrict__ wo) {
  extern __shared__ __attribute__((aligned(16))) char smem[];
  const int b = blockIdx.x, xcd = b & 7, idx = b >> 3;
  const int p = (xcd & 1) * 8 + (idx & 7), q = idx >> 3, cg = (xcd >> 1) * 8 + (q & 3) * 2 + (q >> 2);
  if (idx < 32) ka_body<1>(smem, ws_c, ws, p, cg, b, wsp, wout, wo); else ka_body<0>(smem, ws_c, ws, p, cg, b, wsp, wout, wo);
}

constexpr int KB_STAGE = 36864;
constexpr int KB_LDS = 4 * KB_STAGE;
__global__ __launch_bounds__(512, 2) void kB(const char* __restrict__ ws_c, char* __restrict__ ws, const u16* __restrict__ bsp) {
  extern __shared__ __attribute__((aligned(16))) char smem[];
  const int tid = threadIdx.x, lane = tid & 63;
  const int w = RFL(tid >> 6), wm = w & 3, kh = w >> 2;
  const int b = blockIdx.x, xcd = b & 7, idx = b >> 3;
  const int tp = (xcd & 1) * 8 + (idx & 7), fb = (xcd >> 1) * 4 + (idx >> 3);
  const rsrc_t rZ = mk_rsrc(ws_c + OFF_Z2T + (size_t)fb * (64 * 9216), 64 * 9216);
  const rsrc_t rW = mk_rsrc(ws_c + OFF_WT + (size_t)tp * (64 * 8192), 64 * 8192);
  const unsigned ldsb = (unsigned)(size_t)(__attribute__((address_space(3))) char*)smem;
  const unsigned vl16 = lane * 16;
  const unsigned vW = vl16 + wm * 2048 + kh * 16384;
  const unsigned dmaF = w * 4096, dmaH = 32768 + w * 512;
  unsigned rd01 = ldsb + kh * 18432 + lane * 16;
  unsigned rd23 = rd01 + 2 * KB_STAGE;

  f32x4 acc[2][9];
#pragma unroll
  for (int m = 0; m < 2; ++m)
#pragma unroll
    for (int n = 0; n < 9; ++n) acc[m][n] = (f32x4){0.f, 0.f, 0.f, 0.f};
  bf16x8 Wr[4][4];
  bf16x8 ZA[9], ZB[9];

#define KB_DMA1(stage, slot, imm) do { \
    unsigned so_ = RFL((unsigned)(stage) * KB_STAGE + dmaF), lo_ = RFL(ldsb + (slot) * KB_STAGE + dmaF); DMA16(lo_, vl16, rZ, so_, imm); } while (0)
#define KB_DMAH(stage, slot) do { \
    unsigned so2_ = RFL((unsigned)(stage) * KB_STAGE + dmaH), lo2_ = RFL(ldsb + (slot) * KB_STAGE + dmaH); DMA16H(lo2_, vl16, rZ, so2_); } while (0)
#define KB_W1(stage, slot, e) do { unsigned sw_ = RFL((unsigned)(stage) * 32768u + ((e) >> 1) * 8192u); \
    if ((e) & 1) BLOAD16(Wr[slot][e], vW, rW, sw_, 1024); else BLOAD16(Wr[slot][e], vW, rW, sw_, 0); } while (0)
#define KB_ALL(stage, slot) do { KB_DMA1(stage, slot, 0); KB_DMA1(stage, slot, 1024); KB_DMA1(stage, slot, 2048); KB_DMA1(stage, slot, 3072); KB_DMAH(stage, slot); \
    KB_W1(stage, slot, 0); KB_W1(stage, slot, 1); KB_W1(stage, slot, 2); KB_W1(stage, slot, 3); } while (0)
#define KB_WAIT(slot, N) asm volatile("s_waitcnt vmcnt(" #N ") lgkmcnt(0)" : "+v"(Wr[slot][0]), "+v"(Wr[slot][1]), "+v"(Wr[slot][2]), "+v"(Wr[slot][3]) :: "memory")

  KB_ALL(0, 0);
  __builtin_amdgcn_s_barrier();
  KB_ALL(1, 1);
  KB_WAIT(0, 9);
  __builtin_amdgcn_s_barrier();

  const rsrc_t rZ1 = mk_rsrc(ws_c + OFF_Z1T + ((size_t)(tp * 8 + wm * 2) * 16 + fb) * 4608, 17 * 4608);
  const rsrc_t rB = mk_rsrc(bsp, 4096);
  u32x4 z1q[2][2]; u32x2 z1s[2]; unsigned biasu[2];
  const unsigned vz1 = lane * 16 + kh * 2048, vz8 = lane * 8 + 4096;
  const unsigned vb = (tp * 128 + wm * 32 + ((lane & 15) >> 2) * 8 + (lane & 3)) * 2;
#define KB_EPI_LD(e) do { \
    if ((e) == 0) BLOAD16(z1q[0][0], vz1, rZ1, 0, 0); \
    if ((e) == 1) BLOAD16(z1q[0][1], vz1, rZ1, 0, 1024); \
    if ((e) == 2) asm volatile("buffer_load_dwordx4 %0, %1, %2, %3 offen" : "=v"(z1q[1][0]) : "v"(vz1), "s"(rZ1), "s"(16 * 4608)); \
    if ((e) == 3) asm volatile("buffer_load_dwordx4 %0, %1, %2, %3 offen offset:1024" : "=v"(z1q[1][1]) : "v"(vz1), "s"(rZ1), "s"(16 * 4608)); \
    if ((e) == 4) asm volatile("buffer_load_dwordx2 %0, %1, %2, 0 offen" : "=v"(z1s[0]) : "v"(vz8), "s"(rZ1)); \
    if ((e) == 5) asm volatile("buffer_load_dwordx2 %0, %1, %2, %3 offen" : "=v"(z1s[1]) : "v"(vz8), "s"(rZ1), "s"(16 * 4608)); \
    if ((e) == 6) asm volatile("buffer_load_ushort %0, %1, %2, 0 offen" : "=v"(biasu[0]) : "v"(vb), "s"(rB)); \
    if ((e) == 7) asm volatile("buffer_load_ushort %0, %1, %2, 0 offen offset:8" : "=v"(biasu[1]) : "v"(vb), "s"(rB)); } while (0)

#define KB_ITER(i, j, MODE, WN) do { \
    _Pragma("unroll") for (int n = 0; n < 9; ++n) { \
      ADSRD(ZA[n], ((j) < 2 ? rd01 : rd23), ((j) & 1) * KB_STAGE + n * 1024); \
      if ((MODE) != 1) { AMFMA(acc[0][n], ZB[n], Wr[((j) + 3) & 3][2]); AMFMA(acc[1][n], ZB[n], Wr[((j) + 3) & 3][3]); } \
      if ((MODE) == 0) { \
        if (n == 0) KB_DMA1((i) + 3, ((j) + 3) & 3, 0); \
        if (n == 2) KB_DMA1((i) + 3, ((j) + 3) & 3, 1024); \
        if (n == 4) KB_W1((i) + 3, ((j) + 3) & 3, 0); \
        if (n == 6) KB_DMA1((i) + 3, ((j) + 3) & 3, 2048); \
        if (n == 8) KB_W1((i) + 3, ((j) + 3) & 3, 1); } \
      if ((MODE) == 2 && !(n & 1)) KB_EPI_LD(n >> 1); } \
    if ((MODE) == 1) { KB_ALL(2, 2); KB_DMA1(3, 3, 0); KB_DMA1(3, 3, 1024); KB_W1(3, 3, 0); KB_DMA1(3, 3, 2048); KB_W1(3, 3, 1); } \
    _Pragma("unroll") for (int n = 0; n < 9; ++n) { \
      if (n == 0) asm volatile("s_waitcnt lgkmcnt(6)" ::: "memory"); \
      if (n == 2) asm volatile("s_waitcnt lgkmcnt(5)" ::: "memory"); \
      if (n == 5) asm volatile("s_waitcnt lgkmcnt(5)" ::: "memory"); \
      ADSRD(ZB[n], ((j) < 2 ? rd01 : rd23), ((j) & 1) * KB_STAGE + 9216 + n * 1024); \
      AMFMA(acc[0][n], ZA[n], Wr[j][0]); AMFMA(acc[1][n], ZA[n], Wr[j][1]); \
      if ((MODE) < 2) { \
        if (n == 0) KB_DMA1((i) + 3, ((j) + 3) & 3, 3072); \
        if (n == 2) KB_W1((i) + 3, ((j) + 3) & 3, 2); \
        if (n == 4) KB_DMAH((i) + 3, ((j) + 3) & 3); \
        if (n == 6) KB_W1((i) + 3, ((j) + 3) & 3, 3); } \
      if ((MODE) == 2 && (n == 0 || n == 2 || n == 4)) KB_EPI_LD(5 + (n >> 1)); } \
    KB_WAIT(((j) + 1) & 3, WN); \
    __builtin_amdgcn_s_barrier(); \
    } while (0)

  KB_ITER(0, 0, 1, 18);
  KB_ITER(1, 1, 0, 18);
  KB_ITER(2, 2, 0, 18);
  KB_ITER(3, 3, 0, 18);
  for (int io = 4; io < 12; io += 4) {
    KB_ITER(io + 0, 0, 0, 18);
    KB_ITER(io + 1, 1, 0, 18);
    KB_ITER(io + 2, 2, 0, 18);
    KB_ITER(io + 3, 3, 0, 18);
  }
  KB_ITER(12, 0, 0, 18);
  KB_ITER(13, 1, 2, 17);
  KB_ITER(14, 2, 3, 8);
  {
#pragma unroll
    for (int n = 0; n < 9; ++n) { ADSRD(ZA[n], rd23, KB_STAGE + n * 1024); AMFMA(acc[0][n], ZB[n], Wr[2][2]); AMFMA(acc[1][n], ZB[n], Wr[2][3]); }
#pragma unroll
    for (int n = 0; n < 9; ++n) {
      if (n == 0) asm volatile("s_waitcnt lgkmcnt(6)" ::: "memory");
      if (n == 2) asm volatile("s_waitcnt lgkmcnt(5)" ::: "memory");
      if (n == 5) asm volatile("s_waitcnt lgkmcnt(5)" ::: "memory");
      ADSRD(ZB[n], rd23, KB_STAGE + 9216 + n * 1024);
      AMFMA(acc[0][n], ZA[n], Wr[3][0]); AMFMA(acc[1][n], ZA[n], Wr[3][1]); }
    asm volatile("s_waitcnt lgkmcnt(0)" ::: "memory");
#pragma unroll
    for (int n = 0; n < 9; ++n) { AMFMA(acc[0][n], ZB[n], Wr[3][2]); AMFMA(acc[1][n], ZB[n], Wr[3][3]); }
  }
  asm volatile("s_nop 15" : "+v"(acc[0][0]), "+v"(acc[0][1]), "+v"(acc[0][2]), "+v"(acc[0][3]), "+v"(acc[0][4]), "+v"(acc[0][5]), "+v"(acc[0][6]), "+v"(acc[0][7]), "+v"(acc[0][8]),
               "+v"(acc[1][0]), "+v"(acc[1][1]), "+v"(acc[1][2]), "+v"(acc[1][3]), "+v"(acc[1][4]), "+v"(acc[1][5]), "+v"(acc[1][6]), "+v"(acc[1][7]), "+v"(acc[1][8]) :: "memory");
  asm volatile("s_waitcnt vmcnt(0)" : "+v"(z1q[0][0]), "+v"(z1q[0][1]), "+v"(z1q[1][0]), "+v"(z1q[1][1]), "+v"(z1s[0]), "+v"(z1s[1]), "+v"(biasu[0]), "+v"(biasu[1]) :: "memory");
  SCHED();
  char* hp = ws + OFF_HT + ((size_t)(tp * 8 + wm * 2) * 16 + fb) * 4608;
  float bias[2] = {bf_lo(biasu[0]), bf_lo(biasu[1])};
  char* ex = smem + wm * 18432 + lane * 16;
  if (kh == 0) {
#pragma unroll
    for (int m = 0; m < 2; ++m)
#pragma unroll
      for (int n = 4; n < 8; ++n) *(f32x4*)(ex + (m * 9 + n) * 1024) = acc[m][n];
  } else {
#pragma unroll
    for (int m = 0; m < 2; ++m) {
#pragma unroll
      for (int n = 0; n < 4; ++n) *(f32x4*)(ex + (m * 9 + n) * 1024) = acc[m][n];
      *(f32x4*)(ex + (m * 9 + 8) * 1024) = acc[m][8];
    }
  }
  __syncthreads();
#pragma unroll
  for (int m = 0; m < 2; ++m) {
#pragma unroll
    for (int q = 0; q < 2; ++q) {
      f32x4 g0, g1;
      if (kh == 0) { g0 = acc[m][2 * q] + *(const f32x4*)(ex + (m * 9 + 2 * q) * 1024); g1 = acc[m][2 * q + 1] + *(const f32x4*)(ex + (m * 9 + 2 * q + 1) * 1024); }
      else { g0 = acc[m][4 + 2 * q] + *(const f32x4*)(ex + (m * 9 + 4 + 2 * q) * 1024); g1 = acc[m][5 + 2 * q] + *(const f32x4*)(ex + (m * 9 + 5 + 2 * q) * 1024); }
      u32x4 z = z1q[m][q], o;
      o[0] = pk_bf16(bf_lo(z[0]) * (g0[0] + bias[m]), bf_hi(z[0]) * (g0[1] + bias[m]));
      o[1] = pk_bf16(bf_lo(z[1]) * (g0[2] + bias[m]), bf_hi(z[1]) * (g0[3] + bias[m]));
      o[2] = pk_bf16(bf_lo(z[2]) * (g1[0] + bias[m]), bf_hi(z[2]) * (g1[1] + bias[m]));
      o[3] = pk_bf16(bf_lo(z[3]) * (g1[2] + bias[m]), bf_hi(z[3]) * (g1[3] + bias[m]));
      st16(hp + m * (16 * 4608) + (kh * 2 + q) * 1024 + lane * 16, o);
    }
    if (kh == 0) {
      f32x4 g0 = acc[m][8] + *(const f32x4*)(ex + (m * 9 + 8) * 1024);
      u32x2 z = z1s[m], o;
      o[0] = pk_bf16(bf_lo(z[0]) * (g0[0] + bias[m]), bf_hi(z[0]) * (g0[1] + bias[m]));
      o[1] = pk_bf16(bf_lo(z[1]) * (g0[2] + bias[m]), bf_hi(z[1]) * (g0[3] + bias[m]));
      *(u32x2*)(hp + m * (16 * 4608) + 4096 + lane * 8) = o;
    }
  }
}

constexpr int KC_PITCH = 336;
constexpr int KC_PART = 64 * KC_PITCH;
constexpr int KC_LDS = 5 * KC_PART;
template <bool ISO>
__device__ __forceinline__ void kc_main(f32x4 (&acc)[4][5], rsrc_t rA, rsrc_t rW, unsigned vA16, unsigned vA8, unsigned vW, unsigned sA, unsigned sW) {
  constexpr unsigned TS = ISO ? 18432u : 16u * 4608u;
  constexpr int PD = 3;
  bf16x8 A[PD + 1][4], W[PD + 1][5];
  auto load = [&](int u, int slot) {
#pragma unroll
    for (int m = 0; m < 4; ++m) {
      if (ISO || (u % 9) == 4) {
        unsigned o0, o1;
        if (ISO) { o0 = sA + m * TS + u * 1024; o1 = o0 + 512; }
        else { o0 = sA + m * TS + (u / 9) * 2 * 4608 + 4096; o1 = o0 + 4608; }
        u32x2 lo = __builtin_amdgcn_raw_buffer_load_b64(rA, vA8, o0, 0);
        u32x2 hi = __builtin_amdgcn_raw_buffer_load_b64(rA, vA8, o1, 0);
        u32x4 v = {lo[0], lo[1], hi[0], hi[1]};
        A[slot][m] = __builtin_bit_cast(bf16x8, v);
      } else {
        int i = u % 9;
        unsigned o = sA + m * TS + (u / 9) * 2 * 4608 + (i < 4 ? i * 1024 : 4608 + (i - 5) * 1024);
        A[slot][m] = __builtin_bit_cast(bf16x8, __builtin_amdgcn_raw_buffer_load_b128(rA, vA16, o, 0));
      }
    }
#pragma unroll
    for (int n = 0; n < 5; ++n) W[slot][n] = __builtin_bit_cast(bf16x8, __builtin_amdgcn_raw_buffer_load_b128(rW, vW, sW + (u * 5 + n) * 1024, 0));
  };
#pragma unroll
  for (int u = 0; u < PD; ++u) load(u, u);
  SCHED();
#pragma unroll
  for (int u = 0; u < 18; ++u) {
    if (u + PD < 18) load(u + PD, (u + PD) % (PD + 1));
    SCHED();
#pragma unroll
    for (int m = 0; m < 4; ++m)
#pragma unroll
      for (int n = 0; n < 5; ++n) acc[m][n] = MFMA(W[u % (PD + 1)][n], A[u % (PD + 1)][m], acc[m][n]);
    SCHED();
  }
}
__global__ __launch_bounds__(320, 2) void kC(const char* __restrict__ ws_c, const u16* __restrict__ x, u16* __restrict__ out) {
  extern __shared__ __attribute__((aligned(16))) char smem[];
  const int tid = threadIdx.x, lane = tid & 63;
  const int w = RFL(tid >> 6);
  const int b = blockIdx.x, xcd = b & 7, idx = b >> 3;
  const int rt = (xcd & 3) * 8 + (idx & 7), ct = (xcd >> 2) * 4 + (idx >> 3);
  f32x4 acc[4][5];
#pragma unroll
  for (int m = 0; m < 4; ++m)
#pragma unroll
    for (int n = 0; n < 5; ++n) acc[m][n] = (f32x4){0.f, 0.f, 0.f, 0.f};
  const rsrc_t rW = mk_rsrc(ws_c + OFF_WC, (int)SZ_WC);
  const unsigned sW = RFL((unsigned)((ct * 90 + w * 18) * 5 * 1024));
  const int dstart = ct < 7 ? ct * 72 : 496;
  if (w < 4) {
    const rsrc_t rA = mk_rsrc(ws_c + OFF_HT, (int)SZ_Z);
    kc_main<false>(acc, rA, rW, lane * 16, lane * 8, lane * 16, RFL((unsigned)((rt * 4 * 16 + w * 4) * 4608)), sW);
  } else {
    const rsrc_t rA = mk_rsrc(ws_c + OFF_OT, (int)SZ_OT);
    kc_main<true>(acc, rA, rW, lane * 16, lane * 8, lane * 16, RFL((unsigned)(rt * 4 * 18432)), sW);
  }
  {
    char* p = smem + w * KC_PART + (lane & 15) * KC_PITCH + (lane >> 4) * 16;
#pragma unroll
    for (int m = 0; m < 4; ++m)
#pragma unroll
      for (int n = 0; n < 5; ++n) *(f32x4*)(p + m * 16 * KC_PITCH + n * 64) = acc[m][n];
  }
  __syncthreads();
  const int own0 = ct * 72 - dstart;
  for (int c = tid; c < 576; c += 320) {
    int r = c / 9, ch = c - r * 9;
    const char* p = smem + r * KC_PITCH + (own0 + ch * 8) * 4;
    f32x4 s0 = *(const f32x4*)p, s1 = *(const f32x4*)(p + 16);
#pragma unroll
    for (int ww = 1; ww < 5; ++ww) { s0 += *(const f32x4*)(p + ww * KC_PART); s1 += *(const f32x4*)(p + ww * KC_PART + 16); }
    size_t go = (size_t)(rt * 64 + prow(r >> 4, r & 15)) * 576 + ct * 72 + ch * 8;
    u32x4 xv = *(const u32x4*)(x + go), o;
    o[0] = pk_bf16(s0[0] + bf_lo(xv[0]), s0[1] + bf_hi(xv[0]));
    o[1] = pk_bf16(s0[2] + bf_lo(xv[1]), s0[3] + bf_hi(xv[1]));
    o[2] = pk_bf16(s1[0] + bf_lo(xv[2]), s1[1] + bf_hi(xv[2]));
    o[3] = pk_bf16(s1[2] + bf_lo(xv[3]), s1[3] + bf_hi(xv[3]));
    st16(out + go, o);
  }
}

constexpr int K0_BLOCKS = 280;
constexpr int PFA_BYTES = 36864, PFB_BYTES = 9216, PFC_BYTES = 13312;
#define PF_TLB_N 16
#define PF_TLB_STRIDE 65536
__global__ __launch_bounds__(256) void k0(const u16* __restrict__ x, const u16* __restrict__ wqkv, const u16* __restrict__ win, char* __restrict__ ws, const u16* __restrict__ wsp, const u16* __restrict__ wout, const u16* __restrict__ wo, const u16* __restrict__ bsp) {
  __shared__ __attribute__((aligned(16))) char sm[32 * K0_PITCH];
  const int tid = threadIdx.x, lane = tid & 63, i = lane & 15, g = lane >> 4, w = tid >> 6, b = blockIdx.x;
  const unsigned smb = (unsigned)(size_t)(__attribute__((address_space(3))) char*)sm;
  unsigned pf0 = 0, pf1 = 0, pf2 = 0;
  if (b == 217 && w == 1) { const char* a = (const char*)bsp + (lane & 31) * 128; asm volatile("global_load_dword %0, %1, off" : "=v"(pf1) : "v"(a)); }
  if (b < 144) {
    const int ks = b >> 3, cb = b & 7;
    u32x4 v[9];
#pragma unroll
    for (int k = 0; k < 9; ++k) { int c = tid + k * 256, r = c / 72, ch = c - r * 72; v[k] = *(const u32x4*)(win + (size_t)(ks * 32 + r) * 4608 + cb * 576 + ch * 8); }
#pragma unroll
    for (int k = 0; k < 9; ++k) { int c = tid + k * 256, r = c / 72, ch = c - r * 72; *(u32x4*)(sm + r * K0_PITCH + ch * 16) = v[k]; }
    __syncthreads();
    const unsigned a0 = smb + (8 * g + (i >> 2)) * K0_PITCH + (i & 3) * 8;
#pragma unroll
    for (int e = 0; e < 9; ++e) {
      int nt = w * 9 + e;
      int gt = cb * 36 + nt;
      int z2 = gt >= 144, t = gt - z2 * 144, fb = t / 9, n = t - fb * 9;
      int fast = z2 ? n < 5 : n >= 4, nz = 4 + fast;
      int fr = fb * 468 + (fast ? 0 : 252) + 72 + (z2 ? ks * nz + n - (fast ? 0 : 5) : 18 * nz + ks * nz + n - (fast ? 4 : 0));
      u32x4 o = tr_frag(a0 + nt * 32, a0 + nt * 32 + 4 * K0_PITCH);
      st16(ws + OFF_W1 + (size_t)fr * 1024 + lane * 16, o);
    }
  } else if (b < 216) {
    const int u = b - 144, ks = u >> 2, hq = u & 3;
    u32x4 v[7];
#pragma unroll
    for (int k = 0; k < 7; ++k) { int c = tid + k * 256, r = c / 54, e = c - r * 54, part = e / 18, ch = e - part * 18;
      if (c < 32 * 54) v[k] = *(const u32x4*)(wqkv + (size_t)(ks * 32 + r) * 1728 + part * 576 + hq * 144 + ch * 8); }
#pragma unroll
    for (int k = 0; k < 7; ++k) { int c = tid + k * 256, r = c / 54, e = c - r * 54; if (c < 32 * 54) *(u32x4*)(sm + r * K0_PITCH + e * 16) = v[k]; }
    __syncthreads();
#pragma unroll
    for (int e = 0; e < 8; ++e) {
      int f = w * 8 + e, hl = f >> 2, n = f & 3, c = n * 16 + i, col; bool ok = true;
      if (c < 18) col = 144 + hl * 18 + c; else if (c >= 20 && c < 38) col = 288 + hl * 18 + c - 20; else if (c >= 40 && c < 58) col = hl * 18 + c - 40; else { col = 0; ok = false; }
      unsigned h[8];
#pragma unroll
      for (int j = 0; j < 8; ++j) h[j] = ok ? *(const u16*)(sm + (8 * g + j) * K0_PITCH + col * 2) : 0u;
      st16(ws + OFF_W1 + (size_t)(((hq * 8 + hl) >> 1) * 468 + (hl & 1) * 252 + ks * 4 + n) * 1024 + lane * 16, (u32x4){h[0] | (h[1] << 16), h[2] | (h[3] << 16), h[4] | (h[5] << 16), h[6] | (h[7] << 16)});
    }
  } else {
    if (b == 216) for (int c = tid; c < (2048 * 16 + 16 * 256) / 16; c += 256) *(u32x4*)(ws + OFF_RS + c * 16) = (u32x4){0, 0, 0, 0};
    const int u = (b - 216) * 4 + w, tt = u >> 1, slot = (u & 1) * 8 + (lane >> 3), c0 = lane & 7;
    const u16* xr = x + (size_t)prow(tt, slot) * 576 + c0 * 8;
    u32x4 v[9];
#pragma unroll
    for (int k = 0; k < 9; ++k) v[k] = *(const u32x4*)(xr + k * 64);
    float s = 0.f;
#pragma unroll
    for (int k = 0; k < 9; ++k)
#pragma unroll
      for (int j = 0; j < 4; ++j) s += bf_lo(v[k][j]) + bf_hi(v[k][j]);
    s += __shfl_xor(s, 1); s += __shfl_xor(s, 2); s += __shfl_xor(s, 4);
    const float mean = s * (1.f / 576.f);
    float q = 0.f;
#pragma unroll
    for (int k = 0; k < 9; ++k)
#pragma unroll
      for (int j = 0; j < 4; ++j) { float a = bf_lo(v[k][j]) - mean, c = bf_hi(v[k][j]) - mean; q += a * a + c * c; }
    q += __shfl_xor(q, 1); q += __shfl_xor(q, 2); q += __shfl_xor(q, 4);
    const float mb = rbf(mean), sd = rbf(sqrtf(rbf(rbf(q * (1.f / 576.f)) + 1e-5f))), rs = 1.f / sd;
    char* dst = ws + OFF_XT + (size_t)tt * 18432 + slot * 16;
#pragma unroll
    for (int k = 0; k < 9; ++k) {
      u32x4 o;
#pragma unroll
      for (int j = 0; j < 4; ++j) o[j] = pk_bf16(rbf(bf_lo(v[k][j]) - mb) * rs, rbf(bf_hi(v[k][j]) - mb) * rs);
      int c = c0 + 8 * k;
      st16(dst + (c >> 2) * 1024 + (c & 3) * 256, o);
    }
  }
  asm volatile("s_waitcnt vmcnt(0)" : "+v"(pf0), "+v"(pf1), "+v"(pf2) :: "memory");
}

extern "C" void kernel_launch(void* const* d_in, const int* in_sizes, int n_in, void* d_out, int out_size, void* d_ws, size_t ws_size, hipStream_t stream) {
  (void)in_sizes; (void)n_in; (void)out_size; (void)ws_size;
  const u16* x = (const u16*)d_in[0];
  char* ws = (char*)d_ws;
  (void)hipFuncSetAttribute((const void*)kA, hipFuncAttributeMaxDynamicSharedMemorySize, KA_LDS);
  (void)hipFuncSetAttribute((const void*)kB, hipFuncAttributeMaxDynamicSharedMemorySize, KB_LDS);
  (void)hipFuncSetAttribute((const void*)kC, hipFuncAttributeMaxDynamicSharedMemorySize, KC_LDS);
  k0<<<K0_BLOCKS, 256, 0, stream>>>(x, (const u16*)d_in[1], (const u16*)d_in[4], ws, (const u16*)d_in[5], (const u16*)d_in[7], (const u16*)d_in[2], (const u16*)d_in[6]);
  kA<<<512, 256, KA_LDS, stream>>>(ws, ws, (const u16*)d_in[5], (const u16*)d_in[7], (const u16*)d_in[2]);
  kB<<<256, 512, KB_LDS, stream>>>(ws, ws, (const u16*)d_in[6]);
  kC<<<256, 320, KC_LDS, stream>>>(ws, x, (u16*)d_out);
}
```

```cpp
#include <hip/hip_runtime.h>
#include <hip/hip_bf16.h>
#include <stdint.h>

typedef unsigned short u16;
typedef __attribute__((ext_vector_type(8))) short bf16x8;
typedef __attribute__((ext_vector_type(4))) short bf16x4;
typedef __attribute__((ext_vector_type(4))) float f32x4;
typedef __attribute__((ext_vector_type(4))) unsigned u32x4;
typedef __attribute__((ext_vector_type(2))) unsigned u32x2;
typedef __amdgpu_buffer_rsrc_t rsrc_t;

constexpr int S_ = 2048, D_ = 576, F_ = 4608, FH_ = 2304;
constexpr size_t OFF_Z2T = 0;
constexpr size_t SZ_Z = (size_t)16 * 64 * 9 * 1024;
constexpr size_t OFF_Z1T = OFF_Z2T + SZ_Z;
constexpr size_t OFF_HT = OFF_Z1T + SZ_Z;
constexpr size_t OFF_WT = OFF_HT + SZ_Z;
constexpr size_t SZ_WT = (size_t)16 * 64 * 8 * 1024;
constexpr size_t OFF_OT = OFF_WT + SZ_WT;
constexpr size_t SZ_OT = (size_t)128 * 144 * 128;
constexpr size_t OFF_WC = OFF_OT + SZ_OT;
constexpr size_t SZ_WC = (size_t)8 * 90 * 5 * 1024;
constexpr size_t OFF_XT = OFF_WC + SZ_WC;
constexpr size_t SZ_XT = (size_t)128 * 18 * 1024;
constexpr size_t OFF_W1 = OFF_XT + SZ_XT;
constexpr size_t SZ_W1 = (size_t)32 * 234 * 1024;
constexpr size_t OFF_RS = OFF_W1 + SZ_W1;
constexpr size_t OFF_CNT = OFF_RS + 2048 * 16;
constexpr size_t OFF_ST = OFF_CNT + 16 * 256;
constexpr size_t WS_END = OFF_ST + (1 << 20);

#define RFL(x) __builtin_amdgcn_readfirstlane(x)
#define SCHED() __builtin_amdgcn_sched_barrier(0)
__device__ __forceinline__ rsrc_t mk_rsrc(const void* p, int bytes) { return __builtin_amdgcn_make_buffer_rsrc((void*)p, (short)0, bytes, 0x00020000); }
__device__ __forceinline__ float bf_lo(unsigned w) { return __uint_as_float(w << 16); }
__device__ __forceinline__ float bf_hi(unsigned w) { return __uint_as_float(w & 0xffff0000u); }
__device__ __forceinline__ unsigned pk_bf16(float lo, float hi) {
  typedef __attribute__((ext_vector_type(2))) float f32x2; typedef __attribute__((ext_vector_type(2))) __bf16 bf2;
  f32x2 v = {lo, hi}; bf2 r = __builtin_convertvector(v, bf2); return __builtin_bit_cast(unsigned, r);
}
__device__ __forceinline__ float rbf(float f) { return bf_hi(pk_bf16(0.f, f)); }
__host__ __device__ __forceinline__ int prow(int tt, int i) { return 32 * (tt >> 1) + 8 * (i >> 2) + 4 * (tt & 1) + (i & 3); }
__device__ __forceinline__ void st16(void* p, u32x4 v) { asm volatile("global_store_dwordx4 %0, %1, off sc1\n\ts_nop 1" :: "v"(p), "v"(v) : "memory"); }
#define MFMA(a, b, c) __builtin_amdgcn_mfma_f32_16x16x32_bf16(a, b, c, 0, 0, 0)

#define DMA16(lds, voff, srd, soff, imm) asm volatile("s_mov_b32 m0, %0\n\ts_nop 0\n\tbuffer_load_dwordx4 %1, %2, %3 offen offset:" #imm " lds" :: "s"(lds), "v"(voff), "s"(srd), "s"(soff) : "memory")
#define DMA16H(lds, voff, srd, soff) asm volatile("s_mov_b32 m0, %0\n\ts_lshr_b64 exec, -1, 32\n\tbuffer_load_dwordx4 %1, %2, %3 offen lds\n\ts_mov_b64 exec, -1" :: "s"(lds), "v"(voff), "s"(srd), "s"(soff) : "memory")
typedef __attribute__((address_space(3))) char lds_char;
#define LDS_RD128(addr) (*(const __attribute__((address_space(3))) bf16x8*)(addr))
#define AMFMA(acc, a, b) asm volatile("v_mfma_f32_16x16x32_bf16 %0, %1, %2, %0" : "+v"(acc) : "v"(a), "v"(b))
#define ADSRD(dst, addr, off) asm volatile("ds_read_b128 %0, %1 offset:%2" : "=v"(dst) : "v"(addr), "i"(off))
#define OPAQUE(x) asm volatile("" : "+v"(x))
#define BLOAD16(dst, voff, srd, soff, imm) asm volatile("buffer_load_dwordx4 %0, %1, %2, %3 offen offset:" #imm : "=v"(dst) : "v"(voff), "s"(srd), "s"(soff))

constexpr int K0_PITCH = 1168;
__device__ __forceinline__ int wc_krow(int ks, int g, int j) {
  int r = j & 3, hi = j >> 2;
  if (ks >= 72) { int u = ks - 72; return 2304 + 4 * (8 * u + 4 * hi + g) + r; }
  int w = ks / 18, pi = (ks % 18) / 9, i = ks % 9, fb0 = 4 * w + 2 * pi;
  if (i == 4) return 144 * (fb0 + hi) + 128 + 4 * g + r;
  if (i < 4) return 144 * fb0 + 32 * i + 16 * hi + 4 * g + r;
  return 144 * (fb0 + 1) + 32 * (i - 5) + 16 * hi + 4 * g + r;
}
__device__ __forceinline__ u32x4 tr_frag(unsigned a0, unsigned a1) {
  u32x2 lo, hi;
  asm volatile("ds_read_b64_tr_b16 %0, %2\n\tds_read_b64_tr_b16 %1, %3\n\ts_waitcnt lgkmcnt(0)" : "=&v"(lo), "=&v"(hi) : "v"(a0), "v"(a1) : "memory");
  return (u32x4){lo[0], lo[1], hi[0], hi[1]};
}
#define KA_SLEEP 32
#define PFA_POL ""
#define SJLD(p) (*(p))
constexpr int KA_SLOT = 10240, KA_ATT = 6 * KA_SLOT, KA_PITCH = 144, KA_LDS = KA_ATT + 142 * KA_PITCH;
template <int FAST> struct KaCfg {
  static constexpr int NZ2 = 4 + FAST, NZ1 = 4 + FAST;
  static constexpr int nt(int s) { return s < 9 ? 4 : 4 + FAST; }
  static constexpr int goff(int s) { return s < 9 ? s * 8192 : 73728 + (s - 9) * (4 + FAST) * 2048; }
  static constexpr int ops(int s) { return s > 26 ? 0 : 2 + (nt(s) == 5 ? 1 : 0) + (s < 9 ? 4 + ((s & 1) ? 0 : 1) : 0); }
  static constexpr int wait(int s) { return s == 20 ? ops(23) + ops(24) : ops(s + 2) + ops(s + 3) + ops(s + 4) + ((s >= 9 && s <= 12) ? 6 : 0); }
};
template <int V> struct IC { static constexpr int v = V; };
#define BLD(dst, voff, srd, soff, IMM) asm volatile("buffer_load_dwordx4 %0, %1, %2, %3 offen offset:%4" : "=v"(dst) : "v"(voff), "s"(srd), "s"(soff), "i"(IMM))
#define AMFMA0(acc, a, b) asm volatile("v_mfma_f32_16x16x32_bf16 %0, %1, %2, 0" : "=&v"(acc) : "v"(a), "v"(b))
typedef __attribute__((ext_vector_type(2))) float f32x2;
typedef __attribute__((ext_vector_type(2))) __bf16 bf2_t;
__device__ __forceinline__ float dot2(unsigned a, unsigned b, float c) { return __builtin_amdgcn_fdot2_f32_bf16(__builtin_bit_cast(bf2_t, a), __builtin_bit_cast(bf2_t, b), c, false); }
__device__ __forceinline__ f32x2 gelu2(f32x2 x) {
  f32x2 t = x * x, p = t * (-0.10294324f) + (-2.3022082f), e = x * p;
  f32x2 d = {__builtin_amdgcn_exp2f(e[0]) + 1.f, __builtin_amdgcn_exp2f(e[1]) + 1.f};
  f32x2 r = {__builtin_amdgcn_rcpf(d[0]), __builtin_amdgcn_rcpf(d[1])};
  return x * r;
}
template <int CTRL> __device__ __forceinline__ float dpp_add(float v) {
  return v + __builtin_bit_cast(float, __builtin_amdgcn_update_dpp(0, __builtin_bit_cast(int, v), CTRL, 0xf, 0xf, true));
}

__device__ __forceinline__ float xmax16(float v) { auto r = __builtin_amdgcn_permlane16_swap(__float_as_uint(v), __float_as_uint(v), false, false); return fmaxf(__uint_as_float(r[0]), __uint_as_float(r[1])); }
__device__ __forceinline__ float xmax32(float v) { auto r = __builtin_amdgcn_permlane32_swap(__float_as_uint(v), __float_as_uint(v), false, false); return fmaxf(__uint_as_float(r[0]), __uint_as_float(r[1])); }
__device__ __forceinline__ float xadd16(float v) { auto r = __builtin_amdgcn_permlane16_swap(__float_as_uint(v), __float_as_uint(v), false, false); return __uint_as_float(r[0]) + __uint_as_float(r[1]); }
__device__ __forceinline__ float xadd32(float v) { auto r = __builtin_amdgcn_permlane32_swap(__float_as_uint(v), __float_as_uint(v), false, false); return __uint_as_float(r[0]) + __uint_as_float(r[1]); }
#define PRIO_HI()
#define PRIO_LO()
#define LBAR() asm volatile("s_waitcnt lgkmcnt(0)\n\ts_barrier" ::: "memory")
__device__ __forceinline__ float swap1(float v) { return __builtin_bit_cast(float, __builtin_amdgcn_update_dpp(0, __builtin_bit_cast(int, v), 0xB1, 0xf, 0xf, true)); }
template <int FAST>
__device__ __forceinline__ void ka_body(char* smem, const char* __restrict__ ws_c, char* __restrict__ ws, const int p, const int cg, const int sj, const u16* __restrict__ wsp, const u16* __restrict__ wout, const u16* __restrict__ wo) {
  typedef KaCfg<FAST> C;
  const int tid = threadIdx.x, lane = tid & 63, li = lane & 15, lg = lane >> 4;
  const int w = RFL(tid >> 6), fb = cg >> 1;
  const rsrc_t rW = mk_rsrc(ws_c + OFF_W1 + (size_t)fb * (468 * 1024) + (FAST ? 0 : 252 * 1024), (FAST ? 252 : 216) * 1024);
  const rsrc_t rX = mk_rsrc(ws_c + OFF_XT, (int)SZ_XT);
  const unsigned ldsb = (unsigned)(size_t)(__attribute__((address_space(3))) char*)smem;
  const unsigned vl16 = lane * 16;
  const unsigned vX = vl16 + (unsigned)(p * 8 + w * 2) * 18432u;
  const unsigned vH = p == 0 ? 0x80000000u : (unsigned)(p * 8 - 2 + (li >> 3)) * 18432u + (unsigned)(lg * 16 + (li | 8)) * 16u + (unsigned)w * 1024u;
  const unsigned sF = RFL((unsigned)w * 2048u), sH = RFL(8192u + (unsigned)w * 512u);
  const unsigned rd = ldsb + vl16;
  constexpr int KA_WARM = 0;
  bf16x8 X[2][18], HX[5], F[5];
  f32x4 acc[2][5], hacc[3];
#pragma unroll
  for (int n = 0; n < 3; ++n) hacc[n] = (f32x4){0.f, 0.f, 0.f, 0.f};

  bf16x8 (&X_)[2][18] = X; bf16x8 (&HX_)[5] = HX; bf16x8 (&F_)[5] = F; f32x4 (&acc_)[2][5] = acc; f32x4 (&hacc_)[3] = hacc;
  const rsrc_t rW_ = rW, rX_ = rX; const unsigned vl16_ = vl16, vX_ = vX, vH_ = vH, rd_ = rd;
#define KA_CAP bf16x8 (&X)[2][18] = X_; bf16x8 (&HX)[5] = HX_; bf16x8 (&F)[5] = F_; f32x4 (&acc)[2][5] = acc_; f32x4 (&hacc)[3] = hacc_; \
    const rsrc_t rW = rW_, rX = rX_; const unsigned vl16 = vl16_, vX = vX_, vH = vH_, rd = rd_; (void)X; (void)HX; (void)F; (void)acc; (void)hacc; (void)rW; (void)rX; (void)vl16; (void)vX; (void)vH; (void)rd;
  auto vop = [&](auto S_, auto K_) {
    KA_CAP
    constexpr int s = decltype(S_)::v, k = decltype(K_)::v;
    if constexpr (s <= 26) {
      constexpr int slot = s % 6, nd = 2 + (C::nt(s) == 5 ? 1 : 0);
      if constexpr (k == 0) { unsigned lo = ldsb + slot * KA_SLOT + sF, so = C::goff(s) + sF; DMA16(lo, vl16, rW, so, 0); }
      else if constexpr (k == 1) { unsigned lo = ldsb + slot * KA_SLOT + sF, so = C::goff(s) + sF; DMA16(lo, vl16, rW, so, 1024); }
      else if constexpr (k == 2 && nd == 3) { unsigned lo = ldsb + slot * KA_SLOT + sH, so = C::goff(s) + sH; DMA16H(lo, vl16, rW, so); }
      else if constexpr (s < 9 && k >= 2 && k < 6) {
        constexpr int m = (k - 2) & 1, ks = 2 * s + ((k - 2) >> 1);
        BLD(X[m][ks], vX, rX, m * 18432 + (ks & ~3) * 1024, (ks & 3) * 1024);
      } else if constexpr (s < 9 && k == 6 && !(s & 1)) { BLD(HX[s >> 1], vH, rX, (s >> 1) * 4096, 0); }
    }
  };
  auto group = [&](auto S_) { vop(S_, IC<0>{}); vop(S_, IC<1>{}); vop(S_, IC<2>{}); vop(S_, IC<3>{}); vop(S_, IC<4>{}); vop(S_, IC<5>{}); vop(S_, IC<6>{}); };
  auto sync = [&](auto S_) {
    KA_CAP
    constexpr int s = decltype(S_)::v;
    if constexpr (s < 26) {
      if constexpr (s + 1 < 9) {
        constexpr int k0 = 2 * (s + 1);
        asm volatile("s_waitcnt vmcnt(%5)" : "+v"(X[0][k0]), "+v"(X[1][k0]), "+v"(X[0][k0 + 1]), "+v"(X[1][k0 + 1]), "+v"(HX[(s + 1) >> 1]) : "i"(C::wait(s)) : "memory");
      } else asm volatile("s_waitcnt vmcnt(%0)" :: "i"(C::wait(s)) : "memory");
      __builtin_amdgcn_s_barrier();
    }
  };
  auto step = [&](auto S_, auto H_) {
    KA_CAP
    constexpr int s = decltype(S_)::v, h = decltype(H_)::v;
    constexpr int NT = C::nt(s), job = s / 9, ks = 2 * (s % 9) + h;
    constexpr int s2 = h ? s + 1 : s, h2 = h ^ 1, NT2 = s2 <= 26 ? C::nt(s2) : 0;
    constexpr int roff = (s2 % 6) * KA_SLOT + h2 * NT2 * 1024;
    if constexpr (ks == 0) asm volatile("s_waitcnt lgkmcnt(0)" ::: "memory");
    if constexpr (job == 0) {
      if (w == (ks & 3)) {
        asm volatile("s_waitcnt lgkmcnt(1)" ::: "memory");
#pragma unroll
        for (int n = 0; n < 3; ++n) AMFMA(hacc[n], F[n], HX[ks >> 2]);
      }
    }
#pragma unroll
    for (int n = 0; n < NT; ++n) {
      if constexpr (ks != 0) asm volatile("s_waitcnt lgkmcnt(%0)" :: "i"(NT - 1) : "memory");
      if constexpr (job == 1) {
        if constexpr (ks == 0) { AMFMA0(acc[0][n], X[0][ks], F[n]); AMFMA0(acc[1][n], X[1][ks], F[n]); }
        else { AMFMA(acc[0][n], X[0][ks], F[n]); AMFMA(acc[1][n], X[1][ks], F[n]); }
      } else {
        if constexpr (ks == 0) { AMFMA0(acc[0][n], F[n], X[0][ks]); AMFMA0(acc[1][n], F[n], X[1][ks]); }
        else { AMFMA(acc[0][n], F[n], X[0][ks]); AMFMA(acc[1][n], F[n], X[1][ks]); }
      }
      if (n < NT2) ADSRD(F[n], rd, roff + n * 1024);
      if constexpr (h == 1) {
        if (n == 0) { vop(IC<s + 5>{}, IC<0>{}); vop(IC<s + 5>{}, IC<2>{}); }
        if (n == 1) { vop(IC<s + 5>{}, IC<1>{}); vop(IC<s + 5>{}, IC<3>{}); }
        if (n == 2) { vop(IC<s + 5>{}, IC<4>{}); vop(IC<s + 5>{}, IC<5>{}); }
        if (n == 3) { vop(IC<s + 5>{}, IC<6>{}); }
      }
    }
    if constexpr (NT2 > NT) ADSRD(F[4], rd, roff + 4 * 1024);
  };
#define KA_ACC_DONE() do { asm volatile("s_nop 15" : "+v"(acc[0][0]), "+v"(acc[0][1]), "+v"(acc[0][2]), "+v"(acc[0][3]), "+v"(acc[1][0]), "+v"(acc[1][1]), "+v"(acc[1][2]), "+v"(acc[1][3]) :: "memory"); \
    if constexpr (FAST) asm volatile("" : "+v"(acc[0][4]), "+v"(acc[1][4]) :: "memory"); } while (0)
  auto iter = [&](auto S_) { step(S_, IC<0>{}); sync(S_); step(S_, IC<1>{}); };

  group(IC<0>{}); group(IC<1>{});
  asm volatile("s_waitcnt vmcnt(%5)" : "+v"(X[0][0]), "+v"(X[1][0]), "+v"(X[0][1]), "+v"(X[1][1]), "+v"(HX[0]) : "i"(C::ops(1) + KA_WARM) : "memory");
  __builtin_amdgcn_s_barrier();
#pragma unroll
  for (int n = 0; n < 4; ++n) ADSRD(F[n], rd, n * 1024);

  step(IC<0>{}, IC<0>{}); group(IC<2>{}); group(IC<3>{}); group(IC<4>{}); sync(IC<0>{}); step(IC<0>{}, IC<1>{});
  iter(IC<1>{}); iter(IC<2>{}); iter(IC<3>{}); iter(IC<4>{}); iter(IC<5>{}); iter(IC<6>{}); iter(IC<7>{}); iter(IC<8>{});
  asm volatile("s_nop 15" : "+v"(acc[0][0]), "+v"(acc[0][1]), "+v"(acc[0][2]), "+v"(acc[0][3]), "+v"(acc[1][0]), "+v"(acc[1][1]), "+v"(acc[1][2]), "+v"(acc[1][3]), "+v"(hacc[0]), "+v"(hacc[1]), "+v"(hacc[2]) :: "memory");

  SCHED();
  {
    char* att = smem + KA_ATT;
    char* hp = smem + 2 * KA_SLOT;
#pragma unroll
    for (int n = 0; n < 3; ++n) if (n != w) *(f32x4*)(hp + (n * 3 + (w > n ? w - 1 : w)) * 1024 + vl16) = hacc[n];
#pragma unroll
    for (int m = 0; m < 2; ++m) {
      char* rp = att + (14 + w * 32 + 8 * (li >> 2) + 4 * m + (li & 3)) * KA_PITCH + lg * 8;
#pragma unroll
      for (int n = 0; n < 4; ++n) *(u32x2*)(rp + n * 32) = (u32x2){pk_bf16(acc[m][n][0], acc[m][n][1]), pk_bf16(acc[m][n][2], acc[m][n][3])};
    }
    LBAR();
    if (w < 3) {
      f32x4 a = w == 0 ? hacc[0] : (w == 1 ? hacc[1] : hacc[2]);
#pragma unroll
      for (int q = 0; q < 3; ++q) a += *(const f32x4*)(hp + (w * 3 + q) * 1024 + vl16);
      int ho = (li < 8) ? 8 * (li >> 2) + (li & 3) : 8 * ((li - 8) >> 2) + 4 + (li & 3);
      if (ho >= 2) *(u32x2*)(att + (ho - 2) * KA_PITCH + w * 32 + lg * 8) = (u32x2){pk_bf16(a[0], a[1]), pk_bf16(a[2], a[3])};
    }
    LBAR();
    const unsigned attb = ldsb + KA_ATT;
    float mbias[8];
#pragma unroll
    for (int e = 0; e < 8; ++e) { int d = (e >> 2) * 16 + lg * 4 + (e & 3) - li; mbias[e] = (!(d & 1) && d >= 2 && d <= 16) ? 0.f : -__builtin_inff(); }
    bf16x8 Q[2], K[2][2]; u32x2 V[2][2][2];
#pragma unroll
    for (int jj = 0; jj < 2; ++jj) {
      const int j = w * 2 + jj;
      Q[jj] = *(const bf16x8*)(att + (14 + 16 * j + li) * KA_PITCH + 80 + (lg < 3 ? lg : 2) * 16);
#pragma unroll
      for (int kt = 0; kt < 2; ++kt) {
        int R = 16 * j - 2 + 16 * kt + li; R = R < 0 ? 0 : R;
        K[jj][kt] = *(const bf16x8*)(att + R * KA_PITCH + lg * 16);
        int Rv = 16 * j - 2 + 16 * kt + 4 * lg + (li >> 2); Rv = Rv < 0 ? 0 : Rv;
        const unsigned va = attb + Rv * KA_PITCH + 40 + (li & 3) * 8;
        asm volatile("ds_read_b64_tr_b16 %0, %2\n\tds_read_b64_tr_b16 %1, %2 offset:32" : "=&v"(V[jj][0][kt]), "=&v"(V[jj][1][kt]) : "v"(va) : "memory");
      }
    }
    f32x4 S[2][2];
#pragma unroll
    for (int jj = 0; jj < 2; ++jj) {
      if (lg == 3) Q[jj] = (bf16x8){0, 0, 0, 0, 0, 0, 0, 0};
#pragma unroll
      for (int kt = 0; kt < 2; ++kt) S[jj][kt] = MFMA(K[jj][kt], Q[jj], ((f32x4){0.f, 0.f, 0.f, 0.f}));
    }
    bf16x8 P[2];
#pragma unroll
    for (int jj = 0; jj < 2; ++jj) {
      float l[8];
#pragma unroll
      for (int e = 0; e < 8; ++e) l[e] = S[jj][e >> 2][e & 3] + mbias[e];
      if (p == 0 && w == 0 && jj == 0) {
#pragma unroll
        for (int e = 0; e < 4; ++e) l[e] = -__builtin_inff();
      }
      float mx = fmaxf(fmaxf(fmaxf(l[0], l[1]), fmaxf(l[2], l[3])), fmaxf(fmaxf(l[4], l[5]), fmaxf(l[6], l[7])));
      mx = xmax16(mx); mx = xmax32(mx);
      constexpr float CE = 0.23570226f * 1.44269504f;
      const float nm = -mx * CE;
      float sum = 0.f;
#pragma unroll
      for (int e = 0; e < 8; ++e) { l[e] = __builtin_amdgcn_exp2f(l[e] * CE + nm); sum += l[e]; }
      sum = xadd16(sum); sum = xadd32(sum);
      const float inv = __builtin_amdgcn_rcpf(sum);
      u32x4 pk = {pk_bf16(l[0] * inv, l[1] * inv), pk_bf16(l[2] * inv, l[3] * inv), pk_bf16(l[4] * inv, l[5] * inv), pk_bf16(l[6] * inv, l[7] * inv)};
      P[jj] = __builtin_bit_cast(bf16x8, pk);
    }
    asm volatile("s_waitcnt lgkmcnt(0)" : "+v"(V[0][0][0]), "+v"(V[0][0][1]), "+v"(V[0][1][0]), "+v"(V[0][1][1]), "+v"(V[1][0][0]), "+v"(V[1][0][1]), "+v"(V[1][1][0]), "+v"(V[1][1][1]) :: "memory");
#pragma unroll
    for (int jj = 0; jj < 2; ++jj) {
      f32x4 O[2];
#pragma unroll
      for (int dt = 0; dt < 2; ++dt) {
        u32x4 vv = {V[jj][dt][0][0], V[jj][dt][0][1], V[jj][dt][1][0], V[jj][dt][1][1]};
        O[dt] = MFMA(__builtin_bit_cast(bf16x8, vv), P[jj], ((f32x4){0.f, 0.f, 0.f, 0.f}));
      }
      const int tl = jj * 16 + li, tt = p * 8 + w * 2 + ((tl >> 2) & 1), slot = (tl >> 3) * 4 + (tl & 3);
      char* ob = ws + OFF_OT + (size_t)tt * 18432 + slot * 8;
      const int d0 = cg * 18 + lg * 4, d1 = d0 + 2, d2 = cg * 18 + 16;
      *(unsigned*)(ob + (d0 >> 2) * 128 + (d0 & 3) * 2) = pk_bf16(O[0][0], O[0][1]);
      *(unsigned*)(ob + (d1 >> 2) * 128 + (d1 & 3) * 2) = pk_bf16(O[0][2], O[0][3]);
      if (lg == 0) *(unsigned*)(ob + (d2 >> 2) * 128 + (d2 & 3) * 2) = pk_bf16(O[1][0], O[1][1]);
    }
  }
  SCHED();
  PRIO_LO();

  iter(IC<9>{}); iter(IC<10>{}); iter(IC<11>{}); iter(IC<12>{}); iter(IC<13>{}); iter(IC<14>{}); iter(IC<15>{}); iter(IC<16>{}); iter(IC<17>{});
  KA_ACC_DONE();

  SCHED();
  if constexpr (FAST) {
    const unsigned dump = RFL(ldsb + KA_ATT + 2048 + (unsigned)w * 256u);
    {
      const rsrc_t rS = mk_rsrc(wsp, 2048 * 4096);
      const int ks = (sj * 4 + w) & 63, tp = (sj * 4 + w) >> 6, r = (w & 1) * 64 + lane;
      const unsigned vo = (unsigned)prow(tp * 8 + (r >> 4), r & 15) * 4096u + (unsigned)(ks >> 1) * 128u;
      asm volatile("s_mov_b32 m0, %0\n\ts_nop 0\n\tbuffer_load_dword %1, %2, 0 offen" PFA_POL " lds" :: "s"(dump), "v"(vo), "s"(rS) : "memory");
    }
    if (sj < 90) {
      const int base0 = wc_krow(sj, 0, 0), base1 = wc_krow(sj, 0, 4);
      const rsrc_t rS = sj < 72 ? mk_rsrc(wout, 2304 * 1152) : mk_rsrc(wo - (size_t)2304 * 576, 2880 * 1152);
      const int c = tid, r = c / 9, l = c - r * 9;
      const unsigned vo = (unsigned)((r < 16 ? base0 : base1 - 16) + r) * 1152u + (unsigned)l * 128u;
      asm volatile("s_mov_b32 m0, %0\n\ts_nop 0\n\tbuffer_load_dword %1, %2, 0 offen" PFA_POL " lds" :: "s"(dump), "v"(vo), "s"(rS) : "memory");
      if (w == 0) {
        const int c2 = 256 + (lane & 31), r2 = c2 / 9, l2 = c2 - r2 * 9;
        const unsigned vo2 = (unsigned)(base1 - 16 + r2) * 1152u + (unsigned)l2 * 128u;
        asm volatile("s_mov_b32 m0, %0\n\ts_nop 0\n\tbuffer_load_dword %1, %2, 0 offen" PFA_POL " lds" :: "s"(dump), "v"(vo2), "s"(rS) : "memory");
      }
    }
  }
  SCHED();
  PRIO_HI();
  unsigned zs[C::NZ2][4];
  {
#pragma unroll
    for (int n = 0; n < C::NZ2; ++n) {
#pragma unroll
      for (int m = 0; m < 2; ++m) {
        f32x2 a = gelu2((f32x2){acc[m][n][0], acc[m][n][1]}), b = gelu2((f32x2){acc[m][n][2], acc[m][n][3]});
        zs[n][2 * m] = pk_bf16(a[0], a[1]); zs[n][2 * m + 1] = pk_bf16(b[0], b[1]);
      }
    }
    f32x2 s1[4], s2[4];
#pragma unroll
    for (int j = 0; j < 4; ++j) {
      s1[j] = (f32x2){0.f, 0.f}; s2[j] = s1[j];
#pragma unroll
      for (int n = 0; n < C::NZ2; ++n) { f32x2 v = {bf_lo(zs[n][j]), bf_hi(zs[n][j])}; s1[j] += v; s2[j] += v * v; }
    }
    float r[16];
#pragma unroll
    for (int j = 0; j < 4; ++j) { r[2 * j] = s1[j][0]; r[2 * j + 1] = s1[j][1]; r[8 + 2 * j] = s2[j][0]; r[9 + 2 * j] = s2[j][1]; }
#pragma unroll
    for (int j = 0; j < 16; ++j) { float v = r[j]; v = dpp_add<0x128>(v); v = dpp_add<0x124>(v); v = dpp_add<0x122>(v); v = dpp_add<0x121>(v); r[j] = v; }
    float sel = r[0];
#pragma unroll
    for (int j = 1; j < 16; ++j) sel = (li == j) ? r[j] : sel;
    double* dst = (double*)(ws + OFF_RS) + (size_t)(p * 128 + w * 32 + lg * 8 + (li & 7)) * 2 + (li >> 3);
    double dv = (double)sel;
    asm volatile("global_atomic_add_f64 %0, %1, off sc1" :: "v"(dst), "v"(dv) : "memory");
  }
  SCHED();

  PRIO_LO();

  unsigned* cnt = (unsigned*)(ws + OFF_CNT + p * 256);
  iter(IC<18>{}); iter(IC<19>{});
  step(IC<20>{}, IC<0>{}); sync(IC<20>{});
  if (tid == 0) { unsigned one = 1; asm volatile("global_atomic_add %0, %1, off sc1" :: "v"(cnt), "v"(one) : "memory"); }
  step(IC<20>{}, IC<1>{});
  iter(IC<21>{}); iter(IC<22>{}); iter(IC<23>{}); iter(IC<24>{}); iter(IC<25>{});
  unsigned seen;
  asm volatile("global_load_dword %0, %1, off sc1" : "=v"(seen) : "v"(cnt) : "memory");
  iter(IC<26>{});
  KA_ACC_DONE();
  asm volatile("s_waitcnt vmcnt(0)" : "+v"(seen) :: "memory");
  PRIO_HI();
  const unsigned seen_s = RFL(seen);
  const bool early = seen_s >= 32u;
  const char* sp = ws + OFF_RS + (size_t)(p * 128 + w * 32 + (lane & 31)) * 16;
  u32x4 st = {0, 0, 0, 0};
  asm volatile("s_cmp_lt_u32 %2, 32\n\ts_cbranch_scc1 1f\n\tglobal_load_dwordx4 %0, %1, off sc1\n1:" : "+v"(st) : "v"(sp), "s"(seen_s) : "memory", "scc");
  SCHED();
  u32x4 wt[8];
  if constexpr (FAST) {
    const int f0 = (sj * 4 + w) * 8, ks = (f0 >> 3) & 63, tp = f0 >> 9;
#pragma unroll
    for (int m = 0; m < 8; ++m) wt[m] = SJLD((const u32x4*)(wsp + (size_t)prow(tp * 8 + m, li) * 2048 + ks * 32 + lg * 8));
  }
#pragma unroll
  for (int m = 0; m < 2; ++m) {
    char* zp = ws + OFF_Z1T + ((size_t)(p * 8 + w * 2 + m) * 16 + fb) * 4608;
    unsigned pk[C::NZ1][2];
#pragma unroll
    for (int n = 0; n < C::NZ1; ++n) {
      f32x2 a = gelu2((f32x2){acc[m][n][0], acc[m][n][1]}), b = gelu2((f32x2){acc[m][n][2], acc[m][n][3]});
      pk[n][0] = pk_bf16(a[0], a[1]); pk[n][1] = pk_bf16(b[0], b[1]);
    }
#pragma unroll
    for (int q = 0; q < 2; ++q) st16(zp + (FAST * 2 + q) * 1024 + vl16, (u32x4){pk[2 * q][0], pk[2 * q][1], pk[2 * q + 1][0], pk[2 * q + 1][1]});
    if constexpr (FAST) *(u32x2*)(zp + 4096 + lane * 8) = (u32x2){pk[4][0], pk[4][1]};
  }
  SCHED();
  if constexpr (FAST) {
    const int f0 = (sj * 4 + w) * 8;
#pragma unroll
    for (int m = 0; m < 8; ++m) st16(ws + OFF_WT + (size_t)(f0 + m) * 1024 + vl16, wt[m]);
    if (sj < 90) {
      const int ks = sj, base0 = wc_krow(ks, 0, 0), base1 = wc_krow(ks, 0, 4);
      u32x4 v[9];
#pragma unroll
      for (int k = 0; k < 9; ++k) {
        int c = tid + k * 256, r = c / 72, ch = c - r * 72, row = (r < 16 ? base0 : base1 - 16) + r;
        const u16* src = row < 2304 ? wout + (size_t)row * 576 : wo + (size_t)(row - 2304) * 576;
        v[k] = SJLD((const u32x4*)(src + ch * 8));
      }
      LBAR();
#pragma unroll
      for (int k = 0; k < 9; ++k) { int c = tid + k * 256, r = c / 72, ch = c - r * 72; *(u32x4*)(smem + r * K0_PITCH + ch * 16) = v[k]; }
      LBAR();
      const unsigned a0 = ldsb + (4 * lg + (li >> 2)) * K0_PITCH + (li & 3) * 8;
#pragma unroll
      for (int e = 0; e < 10; ++e) {
        int f = w * 10 + e, ct = f / 5, n = f - ct * 5, d0 = (ct < 7 ? ct * 72 : 496) + n * 16;
        u32x4 o = tr_frag(a0 + d0 * 2, a0 + d0 * 2 + 16 * K0_PITCH);
        st16(ws + OFF_WC + (size_t)((ct * 90 + ks) * 5 + n) * 1024 + vl16, o);
      }
    }
  }
  if (!early) {
    while (RFL(seen) < 32u) {
      __builtin_amdgcn_s_sleep(KA_SLEEP);
      asm volatile("global_load_dword %0, %1, off sc1\n\ts_waitcnt vmcnt(0)" : "=v"(seen) : "v"(cnt) : "memory");
    }
  }
  asm volatile("s_cmp_ge_u32 %2, 32\n\ts_cbranch_scc1 1f\n\tglobal_load_dwordx4 %0, %1, off sc1\n1:\n\ts_waitcnt vmcnt(0)" : "+v"(st) : "v"(sp), "s"(seen_s) : "memory", "scc");
  {
    const double sm = __builtin_bit_cast(double, (u32x2){st[0], st[1]}), sq = __builtin_bit_cast(double, (u32x2){st[2], st[3]});
    const double mean = sm * (1.0 / 2304.0);
    const float var = fmaxf((float)(sq * (1.0 / 2304.0) - mean * mean), 0.f);
    const float mb = rbf((float)mean), rs = 1.f / rbf(sqrtf(rbf(rbf(var) + 1e-5f)));
    f32x2* sl = (f32x2*)(smem + KA_ATT + w * 256);
    if (lane < 32) sl[lane] = (f32x2){mb, rs};
    __builtin_amdgcn_s_waitcnt(0xC07F);
    __builtin_amdgcn_wave_barrier();
    f32x4 ms[4];
#pragma unroll
    for (int j = 0; j < 4; ++j) ms[j] = *(const f32x4*)((const char*)sl + lg * 64 + j * 16);
    char* zp = ws + OFF_Z2T + ((size_t)(fb * 64 + p * 4 + w) * 9 + (FAST ? 0 : 5)) * 1024 + vl16;
#pragma unroll
    for (int n = 0; n < C::NZ2; ++n) {
      u32x4 o;
#pragma unroll
      for (int j = 0; j < 4; ++j) o[j] = pk_bf16((bf_lo(zs[n][j]) - ms[j][0]) * ms[j][1], (bf_hi(zs[n][j]) - ms[j][2]) * ms[j][3]);
      st16(zp + n * 1024, o);
    }
  }
}
__global__ __launch_bounds__(256, 2) void kA(const char* __restrict__ ws_c, char* __restrict__ ws, const u16* __restrict__ wsp, const u16* __restrict__ wout, const u16* __restrict__ wo) {
  extern __shared__ __attribute__((aligned(16))) char smem[];
  const int b = blockIdx.x, xcd = b & 7, idx = b >> 3;
  const int p = (xcd & 1) * 8 + (idx & 7), q = idx >> 3, cg = (xcd >> 1) * 8 + (q & 3) * 2 + (q >> 2);
  if (idx < 32) ka_body<1>(smem, ws_c, ws, p, cg, b, wsp, wout, wo); else ka_body<0>(smem, ws_c, ws, p, cg, b, wsp, wout, wo);
}

constexpr int KB_STAGE = 36864;
constexpr int KB_LDS = 4 * KB_STAGE;
__global__ __launch_bounds__(512, 2) void kB(const char* __restrict__ ws_c, char* __restrict__ ws, const u16* __restrict__ bsp) {
  extern __shared__ __attribute__((aligned(16))) char smem[];
  const int tid = threadIdx.x, lane = tid & 63;
  const int w = RFL(tid >> 6), wm = w & 3, kh = w >> 2;
  const int b = blockIdx.x, xcd = b & 7, idx = b >> 3;
  const int tp = (xcd & 1) * 8 + (idx & 7), fb = (xcd >> 1) * 4 + (idx >> 3);
  const rsrc_t rZ = mk_rsrc(ws_c + OFF_Z2T + (size_t)fb * (64 * 9216), 64 * 9216);
  const rsrc_t rW = mk_rsrc(ws_c + OFF_WT + (size_t)tp * (64 * 8192), 64 * 8192);
  const unsigned ldsb = (unsigned)(size_t)(__attribute__((address_space(3))) char*)smem;
  const unsigned vl16 = lane * 16;
  const unsigned vW = vl16 + wm * 2048 + kh * 16384;
  const unsigned dmaF = w * 4096, dmaH = 32768 + w * 512;
  unsigned rd01 = ldsb + kh * 18432 + lane * 16;
  unsigned rd23 = rd01 + 2 * KB_STAGE;

  f32x4 acc[2][9];
#pragma unroll
  for (int m = 0; m < 2; ++m)
#pragma unroll
    for (int n = 0; n < 9; ++n) acc[m][n] = (f32x4){0.f, 0.f, 0.f, 0.f};
  bf16x8 Wr[4][4];
  bf16x8 ZA[9], ZB[9];

#define KB_DMA1(stage, slot, imm) do { \
    unsigned so_ = RFL((unsigned)(stage) * KB_STAGE + dmaF), lo_ = RFL(ldsb + (slot) * KB_STAGE + dmaF); DMA16(lo_, vl16, rZ, so_, imm); } while (0)
#define KB_DMAH(stage, slot) do { \
    unsigned so2_ = RFL((unsigned)(stage) * KB_STAGE + dmaH), lo2_ = RFL(ldsb + (slot) * KB_STAGE + dmaH); DMA16H(lo2_, vl16, rZ, so2_); } while (0)
#define KB_W1(stage, slot, e) do { unsigned sw_ = RFL((unsigned)(stage) * 32768u + ((e) >> 1) * 8192u); \
    if ((e) & 1) BLOAD16(Wr[slot][e], vW, rW, sw_, 1024); else BLOAD16(Wr[slot][e], vW, rW, sw_, 0); } while (0)
#define KB_ALL(stage, slot) do { KB_DMA1(stage, slot, 0); KB_DMA1(stage, slot, 1024); KB_DMA1(stage, slot, 2048); KB_DMA1(stage, slot, 3072); KB_DMAH(stage, slot); \
    KB_W1(stage, slot, 0); KB_W1(stage, slot, 1); KB_W1(stage, slot, 2); KB_W1(stage, slot, 3); } while (0)
#define KB_WAIT(slot, N) asm volatile("s_waitcnt vmcnt(" #N ") lgkmcnt(0)" : "+v"(Wr[slot][0]), "+v"(Wr[slot][1]), "+v"(Wr[slot][2]), "+v"(Wr[slot][3]) :: "memory")

  KB_ALL(0, 0);
  __builtin_amdgcn_s_barrier();
  KB_ALL(1, 1);
  KB_WAIT(0, 9);
  __builtin_amdgcn_s_barrier();

  const rsrc_t rZ1 = mk_rsrc(ws_c + OFF_Z1T + ((size_t)(tp * 8 + wm * 2) * 16 + fb) * 4608, 17 * 4608);
  const rsrc_t rB = mk_rsrc(bsp, 4096);
  u32x4 z1q[2][2]; u32x2 z1s[2]; unsigned biasu[2];
  const unsigned vz1 = lane * 16 + kh * 2048, vz8 = lane * 8 + 4096;
  const unsigned vb = (tp * 128 + wm * 32 + ((lane & 15) >> 2) * 8 + (lane & 3)) * 2;
#define KB_EPI_LD(e) do { \
    if ((e) == 0) BLOAD16(z1q[0][0], vz1, rZ1, 0, 0); \
    if ((e) == 1) BLOAD16(z1q[0][1], vz1, rZ1, 0, 1024); \
    if ((e) == 2) asm volatile("buffer_load_dwordx4 %0, %1, %2, %3 offen" : "=v"(z1q[1][0]) : "v"(vz1), "s"(rZ1), "s"(16 * 4608)); \
    if ((e) == 3) asm volatile("buffer_load_dwordx4 %0, %1, %2, %3 offen offset:1024" : "=v"(z1q[1][1]) : "v"(vz1), "s"(rZ1), "s"(16 * 4608)); \
    if ((e) == 4) asm volatile("buffer_load_dwordx2 %0, %1, %2, 0 offen" : "=v"(z1s[0]) : "v"(vz8), "s"(rZ1)); \
    if ((e) == 5) asm volatile("buffer_load_dwordx2 %0, %1, %2, %3 offen" : "=v"(z1s[1]) : "v"(vz8), "s"(rZ1), "s"(16 * 4608)); \
    if ((e) == 6) asm volatile("buffer_load_ushort %0, %1, %2, 0 offen" : "=v"(biasu[0]) : "v"(vb), "s"(rB)); \
    if ((e) == 7) asm volatile("buffer_load_ushort %0, %1, %2, 0 offen offset:8" : "=v"(biasu[1]) : "v"(vb), "s"(rB)); } while (0)

#define KB_ITER(i, j, MODE, WN) do { \
    _Pragma("unroll") for (int n = 0; n < 9; ++n) { \
      ADSRD(ZA[n], ((j) < 2 ? rd01 : rd23), ((j) & 1) * KB_STAGE + n * 1024); \
      if ((MODE) != 1) { AMFMA(acc[0][n], ZB[n], Wr[((j) + 3) & 3][2]); AMFMA(acc[1][n], ZB[n], Wr[((j) + 3) & 3][3]); } \
      if ((MODE) == 0) { \
        if (n == 0) KB_DMA1((i) + 3, ((j) + 3) & 3, 0); \
        if (n == 2) KB_DMA1((i) + 3, ((j) + 3) & 3, 1024); \
        if (n == 4) KB_W1((i) + 3, ((j) + 3) & 3, 0); \
        if (n == 6) KB_DMA1((i) + 3, ((j) + 3) & 3, 2048); \
        if (n == 8) KB_W1((i) + 3, ((j) + 3) & 3, 1); } \
      if ((MODE) == 2 && !(n & 1)) KB_EPI_LD(n >> 1); } \
    if ((MODE) == 1) { KB_ALL(2, 2); KB_DMA1(3, 3, 0); KB_DMA1(3, 3, 1024); KB_W1(3, 3, 0); KB_DMA1(3, 3, 2048); KB_W1(3, 3, 1); } \
    _Pragma("unroll") for (int n = 0; n < 9; ++n) { \
      if (n == 0) asm volatile("s_waitcnt lgkmcnt(6)" ::: "memory"); \
      if (n == 2) asm volatile("s_waitcnt lgkmcnt(5)" ::: "memory"); \
      if (n == 5) asm volatile("s_waitcnt lgkmcnt(5)" ::: "memory"); \
      ADSRD(ZB[n], ((j) < 2 ? rd01 : rd23), ((j) & 1) * KB_STAGE + 9216 + n * 1024); \
      AMFMA(acc[0][n], ZA[n], Wr[j][0]); AMFMA(acc[1][n], ZA[n], Wr[j][1]); \
      if ((MODE) < 2) { \
        if (n == 0) KB_DMA1((i) + 3, ((j) + 3) & 3, 3072); \
        if (n == 2) KB_W1((i) + 3, ((j) + 3) & 3, 2); \
        if (n == 4) KB_DMAH((i) + 3, ((j) + 3) & 3); \
        if (n == 6) KB_W1((i) + 3, ((j) + 3) & 3, 3); } \
      if ((MODE) == 2 && (n == 0 || n == 2 || n == 4)) KB_EPI_LD(5 + (n >> 1)); } \
    KB_WAIT(((j) + 1) & 3, WN); \
    __builtin_amdgcn_s_barrier(); \
    } while (0)

  KB_ITER(0, 0, 1, 18);
  KB_ITER(1, 1, 0, 18);
  KB_ITER(2, 2, 0, 18);
  KB_ITER(3, 3, 0, 18);
  for (int io = 4; io < 12; io += 4) {
    KB_ITER(io + 0, 0, 0, 18);
    KB_ITER(io + 1, 1, 0, 18);
    KB_ITER(io + 2, 2, 0, 18);
    KB_ITER(io + 3, 3, 0, 18);
  }
  KB_ITER(12, 0, 0, 18);
  KB_ITER(13, 1, 2, 17);
  KB_ITER(14, 2, 3, 8);
  {
#pragma unroll
    for (int n = 0; n < 9; ++n) { ADSRD(ZA[n], rd23, KB_STAGE + n * 1024); AMFMA(acc[0][n], ZB[n], Wr[2][2]); AMFMA(acc[1][n], ZB[n], Wr[2][3]); }
#pragma unroll
    for (int n = 0; n < 9; ++n) {
      if (n == 0) asm volatile("s_waitcnt lgkmcnt(6)" ::: "memory");
      if (n == 2) asm volatile("s_waitcnt lgkmcnt(5)" ::: "memory");
      if (n == 5) asm volatile("s_waitcnt lgkmcnt(5)" ::: "memory");
      ADSRD(ZB[n], rd23, KB_STAGE + 9216 + n * 1024);
      AMFMA(acc[0][n], ZA[n], Wr[3][0]); AMFMA(acc[1][n], ZA[n], Wr[3][1]); }
    asm volatile("s_waitcnt lgkmcnt(0)" ::: "memory");
#pragma unroll
    for (int n = 0; n < 9; ++n) { AMFMA(acc[0][n], ZB[n], Wr[3][2]); AMFMA(acc[1][n], ZB[n], Wr[3][3]); }
  }
  asm volatile("s_nop 15" : "+v"(acc[0][0]), "+v"(acc[0][1]), "+v"(acc[0][2]), "+v"(acc[0][3]), "+v"(acc[0][4]), "+v"(acc[0][5]), "+v"(acc[0][6]), "+v"(acc[0][7]), "+v"(acc[0][8]),
               "+v"(acc[1][0]), "+v"(acc[1][1]), "+v"(acc[1][2]), "+v"(acc[1][3]), "+v"(acc[1][4]), "+v"(acc[1][5]), "+v"(acc[1][6]), "+v"(acc[1][7]), "+v"(acc[1][8]) :: "memory");
  asm volatile("s_waitcnt vmcnt(0)" : "+v"(z1q[0][0]), "+v"(z1q[0][1]), "+v"(z1q[1][0]), "+v"(z1q[1][1]), "+v"(z1s[0]), "+v"(z1s[1]), "+v"(biasu[0]), "+v"(biasu[1]) :: "memory");
  SCHED();
  char* hp = ws + OFF_HT + ((size_t)(tp * 8 + wm * 2) * 16 + fb) * 4608;
  float bias[2] = {bf_lo(biasu[0]), bf_lo(biasu[1])};
  char* ex = smem + wm * 18432 + lane * 16;
  if (kh == 0) {
#pragma unroll
    for (int m = 0; m < 2; ++m)
#pragma unroll
      for (int n = 4; n < 8; ++n) *(f32x4*)(ex + (m * 9 + n) * 1024) = acc[m][n];
  } else {
#pragma unroll
    for (int m = 0; m < 2; ++m) {
#pragma unroll
      for (int n = 0; n < 4; ++n) *(f32x4*)(ex + (m * 9 + n) * 1024) = acc[m][n];
      *(f32x4*)(ex + (m * 9 + 8) * 1024) = acc[m][8];
    }
  }
  __syncthreads();
#pragma unroll
  for (int m = 0; m < 2; ++m) {
#pragma unroll
    for (int q = 0; q < 2; ++q) {
      f32x4 g0, g1;
      if (kh == 0) { g0 = acc[m][2 * q] + *(const f32x4*)(ex + (m * 9 + 2 * q) * 1024); g1 = acc[m][2 * q + 1] + *(const f32x4*)(ex + (m * 9 + 2 * q + 1) * 1024); }
      else { g0 = acc[m][4 + 2 * q] + *(const f32x4*)(ex + (m * 9 + 4 + 2 * q) * 1024); g1 = acc[m][5 + 2 * q] + *(const f32x4*)(ex + (m * 9 + 5 + 2 * q) * 1024); }
      u32x4 z = z1q[m][q], o;
      o[0] = pk_bf16(bf_lo(z[0]) * (g0[0] + bias[m]), bf_hi(z[0]) * (g0[1] + bias[m]));
      o[1] = pk_bf16(bf_lo(z[1]) * (g0[2] + bias[m]), bf_hi(z[1]) * (g0[3] + bias[m]));
      o[2] = pk_bf16(bf_lo(z[2]) * (g1[0] + bias[m]), bf_hi(z[2]) * (g1[1] + bias[m]));
      o[3] = pk_bf16(bf_lo(z[3]) * (g1[2] + bias[m]), bf_hi(z[3]) * (g1[3] + bias[m]));
      st16(hp + m * (16 * 4608) + (kh * 2 + q) * 1024 + lane * 16, o);
    }
    if (kh == 0) {
      f32x4 g0 = acc[m][8] + *(const f32x4*)(ex + (m * 9 + 8) * 1024);
      u32x2 z = z1s[m], o;
      o[0] = pk_bf16(bf_lo(z[0]) * (g0[0] + bias[m]), bf_hi(z[0]) * (g0[1] + bias[m]));
      o[1] = pk_bf16(bf_lo(z[1]) * (g0[2] + bias[m]), bf_hi(z[1]) * (g0[3] + bias[m]));
      *(u32x2*)(hp + m * (16 * 4608) + 4096 + lane * 8) = o;
    }
  }
}

constexpr int KC_PITCH = 336;
constexpr int KC_PART = 64 * KC_PITCH;
constexpr int KC_LDS = 5 * KC_PART;
template <bool ISO>
__device__ __forceinline__ void kc_main(f32x4 (&acc)[4][5], rsrc_t rA, rsrc_t rW, unsigned vA16, unsigned vA8, unsigned vW, unsigned sA, unsigned sW) {
  constexpr unsigned TS = ISO ? 18432u : 16u * 4608u;
  constexpr int PD = 3;
  bf16x8 A[PD + 1][4], W[PD + 1][5];
  auto load = [&](int u, int slot) {
#pragma unroll
    for (int m = 0; m < 4; ++m) {
      if (ISO || (u % 9) == 4) {
        unsigned o0, o1;
        if (ISO) { o0 = sA + m * TS + u * 1024; o1 = o0 + 512; }
        else { o0 = sA + m * TS + (u / 9) * 2 * 4608 + 4096; o1 = o0 + 4608; }
        u32x2 lo = __builtin_amdgcn_raw_buffer_load_b64(rA, vA8, o0, 0);
        u32x2 hi = __builtin_amdgcn_raw_buffer_load_b64(rA, vA8, o1, 0);
        u32x4 v = {lo[0], lo[1], hi[0], hi[1]};
        A[slot][m] = __builtin_bit_cast(bf16x8, v);
      } else {
        int i = u % 9;
        unsigned o = sA + m * TS + (u / 9) * 2 * 4608 + (i < 4 ? i * 1024 : 4608 + (i - 5) * 1024);
        A[slot][m] = __builtin_bit_cast(bf16x8, __builtin_amdgcn_raw_buffer_load_b128(rA, vA16, o, 0));
      }
    }
#pragma unroll
    for (int n = 0; n < 5; ++n) W[slot][n] = __builtin_bit_cast(bf16x8, __builtin_amdgcn_raw_buffer_load_b128(rW, vW, sW + (u * 5 + n) * 1024, 0));
  };
#pragma unroll
  for (int u = 0; u < PD; ++u) load(u, u);
  SCHED();
#pragma unroll
  for (int u = 0; u < 18; ++u) {
    if (u + PD < 18) load(u + PD, (u + PD) % (PD + 1));
    SCHED();
#pragma unroll
    for (int m = 0; m < 4; ++m)
#pragma unroll
      for (int n = 0; n < 5; ++n) acc[m][n] = MFMA(W[u % (PD + 1)][n], A[u % (PD + 1)][m], acc[m][n]);
    SCHED();
  }
}
__global__ __launch_bounds__(320, 2) void kC(const char* __restrict__ ws_c, const u16* __restrict__ x, u16* __restrict__ out) {
  extern __shared__ __attribute__((aligned(16))) char smem[];
  const int tid = threadIdx.x, lane = tid & 63;
  const int w = RFL(tid >> 6);
  const int b = blockIdx.x, xcd = b & 7, idx = b >> 3;
  const int rt = (xcd & 3) * 8 + (idx & 7), ct = (xcd >> 2) * 4 + (idx >> 3);
  f32x4 acc[4][5];
#pragma unroll
  for (int m = 0; m < 4; ++m)
#pragma unroll
    for (int n = 0; n < 5; ++n) acc[m][n] = (f32x4){0.f, 0.f, 0.f, 0.f};
  const rsrc_t rW = mk_rsrc(ws_c + OFF_WC, (int)SZ_WC);
  const unsigned sW = RFL((unsigned)((ct * 90 + w * 18) * 5 * 1024));
  const int dstart = ct < 7 ? ct * 72 : 496;
  u32x4 xv[2]; unsigned gof[2];
#pragma unroll
  for (int e = 0; e < 2; ++e) {
    int c = tid + e * 320; c = c < 576 ? c : tid;
    const int r = c / 9, ch = c - r * 9;
    gof[e] = (unsigned)(rt * 64 + prow(r >> 4, r & 15)) * 1152u + (unsigned)(ct * 72 + ch * 8) * 2u;
    asm volatile("global_load_dwordx4 %0, %1, %2" : "=v"(xv[e]) : "v"(gof[e]), "s"(x));
  }
  if (w < 4) {
    const rsrc_t rA = mk_rsrc(ws_c + OFF_HT, (int)SZ_Z);
    kc_main<false>(acc, rA, rW, lane * 16, lane * 8, lane * 16, RFL((unsigned)((rt * 4 * 16 + w * 4) * 4608)), sW);
  } else {
    const rsrc_t rA = mk_rsrc(ws_c + OFF_OT, (int)SZ_OT);
    kc_main<true>(acc, rA, rW, lane * 16, lane * 8, lane * 16, RFL((unsigned)(rt * 4 * 18432)), sW);
  }
  {
    char* p = smem + w * KC_PART + (lane & 15) * KC_PITCH + (lane >> 4) * 16;
#pragma unroll
    for (int m = 0; m < 4; ++m)
#pragma unroll
      for (int n = 0; n < 5; ++n) *(f32x4*)(p + m * 16 * KC_PITCH + n * 64) = acc[m][n];
  }
  asm volatile("s_waitcnt vmcnt(0) lgkmcnt(0)\n\ts_barrier" : "+v"(xv[0]), "+v"(xv[1]) :: "memory");
  const int own0 = ct * 72 - dstart;
#pragma unroll
  for (int e = 0; e < 2; ++e) {
    const int c = tid + e * 320;
    if (c < 576) {
      const int r = c / 9, ch = c - r * 9;
      const char* p = smem + r * KC_PITCH + (own0 + ch * 8) * 4;
      f32x4 s0 = *(const f32x4*)p, s1 = *(const f32x4*)(p + 16);
#pragma unroll
      for (int ww = 1; ww < 5; ++ww) { s0 += *(const f32x4*)(p + ww * KC_PART); s1 += *(const f32x4*)(p + ww * KC_PART + 16); }
      u32x4 o;
      o[0] = pk_bf16(s0[0] + bf_lo(xv[e][0]), s0[1] + bf_hi(xv[e][0]));
      o[1] = pk_bf16(s0[2] + bf_lo(xv[e][1]), s0[3] + bf_hi(xv[e][1]));
      o[2] = pk_bf16(s1[0] + bf_lo(xv[e][2]), s1[1] + bf_hi(xv[e][2]));
      o[3] = pk_bf16(s1[2] + bf_lo(xv[e][3]), s1[3] + bf_hi(xv[e][3]));
      st16((char*)out + gof[e], o);
    }
  }
}

constexpr int K0_BLOCKS = 280;
#define K0LD(p) __builtin_nontemporal_load(p)
#define XLD(p) (*(p))
constexpr int PF0_BYTES = 12288;
#define PF_CODE_N 16
#define PF_TLB_N 16
#define PF_TLB_STRIDE 65536
__global__ __launch_bounds__(256) void k0(const u16* __restrict__ x, const u16* __restrict__ wqkv, const u16* __restrict__ win, char* __restrict__ ws, const u16* __restrict__ wsp, const u16* __restrict__ wout, const u16* __restrict__ wo, const u16* __restrict__ bsp) {
  __shared__ __attribute__((aligned(16))) char sm[32 * K0_PITCH];
  const int tid = threadIdx.x, lane = tid & 63, i = lane & 15, g = lane >> 4, w = tid >> 6, b = blockIdx.x;
  const unsigned smb = (unsigned)(size_t)(__attribute__((address_space(3))) char*)sm;
  unsigned pf0 = 0, pf1 = 0, pf2 = 0;
  if (w == 3 && (b >> 3) < PF_CODE_N) {
    const int u = b >> 3;
    unsigned long long pa, p0;
    asm volatile("s_getpc_b64 s[20:21]\n\ts_add_u32 s20, s20, _Z2kAPKcPcPKtS3_S3_@rel32@lo+4\n\ts_addc_u32 s21, s21, _Z2kAPKcPcPKtS3_S3_@rel32@hi+12\n\ts_mov_b64 %0, s[20:21]" : "=s"(pa) :: "scc", "s20", "s21");
    asm volatile("s_getpc_b64 s[20:21]\n\ts_add_u32 s20, s20, _Z2k0PKtS0_S0_PcS0_S0_S0_S0_@rel32@lo+4\n\ts_addc_u32 s21, s21, _Z2k0PKtS0_S0_PcS0_S0_S0_S0_@rel32@hi+12\n\ts_mov_b64 %0, s[20:21]" : "=s"(p0) :: "scc", "s20", "s21");
    const long long span = (long long)(p0 - pa);
    int off = (u < 2 ? u : u - 2) * 8192 + lane * 128;
    const int lim = u < 2 ? PF0_BYTES : (span > 0 && span < (1 << 20) ? (int)span : 0);
    off = off < lim ? off : 0;
    const char* a = (const char*)(u < 2 ? p0 : pa) + off;
    asm volatile("global_load_dword %0, %1, off" : "=v"(pf0) : "v"(a));
  }
  if (b == 217 && w == 1) { const char* a = (const char*)bsp + (lane & 31) * 128; asm volatile("global_load_dword %0, %1, off" : "=v"(pf1) : "v"(a)); }
  if (b < 144) {
    const int ks = b >> 3, cb = b & 7;
    u32x4 v[9];
#pragma unroll
    for (int k = 0; k < 9; ++k) { int c = tid + k * 256, r = c / 72, ch = c - r * 72; v[k] = K0LD((const u32x4*)(win + (size_t)(ks * 32 + r) * 4608 + cb * 576 + ch * 8)); }
#pragma unroll
    for (int k = 0; k < 9; ++k) { int c = tid + k * 256, r = c / 72, ch = c - r * 72; *(u32x4*)(sm + r * K0_PITCH + ch * 16) = v[k]; }
    __syncthreads();
    const unsigned a0 = smb + (8 * g + (i >> 2)) * K0_PITCH + (i & 3) * 8;
#pragma unroll
    for (int e = 0; e < 9; ++e) {
      int nt = w * 9 + e;
      int gt = cb * 36 + nt;
      int z2 = gt >= 144, t = gt - z2 * 144, fb = t / 9, n = t - fb * 9;
      int fast = z2 ? n < 5 : n >= 4, nz = 4 + fast;
      int fr = fb * 468 + (fast ? 0 : 252) + 72 + (z2 ? ks * nz + n - (fast ? 0 : 5) : 18 * nz + ks * nz + n - (fast ? 4 : 0));
      u32x4 o = tr_frag(a0 + nt * 32, a0 + nt * 32 + 4 * K0_PITCH);
      st16(ws + OFF_W1 + (size_t)fr * 1024 + lane * 16, o);
    }
  } else if (b < 216) {
    const int u = b - 144, ks = u >> 2, hq = u & 3;
    u32x4 v[7]; int dst[7], dw0[7];
#pragma unroll
    for (int k = 0; k < 7; ++k) {
      int c = tid + k * 256; c = c < 32 * 54 ? c : 32 * 54 - 1;
      const int r = c / 54, e = c - r * 54, part = e / 18, ch = e - part * 18;
      v[k] = K0LD((const u32x4*)(wqkv + (size_t)(ks * 32 + r) * 1728 + part * 576 + hq * 144 + ch * 8));
      dst[k] = r * K0_PITCH + (part == 0 ? 80 : (part == 1 ? 0 : 40)); dw0[k] = ch * 4;
    }
#pragma unroll
    for (int z = 0; z < 5; ++z) {
      const int c = tid + z * 256, r = c / 40, e = c - r * 40, hl = e / 5, jj = e - hl * 5;
      *(unsigned*)(sm + r * K0_PITCH + hl * 128 + (jj == 0 ? 9 : (jj == 1 ? 19 : 27 + jj)) * 4) = 0u;
    }
#pragma unroll
    for (int k = 0; k < 7; ++k)
#pragma unroll
      for (int q = 0; q < 4; ++q) { const int dw = dw0[k] + q, hl = (dw * 57) >> 9; *(unsigned*)(sm + dst[k] + hl * 128 + (dw - hl * 9) * 4) = v[k][q]; }
    __syncthreads();
    const unsigned a0 = smb + (8 * g + (i >> 2)) * K0_PITCH + (i & 3) * 8;
#pragma unroll
    for (int e = 0; e < 8; ++e) {
      const int f = w * 8 + e, hl = f >> 2, n = f & 3;
      u32x4 o = tr_frag(a0 + f * 32, a0 + f * 32 + 4 * K0_PITCH);
      st16(ws + OFF_W1 + (size_t)(((hq * 8 + hl) >> 1) * 468 + (hl & 1) * 252 + ks * 4 + n) * 1024 + lane * 16, o);
    }
  } else {
    static_assert((2048 * 16 + 16 * 256) / 16 == 64 * 36, "stat words");
    if (tid < 36) *(u32x4*)(ws + OFF_RS + ((b - 216) * 36 + tid) * 16) = (u32x4){0, 0, 0, 0};
    const int u = (b - 216) * 4 + w, tt = u >> 1, slot = (u & 1) * 8 + (lane >> 3), c0 = lane & 7;
    const u16* xr = x + (size_t)prow(tt, slot) * 576 + c0 * 8;
    u32x4 v[9];
#pragma unroll
    for (int k = 0; k < 9; ++k) v[k] = XLD((const u32x4*)(xr + k * 64));
    float s = 0.f;
#pragma unroll
    for (int k = 0; k < 9; ++k)
#pragma unroll
      for (int j = 0; j < 4; ++j) s += bf_lo(v[k][j]) + bf_hi(v[k][j]);
    s += __shfl_xor(s, 1); s += __shfl_xor(s, 2); s += __shfl_xor(s, 4);
    const float mean = s * (1.f / 576.f);
    float q = 0.f;
#pragma unroll
    for (int k = 0; k < 9; ++k)
#pragma unroll
      for (int j = 0; j < 4; ++j) { float a = bf_lo(v[k][j]) - mean, c = bf_hi(v[k][j]) - mean; q += a * a + c * c; }
    q += __shfl_xor(q, 1); q += __shfl_xor(q, 2); q += __shfl_xor(q, 4);
    const float mb = rbf(mean), sd = rbf(sqrtf(rbf(rbf(q * (1.f / 576.f)) + 1e-5f))), rs = 1.f / sd;
    char* dst = ws + OFF_XT + (size_t)tt * 18432 + slot * 16;
#pragma unroll
    for (int k = 0; k < 9; ++k) {
      u32x4 o;
#pragma unroll
      for (int j = 0; j < 4; ++j) o[j] = pk_bf16(rbf(bf_lo(v[k][j]) - mb) * rs, rbf(bf_hi(v[k][j]) - mb) * rs);
      int c = c0 + 8 * k;
      st16(dst + (c >> 2) * 1024 + (c & 3) * 256, o);
    }
  }
  asm volatile("s_waitcnt vmcnt(0)" : "+v"(pf0), "+v"(pf1), "+v"(pf2) :: "memory");
}

extern "C" void kernel_launch(void* const* d_in, const int* in_sizes, int n_in, void* d_out, int out_size, void* d_ws, size_t ws_size, hipStream_t stream) {
  (void)in_sizes; (void)n_in; (void)out_size; (void)ws_size;
  const u16* x = (const u16*)d_in[0];
  char* ws = (char*)d_ws;
  (void)hipFuncSetAttribute((const void*)kA, hipFuncAttributeMaxDynamicSharedMemorySize, KA_LDS);
  (void)hipFuncSetAttribute((const void*)kB, hipFuncAttributeMaxDynamicSharedMemorySize, KB_LDS);
  (void)hipFuncSetAttribute((const void*)kC, hipFuncAttributeMaxDynamicSharedMemorySize, KC_LDS);
  k0<<<K0_BLOCKS, 256, 0, stream>>>(x, (const u16*)d_in[1], (const u16*)d_in[4], ws, (const u16*)d_in[5], (const u16*)d_in[7], (const u16*)d_in[2], (const u16*)d_in[6]);
  kA<<<512, 256, KA_LDS, stream>>>(ws, ws, (const u16*)d_in[5], (const u16*)d_in[7], (const u16*)d_in[2]);
  kB<<<256, 512, KB_LDS, stream>>>(ws, ws, (const u16*)d_in[6]);
  kC<<<256, 320, KC_LDS, stream>>>(ws, x, (u16*)d_out);
}
```

```cpp
#include <hip/hip_runtime.h>
#include <hip/hip_bf16.h>
#include <stdint.h>

typedef unsigned short u16;
typedef __attribute__((ext_vector_type(8))) short bf16x8;
typedef __attribute__((ext_vector_type(4))) short bf16x4;
typedef __attribute__((ext_vector_type(4))) float f32x4;
typedef __attribute__((ext_vector_type(4))) unsigned u32x4;
typedef __attribute__((ext_vector_type(2))) unsigned u32x2;
typedef __amdgpu_buffer_rsrc_t rsrc_t;

constexpr int S_ = 2048, D_ = 576, F_ = 4608, FH_ = 2304;
constexpr size_t OFF_Z2T = 0;
constexpr size_t SZ_Z = (size_t)16 * 64 * 9 * 1024;
constexpr size_t OFF_Z1T = OFF_Z2T + SZ_Z;
constexpr size_t OFF_HT = OFF_Z1T + SZ_Z;
constexpr size_t OFF_WT = OFF_HT + SZ_Z;
constexpr size_t SZ_WT = (size_t)16 * 64 * 8 * 1024;
constexpr size_t OFF_OT = OFF_WT + SZ_WT;
constexpr size_t SZ_OT = (size_t)128 * 144 * 128;
constexpr size_t OFF_WC = OFF_OT + SZ_OT;
constexpr size_t SZ_WC = (size_t)8 * 90 * 5 * 1024;
constexpr size_t OFF_XT = OFF_WC + SZ_WC;
constexpr size_t SZ_XT = (size_t)128 * 18 * 1024;
constexpr size_t OFF_W1 = OFF_XT + SZ_XT;
constexpr size_t SZ_W1 = (size_t)32 * 234 * 1024;
constexpr size_t OFF_RS = OFF_W1 + SZ_W1;
constexpr size_t OFF_CNT = OFF_RS + 2048 * 16;
constexpr size_t OFF_ST = OFF_CNT + 16 * 256;
constexpr size_t WS_END = OFF_ST + (1 << 20);

#define RFL(x) __builtin_amdgcn_readfirstlane(x)
#define SCHED() __builtin_amdgcn_sched_barrier(0)
__device__ __forceinline__ rsrc_t mk_rsrc(const void* p, int bytes) { return __builtin_amdgcn_make_buffer_rsrc((void*)p, (short)0, bytes, 0x00020000); }
__device__ __forceinline__ float bf_lo(unsigned w) { return __uint_as_float(w << 16); }
__device__ __forceinline__ float bf_hi(unsigned w) { return __uint_as_float(w & 0xffff0000u); }
__device__ __forceinline__ unsigned pk_bf16(float lo, float hi) {
  typedef __attribute__((ext_vector_type(2))) float f32x2; typedef __attribute__((ext_vector_type(2))) __bf16 bf2;
  f32x2 v = {lo, hi}; bf2 r = __builtin_convertvector(v, bf2); return __builtin_bit_cast(unsigned, r);
}
__device__ __forceinline__ float rbf(float f) { return bf_hi(pk_bf16(0.f, f)); }
__host__ __device__ __forceinline__ int prow(int tt, int i) { return 32 * (tt >> 1) + 8 * (i >> 2) + 4 * (tt & 1) + (i & 3); }
__device__ __forceinline__ void st16(void* p, u32x4 v) { asm volatile("global_store_dwordx4 %0, %1, off sc1\n\ts_nop 1" :: "v"(p), "v"(v) : "memory"); }
template <int IMM> __device__ __forceinline__ void st16s(const char* base, unsigned voff, u32x4 v) {
  asm volatile("global_store_dwordx4 %0, %1, %2 offset:%3 sc1\n\ts_nop 1" :: "v"(voff), "v"(v), "s"(base), "i"(IMM) : "memory"); }
__device__ __forceinline__ void st8(void* p, u32x2 v) { asm volatile("global_store_dwordx2 %0, %1, off sc1\n\ts_nop 1" :: "v"(p), "v"(v) : "memory"); }
__device__ __forceinline__ void st4(void* p, unsigned v) { asm volatile("global_store_dword %0, %1, off sc1\n\ts_nop 1" :: "v"(p), "v"(v) : "memory"); }
#define MFMA(a, b, c) __builtin_amdgcn_mfma_f32_16x16x32_bf16(a, b, c, 0, 0, 0)

#define DMA16(lds, voff, srd, soff, imm) asm volatile("s_mov_b32 m0, %0\n\ts_nop 0\n\tbuffer_load_dwordx4 %1, %2, %3 offen offset:" #imm " lds" :: "s"(lds), "v"(voff), "s"(srd), "s"(soff) : "memory")
#define DMA16H(lds, voff, srd, soff) asm volatile("s_mov_b32 m0, %0\n\ts_lshr_b64 exec, -1, 32\n\tbuffer_load_dwordx4 %1, %2, %3 offen lds\n\ts_mov_b64 exec, -1" :: "s"(lds), "v"(voff), "s"(srd), "s"(soff) : "memory")
typedef __attribute__((address_space(3))) char lds_char;
#define LDS_RD128(addr) (*(const __attribute__((address_space(3))) bf16x8*)(addr))
#define AMFMA(acc, a, b) asm volatile("v_mfma_f32_16x16x32_bf16 %0, %1, %2, %0" : "+v"(acc) : "v"(a), "v"(b))
#define ADSRD(dst, addr, off) asm volatile("ds_read_b128 %0, %1 offset:%2" : "=v"(dst) : "v"(addr), "i"(off))
#define OPAQUE(x) asm volatile("" : "+v"(x))
#define BLOAD16(dst, voff, srd, soff, imm) asm volatile("buffer_load_dwordx4 %0, %1, %2, %3 offen offset:" #imm : "=v"(dst) : "v"(voff), "s"(srd), "s"(soff))

constexpr int K0_PITCH = 1168;
__device__ __forceinline__ int wc_krow(int ks, int g, int j) {
  int r = j & 3, hi = j >> 2;
  if (ks >= 72) { int u = ks - 72; return 2304 + 4 * (8 * u + 4 * hi + g) + r; }
  int w = ks / 18, pi = (ks % 18) / 9, i = ks % 9, fb0 = 4 * w + 2 * pi;
  if (i == 4) return 144 * (fb0 + hi) + 128 + 4 * g + r;
  if (i < 4) return 144 * fb0 + 32 * i + 16 * hi + 4 * g + r;
  return 144 * (fb0 + 1) + 32 * (i - 5) + 16 * hi + 4 * g + r;
}
__device__ __forceinline__ u32x4 tr_frag(unsigned a0, unsigned a1) {
  u32x2 lo, hi;
  asm volatile("ds_read_b64_tr_b16 %0, %2\n\tds_read_b64_tr_b16 %1, %3\n\ts_waitcnt lgkmcnt(0)" : "=&v"(lo), "=&v"(hi) : "v"(a0), "v"(a1) : "memory");
  return (u32x4){lo[0], lo[1], hi[0], hi[1]};
}
#define KA_SLEEP 32
#define PFA_POL ""
#define SJLD(p) (*(p))
constexpr int KA_SLOT = 10240, KA_ATT = 6 * KA_SLOT, KA_PITCH = 144, KA_LDS = KA_ATT + 142 * KA_PITCH;
template <int FAST> struct KaCfg {
  static constexpr int NZ2 = 4 + FAST, NZ1 = 4 + FAST;
  static constexpr int nt(int s) { return s < 9 ? 4 : 4 + FAST; }
  static constexpr int goff(int s) { return s < 9 ? s * 8192 : 73728 + (s - 9) * (4 + FAST) * 2048; }
  static constexpr int ops(int s) { return s > 26 ? 0 : 2 + (nt(s) == 5 ? 1 : 0) + (s < 9 ? 4 + ((s & 1) ? 0 : 1) : 0); }
  static constexpr int wait(int s) { return s == 20 ? ops(23) + ops(24) : ops(s + 2) + ops(s + 3) + ops(s + 4); }
};
template <int V> struct IC { static constexpr int v = V; };
#define BLD(dst, voff, srd, soff, IMM) asm volatile("buffer_load_dwordx4 %0, %1, %2, %3 offen offset:%4" : "=v"(dst) : "v"(voff), "s"(srd), "s"(soff), "i"(IMM))
#define AMFMA0(acc, a, b) asm volatile("v_mfma_f32_16x16x32_bf16 %0, %1, %2, 0" : "=&v"(acc) : "v"(a), "v"(b))
typedef __attribute__((ext_vector_type(2))) float f32x2;
typedef __attribute__((ext_vector_type(2))) __bf16 bf2_t;
__device__ __forceinline__ float dot2(unsigned a, unsigned b, float c) { return __builtin_amdgcn_fdot2_f32_bf16(__builtin_bit_cast(bf2_t, a), __builtin_bit_cast(bf2_t, b), c, false); }
__device__ __forceinline__ f32x2 gelu2(f32x2 x) {
  f32x2 t = x * x, p = t * (-0.10294324f) + (-2.3022082f), e = x * p;
  f32x2 d = {__builtin_amdgcn_exp2f(e[0]), __builtin_amdgcn_exp2f(e[1])};
  d += 1.f;
  f32x2 r = {__builtin_amdgcn_rcpf(d[0]), __builtin_amdgcn_rcpf(d[1])};
  return x * r;
}
template <int CTRL> __device__ __forceinline__ float dpp_add(float v) {
  return v + __builtin_bit_cast(float, __builtin_amdgcn_update_dpp(0, __builtin_bit_cast(int, v), CTRL, 0xf, 0xf, true));
}

__device__ __forceinline__ float xmax16(float v) { auto r = __builtin_amdgcn_permlane16_swap(__float_as_uint(v), __float_as_uint(v), false, false); return fmaxf(__uint_as_float(r[0]), __uint_as_float(r[1])); }
__device__ __forceinline__ float xmax32(float v) { auto r = __builtin_amdgcn_permlane32_swap(__float_as_uint(v), __float_as_uint(v), false, false); return fmaxf(__uint_as_float(r[0]), __uint_as_float(r[1])); }
__device__ __forceinline__ float xadd16(float v) { auto r = __builtin_amdgcn_permlane16_swap(__float_as_uint(v), __float_as_uint(v), false, false); return __uint_as_float(r[0]) + __uint_as_float(r[1]); }
__device__ __forceinline__ float xadd32(float v) { auto r = __builtin_amdgcn_permlane32_swap(__float_as_uint(v), __float_as_uint(v), false, false); return __uint_as_float(r[0]) + __uint_as_float(r[1]); }
#define PRIO_HI()
#define PRIO_LO()
#define LBAR() asm volatile("s_waitcnt lgkmcnt(0)\n\ts_barrier" ::: "memory")
__device__ __forceinline__ float swap1(float v) { return __builtin_bit_cast(float, __builtin_amdgcn_update_dpp(0, __builtin_bit_cast(int, v), 0xB1, 0xf, 0xf, true)); }
template <int FAST>
__device__ __forceinline__ void ka_body(char* smem, const char* __restrict__ ws_c, char* __restrict__ ws, const int p, const int cg, const int sj, const u16* __restrict__ wsp, const u16* __restrict__ wout, const u16* __restrict__ wo) {
  typedef KaCfg<FAST> C;
  const int tid = threadIdx.x, lane = tid & 63, li = lane & 15, lg = lane >> 4;
  const int w = RFL(tid >> 6), fb = cg >> 1;
  const rsrc_t rW = mk_rsrc(ws_c + OFF_W1 + (size_t)fb * (468 * 1024) + (FAST ? 0 : 252 * 1024), (FAST ? 252 : 216) * 1024);
  const rsrc_t rX = mk_rsrc(ws_c + OFF_XT, (int)SZ_XT);
  const unsigned ldsb = (unsigned)(size_t)(__attribute__((address_space(3))) char*)smem;
  const unsigned vl16 = lane * 16;
  const unsigned vX = vl16 + (unsigned)(p * 8 + w * 2) * 18432u;
  const unsigned vH = p == 0 ? 0x80000000u : (unsigned)(p * 8 - 2 + (li >> 3)) * 18432u + (unsigned)(lg * 16 + (li | 8)) * 16u + (unsigned)w * 1024u;
  const unsigned sF = RFL((unsigned)w * 2048u), sH = RFL(8192u + (unsigned)w * 512u);
  const unsigned rd = ldsb + vl16;
  constexpr int KA_WARM = 0;
  bf16x8 X[2][18], HX[5], F[5];
  f32x4 acc[2][5], hacc[3];
#pragma unroll
  for (int n = 0; n < 3; ++n) hacc[n] = (f32x4){0.f, 0.f, 0.f, 0.f};

  bf16x8 (&X_)[2][18] = X; bf16x8 (&HX_)[5] = HX; bf16x8 (&F_)[5] = F; f32x4 (&acc_)[2][5] = acc; f32x4 (&hacc_)[3] = hacc;
  const rsrc_t rW_ = rW, rX_ = rX; const unsigned vl16_ = vl16, vX_ = vX, vH_ = vH, rd_ = rd;
#define KA_CAP bf16x8 (&X)[2][18] = X_; bf16x8 (&HX)[5] = HX_; bf16x8 (&F)[5] = F_; f32x4 (&acc)[2][5] = acc_; f32x4 (&hacc)[3] = hacc_; \
    const rsrc_t rW = rW_, rX = rX_; const unsigned vl16 = vl16_, vX = vX_, vH = vH_, rd = rd_; (void)X; (void)HX; (void)F; (void)acc; (void)hacc; (void)rW; (void)rX; (void)vl16; (void)vX; (void)vH; (void)rd;
  auto vop = [&](auto S_, auto K_) {
    KA_CAP
    constexpr int s = decltype(S_)::v, k = decltype(K_)::v;
    if constexpr (s <= 26) {
      constexpr int slot = s % 6, nd = 2 + (C::nt(s) == 5 ? 1 : 0);
      if constexpr (k == 0) { unsigned lo = ldsb + slot * KA_SLOT + sF, so = C::goff(s) + sF; DMA16(lo, vl16, rW, so, 0); }
      else if constexpr (k == 1) { unsigned lo = ldsb + slot * KA_SLOT + sF, so = C::goff(s) + sF; DMA16(lo, vl16, rW, so, 1024); }
      else if constexpr (k == 2 && nd == 3) { unsigned lo = ldsb + slot * KA_SLOT + sH, so = C::goff(s) + sH; DMA16H(lo, vl16, rW, so); }
      else if constexpr (s < 9 && k >= 2 && k < 6) {
        constexpr int m = (k - 2) & 1, ks = 2 * s + ((k - 2) >> 1);
        BLD(X[m][ks], vX, rX, m * 18432 + (ks & ~3) * 1024, (ks & 3) * 1024);
      } else if constexpr (s < 9 && k == 6 && !(s & 1)) { BLD(HX[s >> 1], vH, rX, (s >> 1) * 4096, 0); }
    }
  };
  auto group = [&](auto S_) { vop(S_, IC<0>{}); vop(S_, IC<1>{}); vop(S_, IC<2>{}); vop(S_, IC<3>{}); vop(S_, IC<4>{}); vop(S_, IC<5>{}); vop(S_, IC<6>{}); };
  auto sync = [&](auto S_) {
    KA_CAP
    constexpr int s = decltype(S_)::v;
    if constexpr (s < 26) {
      if constexpr (s + 1 < 9) {
        constexpr int k0 = 2 * (s + 1);
        asm volatile("s_waitcnt vmcnt(%5)" : "+v"(X[0][k0]), "+v"(X[1][k0]), "+v"(X[0][k0 + 1]), "+v"(X[1][k0 + 1]), "+v"(HX[(s + 1) >> 1]) : "i"(C::wait(s)) : "memory");
      } else asm volatile("s_waitcnt vmcnt(%0)" :: "i"(C::wait(s)) : "memory");
      __builtin_amdgcn_s_barrier();
    }
  };
  auto step = [&](auto S_, auto H_) {
    KA_CAP
    constexpr int s = decltype(S_)::v, h = decltype(H_)::v;
    constexpr int NT = C::nt(s), job = s / 9, ks = 2 * (s % 9) + h;
    constexpr int s2 = h ? s + 1 : s, h2 = h ^ 1, NT2 = s2 <= 26 ? C::nt(s2) : 0;
    constexpr int roff = (s2 % 6) * KA_SLOT + h2 * NT2 * 1024;
    if constexpr (ks == 0) asm volatile("s_waitcnt lgkmcnt(0)" ::: "memory");
    if constexpr (job == 0) {
      if (w == (ks & 3)) {
        asm volatile("s_waitcnt lgkmcnt(1)" ::: "memory");
#pragma unroll
        for (int n = 0; n < 3; ++n) AMFMA(hacc[n], F[n], HX[ks >> 2]);
      }
    }
#pragma unroll
    for (int n = 0; n < NT; ++n) {
      if constexpr (ks != 0) asm volatile("s_waitcnt lgkmcnt(%0)" :: "i"(NT - 1) : "memory");
      if constexpr (job == 1) {
        if constexpr (ks == 0) { AMFMA0(acc[0][n], X[0][ks], F[n]); AMFMA0(acc[1][n], X[1][ks], F[n]); }
        else { AMFMA(acc[0][n], X[0][ks], F[n]); AMFMA(acc[1][n], X[1][ks], F[n]); }
      } else {
        if constexpr (ks == 0) { AMFMA0(acc[0][n], F[n], X[0][ks]); AMFMA0(acc[1][n], F[n], X[1][ks]); }
        else { AMFMA(acc[0][n], F[n], X[0][ks]); AMFMA(acc[1][n], F[n], X[1][ks]); }
      }
      if (n < NT2) ADSRD(F[n], rd, roff + n * 1024);
      if constexpr (h == 1) {
        if (n == 0) { vop(IC<s + 5>{}, IC<0>{}); vop(IC<s + 5>{}, IC<2>{}); }
        if (n == 1) { vop(IC<s + 5>{}, IC<1>{}); vop(IC<s + 5>{}, IC<3>{}); }
        if (n == 2) { vop(IC<s + 5>{}, IC<4>{}); vop(IC<s + 5>{}, IC<5>{}); }
        if (n == 3) { vop(IC<s + 5>{}, IC<6>{}); }
      }
    }
    if constexpr (NT2 > NT) ADSRD(F[4], rd, roff + 4 * 1024);
  };
#define KA_ACC_DONE() do { asm volatile("s_nop 15" : "+v"(acc[0][0]), "+v"(acc[0][1]), "+v"(acc[0][2]), "+v"(acc[0][3]), "+v"(acc[1][0]), "+v"(acc[1][1]), "+v"(acc[1][2]), "+v"(acc[1][3]) :: "memory"); \
    if constexpr (FAST) asm volatile("" : "+v"(acc[0][4]), "+v"(acc[1][4]) :: "memory"); } while (0)
  auto iter = [&](auto S_) { step(S_, IC<0>{}); sync(S_); step(S_, IC<1>{}); };

  group(IC<0>{}); group(IC<1>{});
  asm volatile("s_waitcnt vmcnt(%5)" : "+v"(X[0][0]), "+v"(X[1][0]), "+v"(X[0][1]), "+v"(X[1][1]), "+v"(HX[0]) : "i"(C::ops(1) + KA_WARM) : "memory");
  __builtin_amdgcn_s_barrier();
#pragma unroll
  for (int n = 0; n < 4; ++n) ADSRD(F[n], rd, n * 1024);

  step(IC<0>{}, IC<0>{}); group(IC<2>{}); group(IC<3>{}); group(IC<4>{}); sync(IC<0>{}); step(IC<0>{}, IC<1>{});
  iter(IC<1>{}); iter(IC<2>{}); iter(IC<3>{}); iter(IC<4>{}); iter(IC<5>{}); iter(IC<6>{}); iter(IC<7>{}); iter(IC<8>{});
  asm volatile("s_nop 15" : "+v"(acc[0][0]), "+v"(acc[0][1]), "+v"(acc[0][2]), "+v"(acc[0][3]), "+v"(acc[1][0]), "+v"(acc[1][1]), "+v"(acc[1][2]), "+v"(acc[1][3]), "+v"(hacc[0]), "+v"(hacc[1]), "+v"(hacc[2]) :: "memory");

  SCHED();
  {
    char* att = smem + KA_ATT;
    char* hp = smem + 2 * KA_SLOT;
#pragma unroll
    for (int n = 0; n < 3; ++n) if (n != w) *(f32x4*)(hp + (n * 3 + (w > n ? w - 1 : w)) * 1024 + vl16) = hacc[n];
#pragma unroll
    for (int m = 0; m < 2; ++m) {
      char* rp = att + (14 + w * 32 + 8 * (li >> 2) + 4 * m + (li & 3)) * KA_PITCH + lg * 8;
#pragma unroll
      for (int n = 0; n < 4; ++n) *(u32x2*)(rp + n * 32) = (u32x2){pk_bf16(acc[m][n][0], acc[m][n][1]), pk_bf16(acc[m][n][2], acc[m][n][3])};
    }
    LBAR();
    if (w < 3) {
      f32x4 a = w == 0 ? hacc[0] : (w == 1 ? hacc[1] : hacc[2]);
#pragma unroll
      for (int q = 0; q < 3; ++q) a += *(const f32x4*)(hp + (w * 3 + q) * 1024 + vl16);
      int ho = (li < 8) ? 8 * (li >> 2) + (li & 3) : 8 * ((li - 8) >> 2) + 4 + (li & 3);
      if (ho >= 2) *(u32x2*)(att + (ho - 2) * KA_PITCH + w * 32 + lg * 8) = (u32x2){pk_bf16(a[0], a[1]), pk_bf16(a[2], a[3])};
    }
    LBAR();
  }
  SCHED();
  PRIO_LO();

  iter(IC<9>{}); iter(IC<10>{}); iter(IC<11>{}); iter(IC<12>{}); iter(IC<13>{}); iter(IC<14>{}); iter(IC<15>{}); iter(IC<16>{}); iter(IC<17>{});
  KA_ACC_DONE();

  SCHED();
  unsigned pfv = 0;
  if constexpr (FAST) {
    {
      const rsrc_t rS = mk_rsrc(wsp, 2048 * 4096);
      const int ks = (sj * 4 + w) & 63, tp = (sj * 4 + w) >> 6, r = (w & 1) * 64 + lane;
      const unsigned vo = (unsigned)prow(tp * 8 + (r >> 4), r & 15) * 4096u + (unsigned)(ks >> 1) * 128u;
      asm volatile("buffer_load_dword %0, %1, %2, 0 offen" : "+v"(pfv) : "v"(vo), "s"(rS));
    }
    if (sj < 90) {
      const int base0 = wc_krow(sj, 0, 0), base1 = wc_krow(sj, 0, 4);
      const rsrc_t rS = sj < 72 ? mk_rsrc(wout, 2304 * 1152) : mk_rsrc(wo - (size_t)2304 * 576, 2880 * 1152);
      const int c = tid, r = c / 9, l = c - r * 9;
      const unsigned vo = (unsigned)((r < 16 ? base0 : base1 - 16) + r) * 1152u + (unsigned)l * 128u;
      asm volatile("buffer_load_dword %0, %1, %2, 0 offen" : "+v"(pfv) : "v"(vo), "s"(rS));
      if (w == 0) {
        const int c2 = 256 + (lane & 31), r2 = c2 / 9, l2 = c2 - r2 * 9;
        const unsigned vo2 = (unsigned)(base1 - 16 + r2) * 1152u + (unsigned)l2 * 128u;
        asm volatile("buffer_load_dword %0, %1, %2, 0 offen" : "+v"(pfv) : "v"(vo2), "s"(rS));
      }
    }
  }
  SCHED();
  PRIO_HI();
  unsigned zs[C::NZ2][4];
  {
#pragma unroll
    for (int n = 0; n < C::NZ2; ++n) {
#pragma unroll
      for (int m = 0; m < 2; ++m) {
        f32x2 a = gelu2((f32x2){acc[m][n][0], acc[m][n][1]}), b = gelu2((f32x2){acc[m][n][2], acc[m][n][3]});
        zs[n][2 * m] = pk_bf16(a[0], a[1]); zs[n][2 * m + 1] = pk_bf16(b[0], b[1]);
      }
    }
    f32x2 s1[4], s2[4];
#pragma unroll
    for (int j = 0; j < 4; ++j) {
      s1[j] = (f32x2){0.f, 0.f}; s2[j] = s1[j];
#pragma unroll
      for (int n = 0; n < C::NZ2; ++n) { f32x2 v = {bf_lo(zs[n][j]), bf_hi(zs[n][j])}; s1[j] += v; s2[j] += v * v; }
    }
    float r[16];
#pragma unroll
    for (int j = 0; j < 4; ++j) { r[2 * j] = s1[j][0]; r[2 * j + 1] = s1[j][1]; r[8 + 2 * j] = s2[j][0]; r[9 + 2 * j] = s2[j][1]; }
    float sel;
    {
      float a[8], c[4];
#pragma unroll
      for (int j = 0; j < 8; j += 4)
        asm volatile("s_nop 1\n\t"
          "v_add_f32_dpp %0, %4, %4 row_ror:8 row_mask:0xf bank_mask:0x3\n\tv_add_f32_dpp %1, %5, %5 row_ror:8 row_mask:0xf bank_mask:0x3\n\t"
          "v_add_f32_dpp %2, %6, %6 row_ror:8 row_mask:0xf bank_mask:0x3\n\tv_add_f32_dpp %3, %7, %7 row_ror:8 row_mask:0xf bank_mask:0x3\n\t"
          "v_add_f32_dpp %0, %8, %8 row_ror:8 row_mask:0xf bank_mask:0xc\n\tv_add_f32_dpp %1, %9, %9 row_ror:8 row_mask:0xf bank_mask:0xc\n\t"
          "v_add_f32_dpp %2, %10, %10 row_ror:8 row_mask:0xf bank_mask:0xc\n\tv_add_f32_dpp %3, %11, %11 row_ror:8 row_mask:0xf bank_mask:0xc"
          : "=&v"(a[j]), "=&v"(a[j + 1]), "=&v"(a[j + 2]), "=&v"(a[j + 3])
          : "v"(r[j]), "v"(r[j + 1]), "v"(r[j + 2]), "v"(r[j + 3]), "v"(r[j + 8]), "v"(r[j + 9]), "v"(r[j + 10]), "v"(r[j + 11]));
      asm volatile("s_nop 1\n\t"
          "v_add_f32_dpp %0, %4, %4 row_half_mirror row_mask:0xf bank_mask:0x5\n\tv_add_f32_dpp %1, %5, %5 row_half_mirror row_mask:0xf bank_mask:0x5\n\t"
          "v_add_f32_dpp %2, %6, %6 row_half_mirror row_mask:0xf bank_mask:0x5\n\tv_add_f32_dpp %3, %7, %7 row_half_mirror row_mask:0xf bank_mask:0x5\n\t"
          "v_add_f32_dpp %0, %8, %8 row_half_mirror row_mask:0xf bank_mask:0xa\n\tv_add_f32_dpp %1, %9, %9 row_half_mirror row_mask:0xf bank_mask:0xa\n\t"
          "v_add_f32_dpp %2, %10, %10 row_half_mirror row_mask:0xf bank_mask:0xa\n\tv_add_f32_dpp %3, %11, %11 row_half_mirror row_mask:0xf bank_mask:0xa\n\ts_nop 1"
          : "=&v"(c[0]), "=&v"(c[1]), "=&v"(c[2]), "=&v"(c[3])
          : "v"(a[0]), "v"(a[1]), "v"(a[2]), "v"(a[3]), "v"(a[4]), "v"(a[5]), "v"(a[6]), "v"(a[7]));
#pragma unroll
      for (int j = 0; j < 4; ++j) c[j] = dpp_add<0x4E>(c[j]);
      float d0 = (li & 2) ? c[2] : c[0], d1 = (li & 2) ? c[3] : c[1];
      d0 = dpp_add<0xB1>(d0); d1 = dpp_add<0xB1>(d1);
      sel = (li & 1) ? d1 : d0;
    }
    double* dst = (double*)(ws + OFF_RS) + (size_t)(p * 128 + w * 32 + lg * 8 + (li & 7)) * 2 + (li >> 3);
    double dv = (double)sel;
    asm volatile("global_atomic_add_f64 %0, %1, off sc1" :: "v"(dst), "v"(dv) : "memory");
  }
  SCHED();

  PRIO_LO();

  unsigned* cnt = (unsigned*)(ws + OFF_CNT + p * 256);
  iter(IC<18>{}); iter(IC<19>{});
  step(IC<20>{}, IC<0>{}); sync(IC<20>{});
  if constexpr (FAST) asm volatile("" :: "v"(pfv));
  if (tid == 0) { unsigned one = 1; asm volatile("global_atomic_add %0, %1, off sc1" :: "v"(cnt), "v"(one) : "memory"); }
  step(IC<20>{}, IC<1>{});
  iter(IC<21>{}); iter(IC<22>{}); iter(IC<23>{}); iter(IC<24>{}); iter(IC<25>{});
  unsigned seen;
  asm volatile("global_load_dword %0, %1, off sc1" : "=v"(seen) : "v"(cnt) : "memory");
  iter(IC<26>{});
  KA_ACC_DONE();
  asm volatile("s_waitcnt vmcnt(0)" : "+v"(seen) :: "memory");
  PRIO_HI();
  const unsigned seen_s = RFL(seen);
  const char* sp = ws + OFF_RS + (size_t)(p * 128 + w * 32 + (lane & 31)) * 16;
  u32x4 st = {0, 0, 0, 0};
  asm volatile("s_cmp_lt_u32 %2, 32\n\ts_cbranch_scc1 1f\n\tglobal_load_dwordx4 %0, %1, off sc1\n1:" : "+v"(st) : "v"(sp), "s"(seen_s) : "memory", "scc");
  SCHED();
  u32x4 wt[8];
  if constexpr (FAST) {
    const int ks = (sj * 4 + w) & 63, tp = (sj * 4 + w) >> 6;
    const rsrc_t rS = mk_rsrc(wsp, 2048 * 4096);
    const unsigned vo = (unsigned)(8 * (li >> 2) + (li & 3)) * 4096u + (unsigned)lg * 16u, so = (unsigned)tp * (128u * 4096u) + (unsigned)ks * 64u;
#pragma unroll
    for (int m = 0; m < 8; ++m) wt[m] = __builtin_amdgcn_raw_buffer_load_b128(rS, vo, so + (unsigned)(32 * (m >> 1) + 4 * (m & 1)) * 4096u, 0);
  }
#pragma unroll
  for (int m = 0; m < 2; ++m) {
    const char* zp = ws + OFF_Z1T + ((size_t)(p * 8 + w * 2 + m) * 16 + fb) * 4608;
    unsigned pk[C::NZ1][2];
#pragma unroll
    for (int n = 0; n < C::NZ1; ++n) {
      f32x2 a = gelu2((f32x2){acc[m][n][0], acc[m][n][1]}), b = gelu2((f32x2){acc[m][n][2], acc[m][n][3]});
      pk[n][0] = pk_bf16(a[0], a[1]); pk[n][1] = pk_bf16(b[0], b[1]);
    }
    st16s<FAST * 2048>(zp, vl16, (u32x4){pk[0][0], pk[0][1], pk[1][0], pk[1][1]});
    st16s<FAST * 2048 + 1024>(zp, vl16, (u32x4){pk[2][0], pk[2][1], pk[3][0], pk[3][1]});
    if constexpr (FAST) st8((char*)zp + 4096 + lane * 8, (u32x2){pk[4][0], pk[4][1]});
  }
  SCHED();
  if constexpr (FAST) {
    const char* wb = ws + OFF_WT + (size_t)(sj * 4 + w) * 8192;
    st16s<0>(wb, vl16, wt[0]); st16s<1024>(wb, vl16, wt[1]); st16s<2048>(wb, vl16, wt[2]); st16s<3072>(wb, vl16, wt[3]);
    st16s<0>(wb + 4096, vl16, wt[4]); st16s<1024>(wb + 4096, vl16, wt[5]); st16s<2048>(wb + 4096, vl16, wt[6]); st16s<3072>(wb + 4096, vl16, wt[7]);
    if (sj < 90) {
      const int ks = sj, base0 = wc_krow(ks, 0, 0), base1 = wc_krow(ks, 0, 4);
      const rsrc_t rS = sj < 72 ? mk_rsrc(wout, 2304 * 1152) : mk_rsrc(wo - (size_t)2304 * 576, 2880 * 1152);
      const int r = tid >> 3;
      const unsigned vo = (unsigned)((r < 16 ? base0 : base1 - 16) + r) * 1152u + (unsigned)(tid & 7) * 16u;
      u32x4 v[9];
#pragma unroll
      for (int k = 0; k < 9; ++k) v[k] = __builtin_amdgcn_raw_buffer_load_b128(rS, vo, k * 128, 0);
      LBAR();
      char* dl = smem + r * K0_PITCH + (tid & 7) * 16;
#pragma unroll
      for (int k = 0; k < 9; ++k) *(u32x4*)(dl + k * 128) = v[k];
      LBAR();
      const unsigned a0 = ldsb + (4 * lg + (li >> 2)) * K0_PITCH + (li & 3) * 8;
#pragma unroll
      for (int e = 0; e < 10; ++e) {
        int f = w * 10 + e, ct = f / 5, n = f - ct * 5, d0 = (ct < 7 ? ct * 72 : 496) + n * 16;
        u32x4 o = tr_frag(a0 + d0 * 2, a0 + d0 * 2 + 16 * K0_PITCH);
        st16s<0>(ws + OFF_WC + (size_t)((ct * 90 + ks) * 5 + n) * 1024, vl16, o);
      }
    }
  }
  asm volatile("s_cmp_ge_u32 %2, 32\n\ts_cbranch_scc1 1f\n\tglobal_load_dword %0, %1, off sc1\n1:" : "+v"(seen) : "v"(cnt), "s"(seen_s) : "memory", "scc");
  unsigned seen_t;
  SCHED();
  {
    char* att = smem + KA_ATT;
    const unsigned attb = ldsb + KA_ATT;
    float mbias[8];
#pragma unroll
    for (int e = 0; e < 8; ++e) { int d = (e >> 2) * 16 + lg * 4 + (e & 3) - li; mbias[e] = (!(d & 1) && d >= 2 && d <= 16) ? 0.f : -__builtin_inff(); }
    bf16x8 Q[2], K[2][2]; u32x2 V[2][2][2];
#pragma unroll
    for (int jj = 0; jj < 2; ++jj) {
      const int j = w * 2 + jj;
      Q[jj] = *(const bf16x8*)(att + (14 + 16 * j + li) * KA_PITCH + 80 + (lg < 3 ? lg : 2) * 16);
#pragma unroll
      for (int kt = 0; kt < 2; ++kt) {
        int R = 16 * j - 2 + 16 * kt + li; R = R < 0 ? 0 : R;
        K[jj][kt] = *(const bf16x8*)(att + R * KA_PITCH + lg * 16);
        int Rv = 16 * j - 2 + 16 * kt + 4 * lg + (li >> 2); Rv = Rv < 0 ? 0 : Rv;
        const unsigned va = attb + Rv * KA_PITCH + 40 + (li & 3) * 8;
        asm volatile("ds_read_b64_tr_b16 %0, %2\n\tds_read_b64_tr_b16 %1, %2 offset:32" : "=&v"(V[jj][0][kt]), "=&v"(V[jj][1][kt]) : "v"(va) : "memory");
      }
    }
    f32x4 S[2][2];
#pragma unroll
    for (int jj = 0; jj < 2; ++jj) {
      if (lg == 3) Q[jj] = (bf16x8){0, 0, 0, 0, 0, 0, 0, 0};
#pragma unroll
      for (int kt = 0; kt < 2; ++kt) S[jj][kt] = MFMA(K[jj][kt], Q[jj], ((f32x4){0.f, 0.f, 0.f, 0.f}));
    }
    bf16x8 P[2];
#pragma unroll
    for (int jj = 0; jj < 2; ++jj) {
      float l[8];
#pragma unroll
      for (int e = 0; e < 8; ++e) l[e] = S[jj][e >> 2][e & 3] + mbias[e];
      if (p == 0 && w == 0 && jj == 0) {
#pragma unroll
        for (int e = 0; e < 4; ++e) l[e] = -__builtin_inff();
      }
      float mx = fmaxf(fmaxf(fmaxf(l[0], l[1]), fmaxf(l[2], l[3])), fmaxf(fmaxf(l[4], l[5]), fmaxf(l[6], l[7])));
      mx = xmax16(mx); mx = xmax32(mx);
      constexpr float CE = 0.23570226f * 1.44269504f;
      const float nm = -mx * CE;
      float sum = 0.f;
#pragma unroll
      for (int e = 0; e < 8; ++e) { l[e] = __builtin_amdgcn_exp2f(l[e] * CE + nm); sum += l[e]; }
      sum = xadd16(sum); sum = xadd32(sum);
      const float inv = __builtin_amdgcn_rcpf(sum);
      u32x4 pk = {pk_bf16(l[0] * inv, l[1] * inv), pk_bf16(l[2] * inv, l[3] * inv), pk_bf16(l[4] * inv, l[5] * inv), pk_bf16(l[6] * inv, l[7] * inv)};
      P[jj] = __builtin_bit_cast(bf16x8, pk);
    }
    asm volatile("s_waitcnt lgkmcnt(0)" : "+v"(V[0][0][0]), "+v"(V[0][0][1]), "+v"(V[0][1][0]), "+v"(V[0][1][1]), "+v"(V[1][0][0]), "+v"(V[1][0][1]), "+v"(V[1][1][0]), "+v"(V[1][1][1]) :: "memory");
    unsigned ov[2][3];
#pragma unroll
    for (int jj = 0; jj < 2; ++jj) {
      f32x4 O[2];
#pragma unroll
      for (int dt = 0; dt < 2; ++dt) {
        u32x4 vv = {V[jj][dt][0][0], V[jj][dt][0][1], V[jj][dt][1][0], V[jj][dt][1][1]};
        O[dt] = MFMA(__builtin_bit_cast(bf16x8, vv), P[jj], ((f32x4){0.f, 0.f, 0.f, 0.f}));
      }
      ov[jj][0] = pk_bf16(O[0][0], O[0][1]); ov[jj][1] = pk_bf16(O[0][2], O[0][3]); ov[jj][2] = pk_bf16(O[1][0], O[1][1]);
    }
    asm volatile("s_waitcnt vmcnt(0)" : "+v"(seen), "+v"(st) :: "memory");
    seen_t = RFL(seen);
    asm volatile("s_cmp_ge_u32 %2, 32\n\ts_cbranch_scc1 1f\n\ts_cmp_lt_u32 %3, 32\n\ts_cbranch_scc1 1f\n\tglobal_load_dwordx4 %0, %1, off sc1\n1:" : "+v"(st) : "v"(sp), "s"(seen_s), "s"(seen_t) : "memory", "scc");
#pragma unroll
    for (int jj = 0; jj < 2; ++jj) {
      const int tl = jj * 16 + li, tt = p * 8 + w * 2 + ((tl >> 2) & 1), slot = (tl >> 3) * 4 + (tl & 3);
      char* ob = ws + OFF_OT + (size_t)tt * 18432 + slot * 8;
      const int d0 = cg * 18 + lg * 4, d1 = d0 + 2, d2 = cg * 18 + 16;
      st4(ob + (d0 >> 2) * 128 + (d0 & 3) * 2, ov[jj][0]);
      st4(ob + (d1 >> 2) * 128 + (d1 & 3) * 2, ov[jj][1]);
      if (lg == 0) st4(ob + (d2 >> 2) * 128 + (d2 & 3) * 2, ov[jj][2]);
    }
  }
  SCHED();
  if (seen_t < 32u) {
    do {
      asm volatile("global_load_dword %0, %1, off sc1\n\ts_waitcnt vmcnt(0)" : "=v"(seen) : "v"(cnt) : "memory");
      if (RFL(seen) >= 32u) break;
      __builtin_amdgcn_s_sleep(KA_SLEEP);
    } while (true);
  }
  asm volatile("s_cmp_ge_u32 %2, 32\n\ts_cbranch_scc1 1f\n\tglobal_load_dwordx4 %0, %1, off sc1\n\ts_waitcnt vmcnt(0)\n1:\n\ts_waitcnt vmcnt(6)" : "+v"(st) : "v"(sp), "s"(seen_t) : "memory", "scc");
  {
    const double sm = __builtin_bit_cast(double, (u32x2){st[0], st[1]}), sq = __builtin_bit_cast(double, (u32x2){st[2], st[3]});
    const double mean = sm * (1.0 / 2304.0);
    const float var = fmaxf((float)(sq * (1.0 / 2304.0) - mean * mean), 0.f);
    const float mb = rbf((float)mean), rs = 1.f / rbf(sqrtf(rbf(rbf(var) + 1e-5f)));
    f32x2* sl = (f32x2*)(smem + 4 * KA_SLOT + w * 256);
    if (lane < 32) sl[lane] = (f32x2){mb, rs};
    __builtin_amdgcn_s_waitcnt(0xC07F);
    __builtin_amdgcn_wave_barrier();
    f32x4 ms[4];
#pragma unroll
    for (int j = 0; j < 4; ++j) ms[j] = *(const f32x4*)((const char*)sl + lg * 64 + j * 16);
    const char* zp = ws + OFF_Z2T + ((size_t)(fb * 64 + p * 4 + w) * 9 + (FAST ? 0 : 5)) * 1024;
    auto z2o = [&](int n) { u32x4 o;
#pragma unroll
      for (int j = 0; j < 4; ++j) o[j] = pk_bf16((bf_lo(zs[n][j]) - ms[j][0]) * ms[j][1], (bf_hi(zs[n][j]) - ms[j][2]) * ms[j][3]);
      return o; };
    st16s<0>(zp, vl16, z2o(0)); st16s<1024>(zp, vl16, z2o(1)); st16s<2048>(zp, vl16, z2o(2)); st16s<3072>(zp, vl16, z2o(3));
    if constexpr (FAST) st16s<0>(zp + 4096, vl16, z2o(4));
  }
}
__global__ __launch_bounds__(256, 2) void kA(const char* __restrict__ ws_c, char* __restrict__ ws, const u16* __restrict__ wsp, const u16* __restrict__ wout, const u16* __restrict__ wo) {
  extern __shared__ __attribute__((aligned(16))) char smem[];
  const int b = blockIdx.x, xcd = b & 7, idx = b >> 3;
  const int p = (xcd & 1) * 8 + (idx & 7), q = idx >> 3, cg = (xcd >> 1) * 8 + (q & 3) * 2 + (q >> 2);
  if (idx < 32) ka_body<1>(smem, ws_c, ws, p, cg, b, wsp, wout, wo); else ka_body<0>(smem, ws_c, ws, p, cg, b, wsp, wout, wo);
}

constexpr int KB_STAGE = 36864;
constexpr int KB_LDS = 4 * KB_STAGE;
__global__ __launch_bounds__(512, 2) void kB(const char* __restrict__ ws_c, char* __restrict__ ws, const u16* __restrict__ bsp) {
  extern __shared__ __attribute__((aligned(16))) char smem[];
  const int tid = threadIdx.x, lane = tid & 63;
  const int w = RFL(tid >> 6), wm = w & 3, kh = w >> 2;
  const int b = blockIdx.x, xcd = b & 7, idx = b >> 3;
  const int tp = (xcd & 1) * 8 + (idx & 7), fb = (xcd >> 1) * 4 + (idx >> 3);
  const rsrc_t rZ = mk_rsrc(ws_c + OFF_Z2T + (size_t)fb * (64 * 9216), 64 * 9216);
  const rsrc_t rW = mk_rsrc(ws_c + OFF_WT + (size_t)tp * (64 * 8192), 64 * 8192);
  const unsigned ldsb = (unsigned)(size_t)(__attribute__((address_space(3))) char*)smem;
  const unsigned vl16 = lane * 16;
  const unsigned vW = vl16 + wm * 2048 + kh * 16384;
  const unsigned dmaF = w * 4096, dmaH = 32768 + w * 512;
  unsigned rd01 = ldsb + kh * 18432 + lane * 16;
  unsigned rd23 = rd01 + 2 * KB_STAGE;

  f32x4 acc[2][9];
#pragma unroll
  for (int m = 0; m < 2; ++m)
#pragma unroll
    for (int n = 0; n < 9; ++n) acc[m][n] = (f32x4){0.f, 0.f, 0.f, 0.f};
  bf16x8 Wr[4][4];
  bf16x8 ZA[9], ZB[9];

#define KB_DMA1(stage, slot, imm) do { \
    unsigned so_ = RFL((unsigned)(stage) * KB_STAGE + dmaF), lo_ = RFL(ldsb + (slot) * KB_STAGE + dmaF); DMA16(lo_, vl16, rZ, so_, imm); } while (0)
#define KB_DMAH(stage, slot) do { \
    unsigned so2_ = RFL((unsigned)(stage) * KB_STAGE + dmaH), lo2_ = RFL(ldsb + (slot) * KB_STAGE + dmaH); DMA16H(lo2_, vl16, rZ, so2_); } while (0)
#define KB_W1(stage, slot, e) do { unsigned sw_ = RFL((unsigned)(stage) * 32768u + ((e) >> 1) * 8192u); \
    if ((e) & 1) BLOAD16(Wr[slot][e], vW, rW, sw_, 1024); else BLOAD16(Wr[slot][e], vW, rW, sw_, 0); } while (0)
#define KB_ALL(stage, slot) do { KB_DMA1(stage, slot, 0); KB_DMA1(stage, slot, 1024); KB_DMA1(stage, slot, 2048); KB_DMA1(stage, slot, 3072); KB_DMAH(stage, slot); \
    KB_W1(stage, slot, 0); KB_W1(stage, slot, 1); KB_W1(stage, slot, 2); KB_W1(stage, slot, 3); } while (0)
#define KB_WAIT(slot, N) asm volatile("s_waitcnt vmcnt(" #N ") lgkmcnt(0)" : "+v"(Wr[slot][0]), "+v"(Wr[slot][1]), "+v"(Wr[slot][2]), "+v"(Wr[slot][3]) :: "memory")

  KB_ALL(0, 0);
  __builtin_amdgcn_s_barrier();
  KB_ALL(1, 1);
  KB_WAIT(0, 9);
  __builtin_amdgcn_s_barrier();

  const rsrc_t rZ1 = mk_rsrc(ws_c + OFF_Z1T + ((size_t)(tp * 8 + wm * 2) * 16 + fb) * 4608, 17 * 4608);
  const rsrc_t rB = mk_rsrc(bsp, 4096);
  u32x4 z1q[2][2]; u32x2 z1s[2]; unsigned biasu[2];
  const unsigned vz1 = lane * 16 + kh * 2048, vz8 = lane * 8 + 4096;
  const unsigned vb = (tp * 128 + wm * 32 + ((lane & 15) >> 2) * 8 + (lane & 3)) * 2;
#define KB_EPI_LD(e) do { \
    if ((e) == 0) BLOAD16(z1q[0][0], vz1, rZ1, 0, 0); \
    if ((e) == 1) BLOAD16(z1q[0][1], vz1, rZ1, 0, 1024); \
    if ((e) == 2) asm volatile("buffer_load_dwordx4 %0, %1, %2, %3 offen" : "=v"(z1q[1][0]) : "v"(vz1), "s"(rZ1), "s"(16 * 4608)); \
    if ((e) == 3) asm volatile("buffer_load_dwordx4 %0, %1, %2, %3 offen offset:1024" : "=v"(z1q[1][1]) : "v"(vz1), "s"(rZ1), "s"(16 * 4608)); \
    if ((e) == 4) asm volatile("buffer_load_dwordx2 %0, %1, %2, 0 offen" : "=v"(z1s[0]) : "v"(vz8), "s"(rZ1)); \
    if ((e) == 5) asm volatile("buffer_load_dwordx2 %0, %1, %2, %3 offen" : "=v"(z1s[1]) : "v"(vz8), "s"(rZ1), "s"(16 * 4608)); \
    if ((e) == 6) asm volatile("buffer_load_ushort %0, %1, %2, 0 offen" : "=v"(biasu[0]) : "v"(vb), "s"(rB)); \
    if ((e) == 7) asm volatile("buffer_load_ushort %0, %1, %2, 0 offen offset:8" : "=v"(biasu[1]) : "v"(vb), "s"(rB)); } while (0)

#define KB_ITER(i, j, MODE, WN) do { \
    _Pragma("unroll") for (int n = 0; n < 9; ++n) { \
      ADSRD(ZA[n], ((j) < 2 ? rd01 : rd23), ((j) & 1) * KB_STAGE + n * 1024); \
      if ((MODE) != 1) { AMFMA(acc[0][n], ZB[n], Wr[((j) + 3) & 3][2]); AMFMA(acc[1][n], ZB[n], Wr[((j) + 3) & 3][3]); } \
      if ((MODE) == 0) { \
        if (n == 0) KB_DMA1((i) + 3, ((j) + 3) & 3, 0); \
        if (n == 2) KB_DMA1((i) + 3, ((j) + 3) & 3, 1024); \
        if (n == 4) KB_W1((i) + 3, ((j) + 3) & 3, 0); \
        if (n == 6) KB_DMA1((i) + 3, ((j) + 3) & 3, 2048); \
        if (n == 8) KB_W1((i) + 3, ((j) + 3) & 3, 1); } \
      if ((MODE) == 2 && !(n & 1)) KB_EPI_LD(n >> 1); } \
    if ((MODE) == 1) { KB_ALL(2, 2); KB_DMA1(3, 3, 0); KB_DMA1(3, 3, 1024); KB_W1(3, 3, 0); KB_DMA1(3, 3, 2048); KB_W1(3, 3, 1); } \
    _Pragma("unroll") for (int n = 0; n < 9; ++n) { \
      if (n == 0) asm volatile("s_waitcnt lgkmcnt(6)" ::: "memory"); \
      if (n == 2) asm volatile("s_waitcnt lgkmcnt(5)" ::: "memory"); \
      if (n == 5) asm volatile("s_waitcnt lgkmcnt(5)" ::: "memory"); \
      ADSRD(ZB[n], ((j) < 2 ? rd01 : rd23), ((j) & 1) * KB_STAGE + 9216 + n * 1024); \
      AMFMA(acc[0][n], ZA[n], Wr[j][0]); AMFMA(acc[1][n], ZA[n], Wr[j][1]); \
      if ((MODE) < 2) { \
        if (n == 0) KB_DMA1((i) + 3, ((j) + 3) & 3, 3072); \
        if (n == 2) KB_W1((i) + 3, ((j) + 3) & 3, 2); \
        if (n == 4) KB_DMAH((i) + 3, ((j) + 3) & 3); \
        if (n == 6) KB_W1((i) + 3, ((j) + 3) & 3, 3); } \
      if ((MODE) == 2 && (n == 0 || n == 2 || n == 4)) KB_EPI_LD(5 + (n >> 1)); } \
    KB_WAIT(((j) + 1) & 3, WN); \
    __builtin_amdgcn_s_barrier(); \
    } while (0)

  KB_ITER(0, 0, 1, 18);
  KB_ITER(1, 1, 0, 18);
  KB_ITER(2, 2, 0, 18);
  KB_ITER(3, 3, 0, 18);
  for (int io = 4; io < 12; io += 4) {
    KB_ITER(io + 0, 0, 0, 18);
    KB_ITER(io + 1, 1, 0, 18);
    KB_ITER(io + 2, 2, 0, 18);
    KB_ITER(io + 3, 3, 0, 18);
  }
  KB_ITER(12, 0, 0, 18);
  KB_ITER(13, 1, 2, 17);
  KB_ITER(14, 2, 3, 8);
  {
#pragma unroll
    for (int n = 0; n < 9; ++n) { ADSRD(ZA[n], rd23, KB_STAGE + n * 1024); AMFMA(acc[0][n], ZB[n], Wr[2][2]); AMFMA(acc[1][n], ZB[n], Wr[2][3]); }
#pragma unroll
    for (int n = 0; n < 9; ++n) {
      if (n == 0) asm volatile("s_waitcnt lgkmcnt(6)" ::: "memory");
      if (n == 2) asm volatile("s_waitcnt lgkmcnt(5)" ::: "memory");
      if (n == 5) asm volatile("s_waitcnt lgkmcnt(5)" ::: "memory");
      ADSRD(ZB[n], rd23, KB_STAGE + 9216 + n * 1024);
      AMFMA(acc[0][n], ZA[n], Wr[3][0]); AMFMA(acc[1][n], ZA[n], Wr[3][1]); }
    asm volatile("s_waitcnt lgkmcnt(0)" ::: "memory");
#pragma unroll
    for (int n = 0; n < 9; ++n) { AMFMA(acc[0][n], ZB[n], Wr[3][2]); AMFMA(acc[1][n], ZB[n], Wr[3][3]); }
  }
  asm volatile("s_nop 15" : "+v"(acc[0][0]), "+v"(acc[0][1]), "+v"(acc[0][2]), "+v"(acc[0][3]), "+v"(acc[0][4]), "+v"(acc[0][5]), "+v"(acc[0][6]), "+v"(acc[0][7]), "+v"(acc[0][8]),
               "+v"(acc[1][0]), "+v"(acc[1][1]), "+v"(acc[1][2]), "+v"(acc[1][3]), "+v"(acc[1][4]), "+v"(acc[1][5]), "+v"(acc[1][6]), "+v"(acc[1][7]), "+v"(acc[1][8]) :: "memory");
  asm volatile("s_waitcnt vmcnt(0)" : "+v"(z1q[0][0]), "+v"(z1q[0][1]), "+v"(z1q[1][0]), "+v"(z1q[1][1]), "+v"(z1s[0]), "+v"(z1s[1]), "+v"(biasu[0]), "+v"(biasu[1]) :: "memory");
  SCHED();
  char* hp = ws + OFF_HT + ((size_t)(tp * 8 + wm * 2) * 16 + fb) * 4608;
  float bias[2] = {bf_lo(biasu[0]), bf_lo(biasu[1])};
  char* ex = smem + wm * 18432 + lane * 16;
  if (kh == 0) {
#pragma unroll
    for (int m = 0; m < 2; ++m)
#pragma unroll
      for (int n = 4; n < 8; ++n) *(f32x4*)(ex + (m * 9 + n) * 1024) = acc[m][n];
  } else {
#pragma unroll
    for (int m = 0; m < 2; ++m) {
#pragma unroll
      for (int n = 0; n < 4; ++n) *(f32x4*)(ex + (m * 9 + n) * 1024) = acc[m][n];
      *(f32x4*)(ex + (m * 9 + 8) * 1024) = acc[m][8];
    }
  }
  __syncthreads();
#pragma unroll
  for (int m = 0; m < 2; ++m) {
#pragma unroll
    for (int q = 0; q < 2; ++q) {
      f32x4 g0, g1;
      if (kh == 0) { g0 = acc[m][2 * q] + *(const f32x4*)(ex + (m * 9 + 2 * q) * 1024); g1 = acc[m][2 * q + 1] + *(const f32x4*)(ex + (m * 9 + 2 * q + 1) * 1024); }
      else { g0 = acc[m][4 + 2 * q] + *(const f32x4*)(ex + (m * 9 + 4 + 2 * q) * 1024); g1 = acc[m][5 + 2 * q] + *(const f32x4*)(ex + (m * 9 + 5 + 2 * q) * 1024); }
      u32x4 z = z1q[m][q], o;
      o[0] = pk_bf16(bf_lo(z[0]) * (g0[0] + bias[m]), bf_hi(z[0]) * (g0[1] + bias[m]));
      o[1] = pk_bf16(bf_lo(z[1]) * (g0[2] + bias[m]), bf_hi(z[1]) * (g0[3] + bias[m]));
      o[2] = pk_bf16(bf_lo(z[2]) * (g1[0] + bias[m]), bf_hi(z[2]) * (g1[1] + bias[m]));
      o[3] = pk_bf16(bf_lo(z[3]) * (g1[2] + bias[m]), bf_hi(z[3]) * (g1[3] + bias[m]));
      st16(hp + m * (16 * 4608) + (kh * 2 + q) * 1024 + lane * 16, o);
    }
    if (kh == 0) {
      f32x4 g0 = acc[m][8] + *(const f32x4*)(ex + (m * 9 + 8) * 1024);
      u32x2 z = z1s[m], o;
      o[0] = pk_bf16(bf_lo(z[0]) * (g0[0] + bias[m]), bf_hi(z[0]) * (g0[1] + bias[m]));
      o[1] = pk_bf16(bf_lo(z[1]) * (g0[2] + bias[m]), bf_hi(z[1]) * (g0[3] + bias[m]));
      st8(hp + m * (16 * 4608) + 4096 + lane * 8, o);
    }
  }
}

constexpr int KC_PITCH = 336;
constexpr int KC_PART = 64 * KC_PITCH;
constexpr int KC_LDS = 5 * KC_PART;
template <bool ISO>
__device__ __forceinline__ void kc_main(f32x4 (&acc)[4][5], rsrc_t rA, rsrc_t rW, unsigned vA16, unsigned vA8, unsigned vW, unsigned sA, unsigned sW) {
  constexpr unsigned TS = ISO ? 18432u : 16u * 4608u;
  constexpr int PD = 3;
  bf16x8 A[PD + 1][4], W[PD + 1][5];
  auto load = [&](int u, int slot) {
#pragma unroll
    for (int m = 0; m < 4; ++m) {
      if (ISO || (u % 9) == 4) {
        unsigned o0, o1;
        if (ISO) { o0 = sA + m * TS + u * 1024; o1 = o0 + 512; }
        else { o0 = sA + m * TS + (u / 9) * 2 * 4608 + 4096; o1 = o0 + 4608; }
        u32x2 lo = __builtin_amdgcn_raw_buffer_load_b64(rA, vA8, o0, 0);
        u32x2 hi = __builtin_amdgcn_raw_buffer_load_b64(rA, vA8, o1, 0);
        u32x4 v = {lo[0], lo[1], hi[0], hi[1]};
        A[slot][m] = __builtin_bit_cast(bf16x8, v);
      } else {
        int i = u % 9;
        unsigned o = sA + m * TS + (u / 9) * 2 * 4608 + (i < 4 ? i * 1024 : 4608 + (i - 5) * 1024);
        A[slot][m] = __builtin_bit_cast(bf16x8, __builtin_amdgcn_raw_buffer_load_b128(rA, vA16, o, 0));
      }
    }
#pragma unroll
    for (int n = 0; n < 5; ++n) W[slot][n] = __builtin_bit_cast(bf16x8, __builtin_amdgcn_raw_buffer_load_b128(rW, vW, sW + (u * 5 + n) * 1024, 0));
  };
#pragma unroll
  for (int u = 0; u < PD; ++u) load(u, u);
  SCHED();
#pragma unroll
  for (int u = 0; u < 18; ++u) {
    if (u + PD < 18) load(u + PD, (u + PD) % (PD + 1));
    SCHED();
#pragma unroll
    for (int m = 0; m < 4; ++m)
#pragma unroll
      for (int n = 0; n < 5; ++n) acc[m][n] = MFMA(W[u % (PD + 1)][n], A[u % (PD + 1)][m], acc[m][n]);
    SCHED();
  }
}
__global__ __launch_bounds__(320, 2) void kC(const char* __restrict__ ws_c, const u16* __restrict__ x, u16* __restrict__ out) {
  extern __shared__ __attribute__((aligned(16))) char smem[];
  const int tid = threadIdx.x, lane = tid & 63;
  const int w = RFL(tid >> 6);
  const int b = blockIdx.x, xcd = b & 7, idx = b >> 3;
  const int rt = (xcd & 3) * 8 + (idx & 7), ct = (xcd >> 2) * 4 + (idx >> 3);
  f32x4 acc[4][5];
#pragma unroll
  for (int m = 0; m < 4; ++m)
#pragma unroll
    for (int n = 0; n < 5; ++n) acc[m][n] = (f32x4){0.f, 0.f, 0.f, 0.f};
  const rsrc_t rW = mk_rsrc(ws_c + OFF_WC, (int)SZ_WC);
  const unsigned sW = RFL((unsigned)((ct * 90 + w * 18) * 5 * 1024));
  const int dstart = ct < 7 ? ct * 72 : 496;
  u32x4 xv[2]; unsigned gof[2];
#pragma unroll
  for (int e = 0; e < 2; ++e) {
    int c = tid + e * 320; c = c < 576 ? c : tid;
    const int r = c / 9, ch = c - r * 9;
    gof[e] = (unsigned)(rt * 64 + prow(r >> 4, r & 15)) * 1152u + (unsigned)(ct * 72 + ch * 8) * 2u;
    asm volatile("global_load_dwordx4 %0, %1, %2" : "=v"(xv[e]) : "v"(gof[e]), "s"(x));
  }
  if (w < 4) {
    const rsrc_t rA = mk_rsrc(ws_c + OFF_HT, (int)SZ_Z);
    kc_main<false>(acc, rA, rW, lane * 16, lane * 8, lane * 16, RFL((unsigned)((rt * 4 * 16 + w * 4) * 4608)), sW);
  } else {
    const rsrc_t rA = mk_rsrc(ws_c + OFF_OT, (int)SZ_OT);
    kc_main<true>(acc, rA, rW, lane * 16, lane * 8, lane * 16, RFL((unsigned)(rt * 4 * 18432)), sW);
  }
  {
    char* p = smem + w * KC_PART + (lane & 15) * KC_PITCH + (lane >> 4) * 16;
#pragma unroll
    for (int m = 0; m < 4; ++m)
#pragma unroll
      for (int n = 0; n < 5; ++n) *(f32x4*)(p + m * 16 * KC_PITCH + n * 64) = acc[m][n];
  }
  asm volatile("s_waitcnt vmcnt(0) lgkmcnt(0)\n\ts_barrier" : "+v"(xv[0]), "+v"(xv[1]) :: "memory");
  const int own0 = ct * 72 - dstart;
#pragma unroll
  for (int e = 0; e < 2; ++e) {
    const int c = tid + e * 320;
    if (c < 576) {
      const int r = c / 9, ch = c - r * 9;
      const char* p = smem + r * KC_PITCH + (own0 + ch * 8) * 4;
      f32x4 s0 = *(const f32x4*)p, s1 = *(const f32x4*)(p + 16);
#pragma unroll
      for (int ww = 1; ww < 5; ++ww) { s0 += *(const f32x4*)(p + ww * KC_PART); s1 += *(const f32x4*)(p + ww * KC_PART + 16); }
      u32x4 o;
      o[0] = pk_bf16(s0[0] + bf_lo(xv[e][0]), s0[1] + bf_hi(xv[e][0]));
      o[1] = pk_bf16(s0[2] + bf_lo(xv[e][1]), s0[3] + bf_hi(xv[e][1]));
      o[2] = pk_bf16(s1[0] + bf_lo(xv[e][2]), s1[1] + bf_hi(xv[e][2]));
      o[3] = pk_bf16(s1[2] + bf_lo(xv[e][3]), s1[3] + bf_hi(xv[e][3]));
      st16((char*)out + gof[e], o);
    }
  }
}

constexpr int K0_BLOCKS = 280;
#define K0LD(p) __builtin_nontemporal_load(p)
#define XLD(p) (*(p))
constexpr int PF0_BYTES = 12288;
#define PF_CODE_N 16
#define PF_TLB_N 16
#define PF_TLB_STRIDE 65536
__global__ __launch_bounds__(256) void k0(const u16* __restrict__ x, const u16* __restrict__ wqkv, const u16* __restrict__ win, char* __restrict__ ws, const u16* __restrict__ wsp, const u16* __restrict__ wout, const u16* __restrict__ wo, const u16* __restrict__ bsp) {
  __shared__ __attribute__((aligned(16))) char sm[32 * K0_PITCH];
  const int tid = threadIdx.x, lane = tid & 63, i = lane & 15, g = lane >> 4, w = tid >> 6, b = blockIdx.x;
  const unsigned smb = (unsigned)(size_t)(__attribute__((address_space(3))) char*)sm;
  unsigned pf0 = 0, pf1 = 0, pf2 = 0;
  if (w == 3 && (b >> 3) < PF_CODE_N) {
    const int u = b >> 3;
    unsigned long long pa, p0;
    asm volatile("s_getpc_b64 s[20:21]\n\ts_add_u32 s20, s20, _Z2kAPKcPcPKtS3_S3_@rel32@lo+4\n\ts_addc_u32 s21, s21, _Z2kAPKcPcPKtS3_S3_@rel32@hi+12\n\ts_mov_b64 %0, s[20:21]" : "=s"(pa) :: "scc", "s20", "s21");
    asm volatile("s_getpc_b64 s[20:21]\n\ts_add_u32 s20, s20, _Z2k0PKtS0_S0_PcS0_S0_S0_S0_@rel32@lo+4\n\ts_addc_u32 s21, s21, _Z2k0PKtS0_S0_PcS0_S0_S0_S0_@rel32@hi+12\n\ts_mov_b64 %0, s[20:21]" : "=s"(p0) :: "scc", "s20", "s21");
    const long long span = (long long)(p0 - pa);
    int off = (u < 2 ? u : u - 2) * 8192 + lane * 128;
    const int lim = u < 2 ? PF0_BYTES : (span > 0 && span < (1 << 20) ? (int)span : 0);
    off = off < lim ? off : 0;
    const char* a = (const char*)(u < 2 ? p0 : pa) + off;
    asm volatile("global_load_dword %0, %1, off" : "=v"(pf0) : "v"(a));
  }
  if (b == 217 && w == 1) { const char* a = (const char*)bsp + (lane & 31) * 128; asm volatile("global_load_dword %0, %1, off" : "=v"(pf1) : "v"(a)); }
  if (b < 144) {
    const int ks = b >> 3, cb = b & 7;
    u32x4 v[9];
#pragma unroll
    for (int k = 0; k < 9; ++k) { int c = tid + k * 256, r = c / 72, ch = c - r * 72; v[k] = K0LD((const u32x4*)(win + (size_t)(ks * 32 + r) * 4608 + cb * 576 + ch * 8)); }
#pragma unroll
    for (int k = 0; k < 9; ++k) { int c = tid + k * 256, r = c / 72, ch = c - r * 72; *(u32x4*)(sm + r * K0_PITCH + ch * 16) = v[k]; }
    __syncthreads();
    const unsigned a0 = smb + (8 * g + (i >> 2)) * K0_PITCH + (i & 3) * 8;
#pragma unroll
    for (int e = 0; e < 9; ++e) {
      int nt = w * 9 + e;
      int gt = cb * 36 + nt;
      int z2 = gt >= 144, t = gt - z2 * 144, fb = t / 9, n = t - fb * 9;
      int fast = z2 ? n < 5 : n >= 4, nz = 4 + fast;
      int fr = fb * 468 + (fast ? 0 : 252) + 72 + (z2 ? ks * nz + n - (fast ? 0 : 5) : 18 * nz + ks * nz + n - (fast ? 4 : 0));
      u32x4 o = tr_frag(a0 + nt * 32, a0 + nt * 32 + 4 * K0_PITCH);
      st16(ws + OFF_W1 + (size_t)fr * 1024 + lane * 16, o);
    }
  } else if (b < 216) {
    const int u = b - 144, ks = u >> 2, hq = u & 3;
    u32x4 v[7]; int dst[7], dw0[7];
#pragma unroll
    for (int k = 0; k < 7; ++k) {
      int c = tid + k * 256; c = c < 32 * 54 ? c : 32 * 54 - 1;
      const int r = c / 54, e = c - r * 54, part = e / 18, ch = e - part * 18;
      v[k] = K0LD((const u32x4*)(wqkv + (size_t)(ks * 32 + r) * 1728 + part * 576 + hq * 144 + ch * 8));
      dst[k] = r * K0_PITCH + (part == 0 ? 80 : (part == 1 ? 0 : 40)); dw0[k] = ch * 4;
    }
#pragma unroll
    for (int z = 0; z < 5; ++z) {
      const int c = tid + z * 256, r = c / 40, e = c - r * 40, hl = e / 5, jj = e - hl * 5;
      *(unsigned*)(sm + r * K0_PITCH + hl * 128 + (jj == 0 ? 9 : (jj == 1 ? 19 : 27 + jj)) * 4) = 0u;
    }
#pragma unroll
    for (int k = 0; k < 7; ++k)
#pragma unroll
      for (int q = 0; q < 4; ++q) { const int dw = dw0[k] + q, hl = (dw * 57) >> 9; *(unsigned*)(sm + dst[k] + hl * 128 + (dw - hl * 9) * 4) = v[k][q]; }
    __syncthreads();
    const unsigned a0 = smb + (8 * g + (i >> 2)) * K0_PITCH + (i & 3) * 8;
#pragma unroll
    for (int e = 0; e < 8; ++e) {
      const int f = w * 8 + e, hl = f >> 2, n = f & 3;
      u32x4 o = tr_frag(a0 + f * 32, a0 + f * 32 + 4 * K0_PITCH);
      st16(ws + OFF_W1 + (size_t)(((hq * 8 + hl) >> 1) * 468 + (hl & 1) * 252 + ks * 4 + n) * 1024 + lane * 16, o);
    }
  } else {
    static_assert((2048 * 16 + 16 * 256) / 16 == 64 * 36, "stat words");
    if (tid < 36) st16(ws + OFF_RS + ((b - 216) * 36 + tid) * 16, (u32x4){0, 0, 0, 0});
    const int u = (b - 216) * 4 + w, tt = u >> 1, slot = (u & 1) * 8 + (lane >> 3), c0 = lane & 7;
    const u16* xr = x + (size_t)prow(tt, slot) * 576 + c0 * 8;
    u32x4 v[9];
#pragma unroll
    for (int k = 0; k < 9; ++k) v[k] = XLD((const u32x4*)(xr + k * 64));
    float s = 0.f;
#pragma unroll
    for (int k = 0; k < 9; ++k)
#pragma unroll
      for (int j = 0; j < 4; ++j) s += bf_lo(v[k][j]) + bf_hi(v[k][j]);
    s += __shfl_xor(s, 1); s += __shfl_xor(s, 2); s += __shfl_xor(s, 4);
    const float mean = s * (1.f / 576.f);
    float q = 0.f;
#pragma unroll
    for (int k = 0; k < 9; ++k)
#pragma unroll
      for (int j = 0; j < 4; ++j) { float a = bf_lo(v[k][j]) - mean, c = bf_hi(v[k][j]) - mean; q += a * a + c * c; }
    q += __shfl_xor(q, 1); q += __shfl_xor(q, 2); q += __shfl_xor(q, 4);
    const float mb = rbf(mean), sd = rbf(sqrtf(rbf(rbf(q * (1.f / 576.f)) + 1e-5f))), rs = 1.f / sd;
    char* dst = ws + OFF_XT + (size_t)tt * 18432 + slot * 16;
#pragma unroll
    for (int k = 0; k < 9; ++k) {
      u32x4 o;
#pragma unroll
      for (int j = 0; j < 4; ++j) o[j] = pk_bf16(rbf(bf_lo(v[k][j]) - mb) * rs, rbf(bf_hi(v[k][j]) - mb) * rs);
      int c = c0 + 8 * k;
      st16(dst + (c >> 2) * 1024 + (c & 3) * 256, o);
    }
  }
  asm volatile("s_waitcnt vmcnt(0)" : "+v"(pf0), "+v"(pf1), "+v"(pf2) :: "memory");
}

extern "C" void kernel_launch(void* const* d_in, const int* in_sizes, int n_in, void* d_out, int out_size, void* d_ws, size_t ws_size, hipStream_t stream) {
  (void)in_sizes; (void)n_in; (void)out_size; (void)ws_size;
  const u16* x = (const u16*)d_in[0];
  char* ws = (char*)d_ws;
  (void)hipFuncSetAttribute((const void*)kA, hipFuncAttributeMaxDynamicSharedMemorySize, KA_LDS);
  (void)hipFuncSetAttribute((const void*)kB, hipFuncAttributeMaxDynamicSharedMemorySize, KB_LDS);
  (void)hipFuncSetAttribute((const void*)kC, hipFuncAttributeMaxDynamicSharedMemorySize, KC_LDS);
  k0<<<K0_BLOCKS, 256, 0, stream>>>(x, (const u16*)d_in[1], (const u16*)d_in[4], ws, (const u16*)d_in[5], (const u16*)d_in[7], (const u16*)d_in[2], (const u16*)d_in[6]);
  kA<<<512, 256, KA_LDS, stream>>>(ws, ws, (const u16*)d_in[5], (const u16*)d_in[7], (const u16*)d_in[2]);
  kB<<<256, 512, KB_LDS, stream>>>(ws, ws, (const u16*)d_in[6]);
  kC<<<256, 320, KC_LDS, stream>>>(ws, x, (u16*)d_out);
}
```

```cpp
#include <hip/hip_runtime.h>
#include <hip/hip_bf16.h>
#include <stdint.h>

typedef unsigned short u16;
typedef __attribute__((ext_vector_type(8))) short bf16x8;
typedef __attribute__((ext_vector_type(4))) short bf16x4;
typedef __attribute__((ext_vector_type(4))) float f32x4;
typedef __attribute__((ext_vector_type(4))) unsigned u32x4;
typedef __attribute__((ext_vector_type(2))) unsigned u32x2;
typedef __amdgpu_buffer_rsrc_t rsrc_t;

constexpr int S_ = 2048, D_ = 576, F_ = 4608, FH_ = 2304;
constexpr size_t OFF_Z2T = 0;
constexpr size_t SZ_Z = (size_t)16 * 64 * 9 * 1024;
constexpr size_t OFF_Z1T = OFF_Z2T + SZ_Z;
constexpr size_t OFF_HT = OFF_Z1T + SZ_Z;
constexpr size_t OFF_WT = OFF_HT + SZ_Z;
constexpr size_t SZ_WT = (size_t)16 * 64 * 8 * 1024;
constexpr size_t OFF_OT = OFF_WT + SZ_WT;
constexpr size_t SZ_OT = (size_t)128 * 144 * 128;
constexpr size_t OFF_WC = OFF_OT + SZ_OT;
constexpr size_t SZ_WC = (size_t)8 * 90 * 5 * 1024;
constexpr size_t OFF_XT = OFF_WC + SZ_WC;
constexpr size_t SZ_XT = (size_t)128 * 18 * 1024;
constexpr size_t OFF_W1 = OFF_XT + SZ_XT;
constexpr size_t SZ_W1 = (size_t)32 * 234 * 1024;
constexpr size_t OFF_RS = OFF_W1 + SZ_W1;
constexpr size_t OFF_CNT = OFF_RS + 2048 * 16;
constexpr size_t OFF_ST = OFF_CNT + 16 * 256;
constexpr size_t WS_END = OFF_ST + (1 << 20);

#define RFL(x) __builtin_amdgcn_readfirstlane(x)
#define SCHED() __builtin_amdgcn_sched_barrier(0)
__device__ __forceinline__ rsrc_t mk_rsrc(const void* p, int bytes) { return __builtin_amdgcn_make_buffer_rsrc((void*)p, (short)0, bytes, 0x00020000); }
__device__ __forceinline__ float bf_lo(unsigned w) { return __uint_as_float(w << 16); }
__device__ __forceinline__ float bf_hi(unsigned w) { return __uint_as_float(w & 0xffff0000u); }
__device__ __forceinline__ unsigned pk_bf16(float lo, float hi) {
  typedef __attribute__((ext_vector_type(2))) float f32x2; typedef __attribute__((ext_vector_type(2))) __bf16 bf2;
  f32x2 v = {lo, hi}; bf2 r = __builtin_convertvector(v, bf2); return __builtin_bit_cast(unsigned, r);
}
__device__ __forceinline__ float rbf(float f) { return bf_hi(pk_bf16(0.f, f)); }
__host__ __device__ __forceinline__ int prow(int tt, int i) { return 32 * (tt >> 1) + 8 * (i >> 2) + 4 * (tt & 1) + (i & 3); }
__device__ __forceinline__ void st16(void* p, u32x4 v) { asm volatile("global_store_dwordx4 %0, %1, off sc1\n\ts_nop 1" :: "v"(p), "v"(v) : "memory"); }
template <int IMM> __device__ __forceinline__ void st16s(const char* base, unsigned voff, u32x4 v) {
  asm volatile("global_store_dwordx4 %0, %1, %2 offset:%3 sc1\n\ts_nop 1" :: "v"(voff), "v"(v), "s"(base), "i"(IMM) : "memory"); }
__device__ __forceinline__ void st8(void* p, u32x2 v) { asm volatile("global_store_dwordx2 %0, %1, off sc1\n\ts_nop 1" :: "v"(p), "v"(v) : "memory"); }
__device__ __forceinline__ void st4(void* p, unsigned v) { asm volatile("global_store_dword %0, %1, off sc1\n\ts_nop 1" :: "v"(p), "v"(v) : "memory"); }
#define MFMA(a, b, c) __builtin_amdgcn_mfma_f32_16x16x32_bf16(a, b, c, 0, 0, 0)

#define DMA16(lds, voff, srd, soff, imm) asm volatile("s_mov_b32 m0, %0\n\ts_nop 0\n\tbuffer_load_dwordx4 %1, %2, %3 offen offset:" #imm " lds" :: "s"(lds), "v"(voff), "s"(srd), "s"(soff) : "memory")
#define DMA16H(lds, voff, srd, soff) asm volatile("s_mov_b32 m0, %0\n\ts_lshr_b64 exec, -1, 32\n\tbuffer_load_dwordx4 %1, %2, %3 offen lds\n\ts_mov_b64 exec, -1" :: "s"(lds), "v"(voff), "s"(srd), "s"(soff) : "memory")
typedef __attribute__((address_space(3))) char lds_char;
#define LDS_RD128(addr) (*(const __attribute__((address_space(3))) bf16x8*)(addr))
#define AMFMA(acc, a, b) asm volatile("v_mfma_f32_16x16x32_bf16 %0, %1, %2, %0" : "+v"(acc) : "v"(a), "v"(b))
#define ADSRD(dst, addr, off) asm volatile("ds_read_b128 %0, %1 offset:%2" : "=v"(dst) : "v"(addr), "i"(off))
#define OPAQUE(x) asm volatile("" : "+v"(x))
#define BLOAD16(dst, voff, srd, soff, imm) asm volatile("buffer_load_dwordx4 %0, %1, %2, %3 offen offset:" #imm : "=v"(dst) : "v"(voff), "s"(srd), "s"(soff))

constexpr int K0_PITCH = 1168;
__device__ __forceinline__ int wc_krow(int ks, int g, int j) {
  int r = j & 3, hi = j >> 2;
  if (ks >= 72) { int u = ks - 72; return 2304 + 4 * (8 * u + 4 * hi + g) + r; }
  int w = ks / 18, pi = (ks % 18) / 9, i = ks % 9, fb0 = 4 * w + 2 * pi;
  if (i == 4) return 144 * (fb0 + hi) + 128 + 4 * g + r;
  if (i < 4) return 144 * fb0 + 32 * i + 16 * hi + 4 * g + r;
  return 144 * (fb0 + 1) + 32 * (i - 5) + 16 * hi + 4 * g + r;
}
__device__ __forceinline__ u32x4 tr_frag(unsigned a0, unsigned a1) {
  u32x2 lo, hi;
  asm volatile("ds_read_b64_tr_b16 %0, %2\n\tds_read_b64_tr_b16 %1, %3\n\ts_waitcnt lgkmcnt(0)" : "=&v"(lo), "=&v"(hi) : "v"(a0), "v"(a1) : "memory");
  return (u32x4){lo[0], lo[1], hi[0], hi[1]};
}
#define KA_SLEEP 32
#define PFA_POL ""
#define SJLD(p) (*(p))
constexpr int KA_SLOT = 10240, KA_ATT = 6 * KA_SLOT, KA_PITCH = 144, KA_LDS = KA_ATT + 142 * KA_PITCH;
template <int FAST> struct KaCfg {
  static constexpr int NZ2 = 4 + FAST, NZ1 = 4 + FAST;
  static constexpr int nt(int s) { return s < 9 ? 4 : 4 + FAST; }
  static constexpr int goff(int s) { return s < 9 ? s * 8192 : 73728 + (s - 9) * (4 + FAST) * 2048; }
  static constexpr int ops(int s) { return s > 26 ? 0 : 2 + (nt(s) == 5 ? 1 : 0) + (s < 9 ? 4 + ((s & 1) ? 0 : 1) : 0); }
  static constexpr int wait(int s) { return s == 20 ? ops(23) + ops(24) : ops(s + 2) + ops(s + 3) + ops(s + 4) + ((s >= 9 && s <= 12) ? 6 : 0); }
};
template <int V> struct IC { static constexpr int v = V; };
#define BLD(dst, voff, srd, soff, IMM) asm volatile("buffer_load_dwordx4 %0, %1, %2, %3 offen offset:%4" : "=v"(dst) : "v"(voff), "s"(srd), "s"(soff), "i"(IMM))
#define AMFMA0(acc, a, b) asm volatile("v_mfma_f32_16x16x32_bf16 %0, %1, %2, 0" : "=&v"(acc) : "v"(a), "v"(b))
typedef __attribute__((ext_vector_type(2))) float f32x2;
typedef __attribute__((ext_vector_type(2))) __bf16 bf2_t;
__device__ __forceinline__ float dot2(unsigned a, unsigned b, float c) { return __builtin_amdgcn_fdot2_f32_bf16(__builtin_bit_cast(bf2_t, a), __builtin_bit_cast(bf2_t, b), c, false); }
__device__ __forceinline__ f32x2 gelu2(f32x2 x) {
  f32x2 t = x * x, p = t * (-0.10294324f) + (-2.3022082f), e = x * p;
  f32x2 d = {__builtin_amdgcn_exp2f(e[0]), __builtin_amdgcn_exp2f(e[1])};
  d += 1.f;
  f32x2 r = {__builtin_amdgcn_rcpf(d[0]), __builtin_amdgcn_rcpf(d[1])};
  return x * r;
}
template <int CTRL> __device__ __forceinline__ float dpp_add(float v) {
  return v + __builtin_bit_cast(float, __builtin_amdgcn_update_dpp(0, __builtin_bit_cast(int, v), CTRL, 0xf, 0xf, true));
}

__device__ __forceinline__ float xmax16(float v) { auto r = __builtin_amdgcn_permlane16_swap(__float_as_uint(v), __float_as_uint(v), false, false); return fmaxf(__uint_as_float(r[0]), __uint_as_float(r[1])); }
__device__ __forceinline__ float xmax32(float v) { auto r = __builtin_amdgcn_permlane32_swap(__float_as_uint(v), __float_as_uint(v), false, false); return fmaxf(__uint_as_float(r[0]), __uint_as_float(r[1])); }
__device__ __forceinline__ float xadd16(float v) { auto r = __builtin_amdgcn_permlane16_swap(__float_as_uint(v), __float_as_uint(v), false, false); return __uint_as_float(r[0]) + __uint_as_float(r[1]); }
__device__ __forceinline__ float xadd32(float v) { auto r = __builtin_amdgcn_permlane32_swap(__float_as_uint(v), __float_as_uint(v), false, false); return __uint_as_float(r[0]) + __uint_as_float(r[1]); }
#define PRIO_HI()
#define PRIO_LO()
#define LBAR() asm volatile("s_waitcnt lgkmcnt(0)\n\ts_barrier" ::: "memory")
__device__ __forceinline__ float swap1(float v) { return __builtin_bit_cast(float, __builtin_amdgcn_update_dpp(0, __builtin_bit_cast(int, v), 0xB1, 0xf, 0xf, true)); }
template <int FAST>
__device__ __forceinline__ void ka_body(char* smem, const char* __restrict__ ws_c, char* __restrict__ ws, const int p, const int cg, const int sj, const u16* __restrict__ wsp, const u16* __restrict__ wout, const u16* __restrict__ wo) {
  typedef KaCfg<FAST> C;
  const int tid = threadIdx.x, lane = tid & 63, li = lane & 15, lg = lane >> 4;
  const int w = RFL(tid >> 6), fb = cg >> 1;
  const rsrc_t rW = mk_rsrc(ws_c + OFF_W1 + (size_t)fb * (468 * 1024) + (FAST ? 0 : 252 * 1024), (FAST ? 252 : 216) * 1024);
  const rsrc_t rX = mk_rsrc(ws_c + OFF_XT, (int)SZ_XT);
  const unsigned ldsb = (unsigned)(size_t)(__attribute__((address_space(3))) char*)smem;
  const unsigned vl16 = lane * 16;
  const unsigned vX = vl16 + (unsigned)(p * 8 + w * 2) * 18432u;
  const unsigned vH = p == 0 ? 0x80000000u : (unsigned)(p * 8 - 2 + (li >> 3)) * 18432u + (unsigned)(lg * 16 + (li | 8)) * 16u + (unsigned)w * 1024u;
  const unsigned sF = RFL((unsigned)w * 2048u), sH = RFL(8192u + (unsigned)w * 512u);
  const unsigned rd = ldsb + vl16;
  constexpr int KA_WARM = 0;
  bf16x8 X[2][18], HX[5], F[5];
  f32x4 acc[2][5], hacc[3];
#pragma unroll
  for (int n = 0; n < 3; ++n) hacc[n] = (f32x4){0.f, 0.f, 0.f, 0.f};

  bf16x8 (&X_)[2][18] = X; bf16x8 (&HX_)[5] = HX; bf16x8 (&F_)[5] = F; f32x4 (&acc_)[2][5] = acc; f32x4 (&hacc_)[3] = hacc;
  const rsrc_t rW_ = rW, rX_ = rX; const unsigned vl16_ = vl16, vX_ = vX, vH_ = vH, rd_ = rd;
#define KA_CAP bf16x8 (&X)[2][18] = X_; bf16x8 (&HX)[5] = HX_; bf16x8 (&F)[5] = F_; f32x4 (&acc)[2][5] = acc_; f32x4 (&hacc)[3] = hacc_; \
    const rsrc_t rW = rW_, rX = rX_; const unsigned vl16 = vl16_, vX = vX_, vH = vH_, rd = rd_; (void)X; (void)HX; (void)F; (void)acc; (void)hacc; (void)rW; (void)rX; (void)vl16; (void)vX; (void)vH; (void)rd;
  auto vop = [&](auto S_, auto K_) {
    KA_CAP
    constexpr int s = decltype(S_)::v, k = decltype(K_)::v;
    if constexpr (s <= 26) {
      constexpr int slot = s % 6, nd = 2 + (C::nt(s) == 5 ? 1 : 0);
      if constexpr (k == 0) { unsigned lo = ldsb + slot * KA_SLOT + sF, so = C::goff(s) + sF; DMA16(lo, vl16, rW, so, 0); }
      else if constexpr (k == 1) { unsigned lo = ldsb + slot * KA_SLOT + sF, so = C::goff(s) + sF; DMA16(lo, vl16, rW, so, 1024); }
      else if constexpr (k == 2 && nd == 3) { unsigned lo = ldsb + slot * KA_SLOT + sH, so = C::goff(s) + sH; DMA16H(lo, vl16, rW, so); }
      else if constexpr (s < 9 && k >= 2 && k < 6) {
        constexpr int m = (k - 2) & 1, ks = 2 * s + ((k - 2) >> 1);
        BLD(X[m][ks], vX, rX, m * 18432 + (ks & ~3) * 1024, (ks & 3) * 1024);
      } else if constexpr (s < 9 && k == 6 && !(s & 1)) { BLD(HX[s >> 1], vH, rX, (s >> 1) * 4096, 0); }
    }
  };
  auto group = [&](auto S_) { vop(S_, IC<0>{}); vop(S_, IC<1>{}); vop(S_, IC<2>{}); vop(S_, IC<3>{}); vop(S_, IC<4>{}); vop(S_, IC<5>{}); vop(S_, IC<6>{}); };
  auto sync = [&](auto S_) {
    KA_CAP
    constexpr int s = decltype(S_)::v;
    if constexpr (s < 26) {
      if constexpr (s + 1 < 9) {
        constexpr int k0 = 2 * (s + 1);
        asm volatile("s_waitcnt vmcnt(%5)" : "+v"(X[0][k0]), "+v"(X[1][k0]), "+v"(X[0][k0 + 1]), "+v"(X[1][k0 + 1]), "+v"(HX[(s + 1) >> 1]) : "i"(C::wait(s)) : "memory");
      } else asm volatile("s_waitcnt vmcnt(%0)" :: "i"(C::wait(s)) : "memory");
      __builtin_amdgcn_s_barrier();
    }
  };
  auto step = [&](auto S_, auto H_) {
    KA_CAP
    constexpr int s = decltype(S_)::v, h = decltype(H_)::v;
    constexpr int NT = C::nt(s), job = s / 9, ks = 2 * (s % 9) + h;
    constexpr int s2 = h ? s + 1 : s, h2 = h ^ 1, NT2 = s2 <= 26 ? C::nt(s2) : 0;
    constexpr int roff = (s2 % 6) * KA_SLOT + h2 * NT2 * 1024;
    if constexpr (ks == 0) asm volatile("s_waitcnt lgkmcnt(0)" ::: "memory");
    if constexpr (job == 0) {
      if (w == (ks & 3)) {
        asm volatile("s_waitcnt lgkmcnt(1)" ::: "memory");
#pragma unroll
        for (int n = 0; n < 3; ++n) AMFMA(hacc[n], F[n], HX[ks >> 2]);
      }
    }
#pragma unroll
    for (int n = 0; n < NT; ++n) {
      if constexpr (ks != 0) asm volatile("s_waitcnt lgkmcnt(%0)" :: "i"(NT - 1) : "memory");
      if constexpr (job == 1) {
        if constexpr (ks == 0) { AMFMA0(acc[0][n], X[0][ks], F[n]); AMFMA0(acc[1][n], X[1][ks], F[n]); }
        else { AMFMA(acc[0][n], X[0][ks], F[n]); AMFMA(acc[1][n], X[1][ks], F[n]); }
      } else {
        if constexpr (ks == 0) { AMFMA0(acc[0][n], F[n], X[0][ks]); AMFMA0(acc[1][n], F[n], X[1][ks]); }
        else { AMFMA(acc[0][n], F[n], X[0][ks]); AMFMA(acc[1][n], F[n], X[1][ks]); }
      }
      if (n < NT2) ADSRD(F[n], rd, roff + n * 1024);
      if constexpr (h == 1) {
        if (n == 0) { vop(IC<s + 5>{}, IC<0>{}); vop(IC<s + 5>{}, IC<2>{}); }
        if (n == 1) { vop(IC<s + 5>{}, IC<1>{}); vop(IC<s + 5>{}, IC<3>{}); }
        if (n == 2) { vop(IC<s + 5>{}, IC<4>{}); vop(IC<s + 5>{}, IC<5>{}); }
        if (n == 3) { vop(IC<s + 5>{}, IC<6>{}); }
      }
    }
    if constexpr (NT2 > NT) ADSRD(F[4], rd, roff + 4 * 1024);
  };
#define KA_ACC_DONE() do { asm volatile("s_nop 15" : "+v"(acc[0][0]), "+v"(acc[0][1]), "+v"(acc[0][2]), "+v"(acc[0][3]), "+v"(acc[1][0]), "+v"(acc[1][1]), "+v"(acc[1][2]), "+v"(acc[1][3]) :: "memory"); \
    if constexpr (FAST) asm volatile("" : "+v"(acc[0][4]), "+v"(acc[1][4]) :: "memory"); } while (0)
  auto iter = [&](auto S_) { step(S_, IC<0>{}); sync(S_); step(S_, IC<1>{}); };

  group(IC<0>{}); group(IC<1>{});
  asm volatile("s_waitcnt vmcnt(%5)" : "+v"(X[0][0]), "+v"(X[1][0]), "+v"(X[0][1]), "+v"(X[1][1]), "+v"(HX[0]) : "i"(C::ops(1) + KA_WARM) : "memory");
  __builtin_amdgcn_s_barrier();
#pragma unroll
  for (int n = 0; n < 4; ++n) ADSRD(F[n], rd, n * 1024);

  step(IC<0>{}, IC<0>{}); group(IC<2>{}); group(IC<3>{}); group(IC<4>{}); sync(IC<0>{}); step(IC<0>{}, IC<1>{});
  iter(IC<1>{}); iter(IC<2>{}); iter(IC<3>{}); iter(IC<4>{}); iter(IC<5>{}); iter(IC<6>{}); iter(IC<7>{}); iter(IC<8>{});
  asm volatile("s_nop 15" : "+v"(acc[0][0]), "+v"(acc[0][1]), "+v"(acc[0][2]), "+v"(acc[0][3]), "+v"(acc[1][0]), "+v"(acc[1][1]), "+v"(acc[1][2]), "+v"(acc[1][3]), "+v"(hacc[0]), "+v"(hacc[1]), "+v"(hacc[2]) :: "memory");

  SCHED();
  {
    char* att = smem + KA_ATT;
    char* hp = smem + 2 * KA_SLOT;
#pragma unroll
    for (int n = 0; n < 3; ++n) if (n != w) *(f32x4*)(hp + (n * 3 + (w > n ? w - 1 : w)) * 1024 + vl16) = hacc[n];
#pragma unroll
    for (int m = 0; m < 2; ++m) {
      char* rp = att + (14 + w * 32 + 8 * (li >> 2) + 4 * m + (li & 3)) * KA_PITCH + lg * 8;
#pragma unroll
      for (int n = 0; n < 4; ++n) *(u32x2*)(rp + n * 32) = (u32x2){pk_bf16(acc[m][n][0], acc[m][n][1]), pk_bf16(acc[m][n][2], acc[m][n][3])};
    }
    LBAR();
    if (w < 3) {
      f32x4 a = w == 0 ? hacc[0] : (w == 1 ? hacc[1] : hacc[2]);
#pragma unroll
      for (int q = 0; q < 3; ++q) a += *(const f32x4*)(hp + (w * 3 + q) * 1024 + vl16);
      int ho = (li < 8) ? 8 * (li >> 2) + (li & 3) : 8 * ((li - 8) >> 2) + 4 + (li & 3);
      if (ho >= 2) *(u32x2*)(att + (ho - 2) * KA_PITCH + w * 32 + lg * 8) = (u32x2){pk_bf16(a[0], a[1]), pk_bf16(a[2], a[3])};
    }
    LBAR();
    const unsigned attb = ldsb + KA_ATT;
    float mbias[8];
#pragma unroll
    for (int e = 0; e < 8; ++e) { int d = (e >> 2) * 16 + lg * 4 + (e & 3) - li; mbias[e] = (!(d & 1) && d >= 2 && d <= 16) ? 0.f : -__builtin_inff(); }
    bf16x8 Q[2], K[2][2]; u32x2 V[2][2][2];
#pragma unroll
    for (int jj = 0; jj < 2; ++jj) {
      const int j = w * 2 + jj;
      Q[jj] = *(const bf16x8*)(att + (14 + 16 * j + li) * KA_PITCH + 80 + (lg < 3 ? lg : 2) * 16);
#pragma unroll
      for (int kt = 0; kt < 2; ++kt) {
        int R = 16 * j - 2 + 16 * kt + li; R = R < 0 ? 0 : R;
        K[jj][kt] = *(const bf16x8*)(att + R * KA_PITCH + lg * 16);
        int Rv = 16 * j - 2 + 16 * kt + 4 * lg + (li >> 2); Rv = Rv < 0 ? 0 : Rv;
        const unsigned va = attb + Rv * KA_PITCH + 40 + (li & 3) * 8;
        asm volatile("ds_read_b64_tr_b16 %0, %2\n\tds_read_b64_tr_b16 %1, %2 offset:32" : "=&v"(V[jj][0][kt]), "=&v"(V[jj][1][kt]) : "v"(va) : "memory");
      }
    }
    f32x4 S[2][2];
#pragma unroll
    for (int jj = 0; jj < 2; ++jj) {
      if (lg == 3) Q[jj] = (bf16x8){0, 0, 0, 0, 0, 0, 0, 0};
#pragma unroll
      for (int kt = 0; kt < 2; ++kt) S[jj][kt] = MFMA(K[jj][kt], Q[jj], ((f32x4){0.f, 0.f, 0.f, 0.f}));
    }
    bf16x8 P[2];
#pragma unroll
    for (int jj = 0; jj < 2; ++jj) {
      float l[8];
#pragma unroll
      for (int e = 0; e < 8; ++e) l[e] = S[jj][e >> 2][e & 3] + mbias[e];
      if (p == 0 && w == 0 && jj == 0) {
#pragma unroll
        for (int e = 0; e < 4; ++e) l[e] = -__builtin_inff();
      }
      float mx = fmaxf(fmaxf(fmaxf(l[0], l[1]), fmaxf(l[2], l[3])), fmaxf(fmaxf(l[4], l[5]), fmaxf(l[6], l[7])));
      mx = xmax16(mx); mx = xmax32(mx);
      constexpr float CE = 0.23570226f * 1.44269504f;
      const float nm = -mx * CE;
      float sum = 0.f;
#pragma unroll
      for (int e = 0; e < 8; ++e) { l[e] = __builtin_amdgcn_exp2f(l[e] * CE + nm); sum += l[e]; }
      sum = xadd16(sum); sum = xadd32(sum);
      const float inv = __builtin_amdgcn_rcpf(sum);
      u32x4 pk = {pk_bf16(l[0] * inv, l[1] * inv), pk_bf16(l[2] * inv, l[3] * inv), pk_bf16(l[4] * inv, l[5] * inv), pk_bf16(l[6] * inv, l[7] * inv)};
      P[jj] = __builtin_bit_cast(bf16x8, pk);
    }
    asm volatile("s_waitcnt lgkmcnt(0)" : "+v"(V[0][0][0]), "+v"(V[0][0][1]), "+v"(V[0][1][0]), "+v"(V[0][1][1]), "+v"(V[1][0][0]), "+v"(V[1][0][1]), "+v"(V[1][1][0]), "+v"(V[1][1][1]) :: "memory");
#pragma unroll
    for (int jj = 0; jj < 2; ++jj) {
      f32x4 O[2];
#pragma unroll
      for (int dt = 0; dt < 2; ++dt) {
        u32x4 vv = {V[jj][dt][0][0], V[jj][dt][0][1], V[jj][dt][1][0], V[jj][dt][1][1]};
        O[dt] = MFMA(__builtin_bit_cast(bf16x8, vv), P[jj], ((f32x4){0.f, 0.f, 0.f, 0.f}));
      }
      const int tl = jj * 16 + li, tt = p * 8 + w * 2 + ((tl >> 2) & 1), slot = (tl >> 3) * 4 + (tl & 3);
      char* ob = ws + OFF_OT + (size_t)tt * 18432 + slot * 8;
      const int d0 = cg * 18 + lg * 4, d1 = d0 + 2, d2 = cg * 18 + 16;
      st4(ob + (d0 >> 2) * 128 + (d0 & 3) * 2, pk_bf16(O[0][0], O[0][1]));
      st4(ob + (d1 >> 2) * 128 + (d1 & 3) * 2, pk_bf16(O[0][2], O[0][3]));
      if (lg == 0) st4(ob + (d2 >> 2) * 128 + (d2 & 3) * 2, pk_bf16(O[1][0], O[1][1]));
    }
  }
  SCHED();
  PRIO_LO();

  iter(IC<9>{}); iter(IC<10>{}); iter(IC<11>{}); iter(IC<12>{}); iter(IC<13>{}); iter(IC<14>{}); iter(IC<15>{}); iter(IC<16>{}); iter(IC<17>{});
  KA_ACC_DONE();

  SCHED();
  if constexpr (FAST) {
    const unsigned dump = RFL(ldsb + KA_ATT + 2048 + (unsigned)w * 256u);
    {
      const rsrc_t rS = mk_rsrc(wsp, 2048 * 4096);
      const int ks = (sj * 4 + w) & 63, tp = (sj * 4 + w) >> 6, r = (w & 1) * 64 + lane;
      const unsigned vo = (unsigned)prow(tp * 8 + (r >> 4), r & 15) * 4096u + (unsigned)(ks >> 1) * 128u;
      asm volatile("s_mov_b32 m0, %0\n\ts_nop 0\n\tbuffer_load_dword %1, %2, 0 offen" PFA_POL " lds" :: "s"(dump), "v"(vo), "s"(rS) : "memory");
    }
    if (sj < 90) {
      const int base0 = wc_krow(sj, 0, 0), base1 = wc_krow(sj, 0, 4);
      const rsrc_t rS = sj < 72 ? mk_rsrc(wout, 2304 * 1152) : mk_rsrc(wo - (size_t)2304 * 576, 2880 * 1152);
      const int c = tid, r = c / 9, l = c - r * 9;
      const unsigned vo = (unsigned)((r < 16 ? base0 : base1 - 16) + r) * 1152u + (unsigned)l * 128u;
      asm volatile("s_mov_b32 m0, %0\n\ts_nop 0\n\tbuffer_load_dword %1, %2, 0 offen" PFA_POL " lds" :: "s"(dump), "v"(vo), "s"(rS) : "memory");
      if (w == 0) {
        const int c2 = 256 + (lane & 31), r2 = c2 / 9, l2 = c2 - r2 * 9;
        const unsigned vo2 = (unsigned)(base1 - 16 + r2) * 1152u + (unsigned)l2 * 128u;
        asm volatile("s_mov_b32 m0, %0\n\ts_nop 0\n\tbuffer_load_dword %1, %2, 0 offen" PFA_POL " lds" :: "s"(dump), "v"(vo2), "s"(rS) : "memory");
      }
    }
  }
  SCHED();
  PRIO_HI();
  unsigned zs[C::NZ2][4];
  {
#pragma unroll
    for (int n = 0; n < C::NZ2; ++n) {
#pragma unroll
      for (int m = 0; m < 2; ++m) {
        f32x2 a = gelu2((f32x2){acc[m][n][0], acc[m][n][1]}), b = gelu2((f32x2){acc[m][n][2], acc[m][n][3]});
        zs[n][2 * m] = pk_bf16(a[0], a[1]); zs[n][2 * m + 1] = pk_bf16(b[0], b[1]);
      }
    }
    f32x2 s1[4], s2[4];
#pragma unroll
    for (int j = 0; j < 4; ++j) {
      s1[j] = (f32x2){0.f, 0.f}; s2[j] = s1[j];
#pragma unroll
      for (int n = 0; n < C::NZ2; ++n) { f32x2 v = {bf_lo(zs[n][j]), bf_hi(zs[n][j])}; s1[j] += v; s2[j] += v * v; }
    }
    float r[16];
#pragma unroll
    for (int j = 0; j < 4; ++j) { r[2 * j] = s1[j][0]; r[2 * j + 1] = s1[j][1]; r[8 + 2 * j] = s2[j][0]; r[9 + 2 * j] = s2[j][1]; }
    float sel;
    {
      float a[8], c[4];
#pragma unroll
      for (int j = 0; j < 8; j += 4)
        asm volatile("s_nop 1\n\t"
          "v_add_f32_dpp %0, %4, %4 row_ror:8 row_mask:0xf bank_mask:0x3\n\tv_add_f32_dpp %1, %5, %5 row_ror:8 row_mask:0xf bank_mask:0x3\n\t"
          "v_add_f32_dpp %2, %6, %6 row_ror:8 row_mask:0xf bank_mask:0x3\n\tv_add_f32_dpp %3, %7, %7 row_ror:8 row_mask:0xf bank_mask:0x3\n\t"
          "v_add_f32_dpp %0, %8, %8 row_ror:8 row_mask:0xf bank_mask:0xc\n\tv_add_f32_dpp %1, %9, %9 row_ror:8 row_mask:0xf bank_mask:0xc\n\t"
          "v_add_f32_dpp %2, %10, %10 row_ror:8 row_mask:0xf bank_mask:0xc\n\tv_add_f32_dpp %3, %11, %11 row_ror:8 row_mask:0xf bank_mask:0xc"
          : "=&v"(a[j]), "=&v"(a[j + 1]), "=&v"(a[j + 2]), "=&v"(a[j + 3])
          : "v"(r[j]), "v"(r[j + 1]), "v"(r[j + 2]), "v"(r[j + 3]), "v"(r[j + 8]), "v"(r[j + 9]), "v"(r[j + 10]), "v"(r[j + 11]));
      asm volatile("s_nop 1\n\t"
          "v_add_f32_dpp %0, %4, %4 row_half_mirror row_mask:0xf bank_mask:0x5\n\tv_add_f32_dpp %1, %5, %5 row_half_mirror row_mask:0xf bank_mask:0x5\n\t"
          "v_add_f32_dpp %2, %6, %6 row_half_mirror row_mask:0xf bank_mask:0x5\n\tv_add_f32_dpp %3, %7, %7 row_half_mirror row_mask:0xf bank_mask:0x5\n\t"
          "v_add_f32_dpp %0, %8, %8 row_half_mirror row_mask:0xf bank_mask:0xa\n\tv_add_f32_dpp %1, %9, %9 row_half_mirror row_mask:0xf bank_mask:0xa\n\t"
          "v_add_f32_dpp %2, %10, %10 row_half_mirror row_mask:0xf bank_mask:0xa\n\tv_add_f32_dpp %3, %11, %11 row_half_mirror row_mask:0xf bank_mask:0xa\n\ts_nop 1"
          : "=&v"(c[0]), "=&v"(c[1]), "=&v"(c[2]), "=&v"(c[3])
          : "v"(a[0]), "v"(a[1]), "v"(a[2]), "v"(a[3]), "v"(a[4]), "v"(a[5]), "v"(a[6]), "v"(a[7]));
#pragma unroll
      for (int j = 0; j < 4; ++j) c[j] = dpp_add<0x4E>(c[j]);
      float d0 = (li & 2) ? c[2] : c[0], d1 = (li & 2) ? c[3] : c[1];
      d0 = dpp_add<0xB1>(d0); d1 = dpp_add<0xB1>(d1);
      sel = (li & 1) ? d1 : d0;
    }
    double* dst = (double*)(ws + OFF_RS) + (size_t)(p * 128 + w * 32 + lg * 8 + (li & 7)) * 2 + (li >> 3);
    double dv = (double)sel;
    asm volatile("global_atomic_add_f64 %0, %1, off sc1" :: "v"(dst), "v"(dv) : "memory");
  }
  SCHED();

  PRIO_LO();

  unsigned* cnt = (unsigned*)(ws + OFF_CNT + p * 256);
  iter(IC<18>{}); iter(IC<19>{});
  step(IC<20>{}, IC<0>{}); sync(IC<20>{});
  if (tid == 0) { unsigned one = 1; asm volatile("global_atomic_add %0, %1, off sc1" :: "v"(cnt), "v"(one) : "memory"); }
  step(IC<20>{}, IC<1>{});
  iter(IC<21>{}); iter(IC<22>{}); iter(IC<23>{}); iter(IC<24>{}); iter(IC<25>{});
  unsigned seen;
  asm volatile("global_load_dword %0, %1, off sc1" : "=v"(seen) : "v"(cnt) : "memory");
  iter(IC<26>{});
  KA_ACC_DONE();
  asm volatile("s_waitcnt vmcnt(0)" : "+v"(seen) :: "memory");
  PRIO_HI();
  const unsigned seen_s = RFL(seen);
  const bool early = seen_s >= 32u;
  const char* sp = ws + OFF_RS + (size_t)(p * 128 + w * 32 + (lane & 31)) * 16;
  u32x4 st = {0, 0, 0, 0};
  asm volatile("s_cmp_lt_u32 %2, 32\n\ts_cbranch_scc1 1f\n\tglobal_load_dwordx4 %0, %1, off sc1\n1:" : "+v"(st) : "v"(sp), "s"(seen_s) : "memory", "scc");
  SCHED();
  u32x4 wt[8];
  if constexpr (FAST) {
    const int ks = (sj * 4 + w) & 63, tp = (sj * 4 + w) >> 6;
    const rsrc_t rS = mk_rsrc(wsp, 2048 * 4096);
    const unsigned vo = (unsigned)(8 * (li >> 2) + (li & 3)) * 4096u + (unsigned)lg * 16u, so = (unsigned)tp * (128u * 4096u) + (unsigned)ks * 64u;
#pragma unroll
    for (int m = 0; m < 8; ++m) wt[m] = __builtin_amdgcn_raw_buffer_load_b128(rS, vo, so + (unsigned)(32 * (m >> 1) + 4 * (m & 1)) * 4096u, 0);
  }
#pragma unroll
  for (int m = 0; m < 2; ++m) {
    const char* zp = ws + OFF_Z1T + ((size_t)(p * 8 + w * 2 + m) * 16 + fb) * 4608;
    unsigned pk[C::NZ1][2];
#pragma unroll
    for (int n = 0; n < C::NZ1; ++n) {
      f32x2 a = gelu2((f32x2){acc[m][n][0], acc[m][n][1]}), b = gelu2((f32x2){acc[m][n][2], acc[m][n][3]});
      pk[n][0] = pk_bf16(a[0], a[1]); pk[n][1] = pk_bf16(b[0], b[1]);
    }
    st16s<FAST * 2048>(zp, vl16, (u32x4){pk[0][0], pk[0][1], pk[1][0], pk[1][1]});
    st16s<FAST * 2048 + 1024>(zp, vl16, (u32x4){pk[2][0], pk[2][1], pk[3][0], pk[3][1]});
    if constexpr (FAST) st8((char*)zp + 4096 + lane * 8, (u32x2){pk[4][0], pk[4][1]});
  }
  SCHED();
  if constexpr (FAST) {
    const char* wb = ws + OFF_WT + (size_t)(sj * 4 + w) * 8192;
    st16s<0>(wb, vl16, wt[0]); st16s<1024>(wb, vl16, wt[1]); st16s<2048>(wb, vl16, wt[2]); st16s<3072>(wb, vl16, wt[3]);
    st16s<0>(wb + 4096, vl16, wt[4]); st16s<1024>(wb + 4096, vl16, wt[5]); st16s<2048>(wb + 4096, vl16, wt[6]); st16s<3072>(wb + 4096, vl16, wt[7]);
    if (sj < 90) {
      const int ks = sj, base0 = wc_krow(ks, 0, 0), base1 = wc_krow(ks, 0, 4);
      const rsrc_t rS = sj < 72 ? mk_rsrc(wout, 2304 * 1152) : mk_rsrc(wo - (size_t)2304 * 576, 2880 * 1152);
      const int r = tid >> 3;
      const unsigned vo = (unsigned)((r < 16 ? base0 : base1 - 16) + r) * 1152u + (unsigned)(tid & 7) * 16u;
      u32x4 v[9];
#pragma unroll
      for (int k = 0; k < 9; ++k) v[k] = __builtin_amdgcn_raw_buffer_load_b128(rS, vo, k * 128, 0);
      LBAR();
      char* dl = smem + r * K0_PITCH + (tid & 7) * 16;
#pragma unroll
      for (int k = 0; k < 9; ++k) *(u32x4*)(dl + k * 128) = v[k];
      LBAR();
      const unsigned a0 = ldsb + (4 * lg + (li >> 2)) * K0_PITCH + (li & 3) * 8;
#pragma unroll
      for (int e = 0; e < 10; ++e) {
        int f = w * 10 + e, ct = f / 5, n = f - ct * 5, d0 = (ct < 7 ? ct * 72 : 496) + n * 16;
        u32x4 o = tr_frag(a0 + d0 * 2, a0 + d0 * 2 + 16 * K0_PITCH);
        st16s<0>(ws + OFF_WC + (size_t)((ct * 90 + ks) * 5 + n) * 1024, vl16, o);
      }
    }
  }
  if (!early) {
    while (RFL(seen) < 32u) {
      __builtin_amdgcn_s_sleep(KA_SLEEP);
      asm volatile("global_load_dword %0, %1, off sc1\n\ts_waitcnt vmcnt(0)" : "=v"(seen) : "v"(cnt) : "memory");
    }
  }
  asm volatile("s_cmp_ge_u32 %2, 32\n\ts_cbranch_scc1 1f\n\tglobal_load_dwordx4 %0, %1, off sc1\n1:\n\ts_waitcnt vmcnt(0)" : "+v"(st) : "v"(sp), "s"(seen_s) : "memory", "scc");
  {
    const double sm = __builtin_bit_cast(double, (u32x2){st[0], st[1]}), sq = __builtin_bit_cast(double, (u32x2){st[2], st[3]});
    const double mean = sm * (1.0 / 2304.0);
    const float var = fmaxf((float)(sq * (1.0 / 2304.0) - mean * mean), 0.f);
    const float mb = rbf((float)mean), rs = 1.f / rbf(sqrtf(rbf(rbf(var) + 1e-5f)));
    f32x2* sl = (f32x2*)(smem + KA_ATT + w * 256);
    if (lane < 32) sl[lane] = (f32x2){mb, rs};
    __builtin_amdgcn_s_waitcnt(0xC07F);
    __builtin_amdgcn_wave_barrier();
    f32x4 ms[4];
#pragma unroll
    for (int j = 0; j < 4; ++j) ms[j] = *(const f32x4*)((const char*)sl + lg * 64 + j * 16);
    const char* zp = ws + OFF_Z2T + ((size_t)(fb * 64 + p * 4 + w) * 9 + (FAST ? 0 : 5)) * 1024;
    auto z2o = [&](int n) { u32x4 o;
#pragma unroll
      for (int j = 0; j < 4; ++j) o[j] = pk_bf16((bf_lo(zs[n][j]) - ms[j][0]) * ms[j][1], (bf_hi(zs[n][j]) - ms[j][2]) * ms[j][3]);
      return o; };
    st16s<0>(zp, vl16, z2o(0)); st16s<1024>(zp, vl16, z2o(1)); st16s<2048>(zp, vl16, z2o(2)); st16s<3072>(zp, vl16, z2o(3));
    if constexpr (FAST) st16s<0>(zp + 4096, vl16, z2o(4));
  }
}
__global__ __launch_bounds__(256, 2) void kA(const char* __restrict__ ws_c, char* __restrict__ ws, const u16* __restrict__ wsp, const u16* __restrict__ wout, const u16* __restrict__ wo) {
  extern __shared__ __attribute__((aligned(16))) char smem[];
  const int b = blockIdx.x, xcd = b & 7, idx = b >> 3;
  const int p = (xcd & 1) * 8 + (idx & 7), q = idx >> 3, cg = (xcd >> 1) * 8 + (q & 3) * 2 + (q >> 2);
  if (idx < 32) ka_body<1>(smem, ws_c, ws, p, cg, b, wsp, wout, wo); else ka_body<0>(smem, ws_c, ws, p, cg, b, wsp, wout, wo);
}

constexpr int KB_STAGE = 36864;
constexpr int KB_LDS = 4 * KB_STAGE;
__global__ __launch_bounds__(512, 2) void kB(const char* __restrict__ ws_c, char* __restrict__ ws, const u16* __restrict__ bsp) {
  extern __shared__ __attribute__((aligned(16))) char smem[];
  const int tid = threadIdx.x, lane = tid & 63;
  const int w = RFL(tid >> 6), wm = w & 3, kh = w >> 2;
  const int b = blockIdx.x, xcd = b & 7, idx = b >> 3;
  const int tp = (xcd & 1) * 8 + (idx & 7), fb = (xcd >> 1) * 4 + (idx >> 3);
  const rsrc_t rZ = mk_rsrc(ws_c + OFF_Z2T + (size_t)fb * (64 * 9216), 64 * 9216);
  const rsrc_t rW = mk_rsrc(ws_c + OFF_WT + (size_t)tp * (64 * 8192), 64 * 8192);
  const unsigned ldsb = (unsigned)(size_t)(__attribute__((address_space(3))) char*)smem;
  const unsigned vl16 = lane * 16;
  const unsigned vW = vl16 + wm * 2048 + kh * 16384;
  const unsigned dmaF = w * 4096, dmaH = 32768 + w * 512;
  unsigned rd01 = ldsb + kh * 18432 + lane * 16;
  unsigned rd23 = rd01 + 2 * KB_STAGE;

  f32x4 acc[2][9];
#pragma unroll
  for (int m = 0; m < 2; ++m)
#pragma unroll
    for (int n = 0; n < 9; ++n) acc[m][n] = (f32x4){0.f, 0.f, 0.f, 0.f};
  bf16x8 Wr[4][4];
  bf16x8 ZA[9], ZB[9];

#define KB_DMA1(stage, slot, imm) do { \
    unsigned so_ = RFL((unsigned)(stage) * KB_STAGE + dmaF), lo_ = RFL(ldsb + (slot) * KB_STAGE + dmaF); DMA16(lo_, vl16, rZ, so_, imm); } while (0)
#define KB_DMAH(stage, slot) do { \
    unsigned so2_ = RFL((unsigned)(stage) * KB_STAGE + dmaH), lo2_ = RFL(ldsb + (slot) * KB_STAGE + dmaH); DMA16H(lo2_, vl16, rZ, so2_); } while (0)
#define KB_W1(stage, slot, e) do { unsigned sw_ = RFL((unsigned)(stage) * 32768u + ((e) >> 1) * 8192u); \
    if ((e) & 1) BLOAD16(Wr[slot][e], vW, rW, sw_, 1024); else BLOAD16(Wr[slot][e], vW, rW, sw_, 0); } while (0)
#define KB_ALL(stage, slot) do { KB_DMA1(stage, slot, 0); KB_DMA1(stage, slot, 1024); KB_DMA1(stage, slot, 2048); KB_DMA1(stage, slot, 3072); KB_DMAH(stage, slot); \
    KB_W1(stage, slot, 0); KB_W1(stage, slot, 1); KB_W1(stage, slot, 2); KB_W1(stage, slot, 3); } while (0)
#define KB_WAIT(slot, N) asm volatile("s_waitcnt vmcnt(" #N ") lgkmcnt(0)" : "+v"(Wr[slot][0]), "+v"(Wr[slot][1]), "+v"(Wr[slot][2]), "+v"(Wr[slot][3]) :: "memory")

  KB_ALL(0, 0);
  __builtin_amdgcn_s_barrier();
  KB_ALL(1, 1);
  KB_WAIT(0, 9);
  __builtin_amdgcn_s_barrier();

  const rsrc_t rZ1 = mk_rsrc(ws_c + OFF_Z1T + ((size_t)(tp * 8 + wm * 2) * 16 + fb) * 4608, 17 * 4608);
  const rsrc_t rB = mk_rsrc(bsp, 4096);
  u32x4 z1q[2][2]; u32x2 z1s[2]; unsigned biasu[2];
  const unsigned vz1 = lane * 16 + kh * 2048, vz8 = lane * 8 + 4096;
  const unsigned vb = (tp * 128 + wm * 32 + ((lane & 15) >> 2) * 8 + (lane & 3)) * 2;
#define KB_EPI_LD(e) do { \
    if ((e) == 0) BLOAD16(z1q[0][0], vz1, rZ1, 0, 0); \
    if ((e) == 1) BLOAD16(z1q[0][1], vz1, rZ1, 0, 1024); \
    if ((e) == 2) asm volatile("buffer_load_dwordx4 %0, %1, %2, %3 offen" : "=v"(z1q[1][0]) : "v"(vz1), "s"(rZ1), "s"(16 * 4608)); \
    if ((e) == 3) asm volatile("buffer_load_dwordx4 %0, %1, %2, %3 offen offset:1024" : "=v"(z1q[1][1]) : "v"(vz1), "s"(rZ1), "s"(16 * 4608)); \
    if ((e) == 4) asm volatile("buffer_load_dwordx2 %0, %1, %2, 0 offen" : "=v"(z1s[0]) : "v"(vz8), "s"(rZ1)); \
    if ((e) == 5) asm volatile("buffer_load_dwordx2 %0, %1, %2, %3 offen" : "=v"(z1s[1]) : "v"(vz8), "s"(rZ1), "s"(16 * 4608)); \
    if ((e) == 6) asm volatile("buffer_load_ushort %0, %1, %2, 0 offen" : "=v"(biasu[0]) : "v"(vb), "s"(rB)); \
    if ((e) == 7) asm volatile("buffer_load_ushort %0, %1, %2, 0 offen offset:8" : "=v"(biasu[1]) : "v"(vb), "s"(rB)); } while (0)

#define KB_ITER(i, j, MODE, WN) do { \
    _Pragma("unroll") for (int n = 0; n < 9; ++n) { \
      ADSRD(ZA[n], ((j) < 2 ? rd01 : rd23), ((j) & 1) * KB_STAGE + n * 1024); \
      if ((MODE) != 1) { AMFMA(acc[0][n], ZB[n], Wr[((j) + 3) & 3][2]); AMFMA(acc[1][n], ZB[n], Wr[((j) + 3) & 3][3]); } \
      if ((MODE) == 0) { \
        if (n == 0) KB_DMA1((i) + 3, ((j) + 3) & 3, 0); \
        if (n == 2) KB_DMA1((i) + 3, ((j) + 3) & 3, 1024); \
        if (n == 4) KB_W1((i) + 3, ((j) + 3) & 3, 0); \
        if (n == 6) KB_DMA1((i) + 3, ((j) + 3) & 3, 2048); \
        if (n == 8) KB_W1((i) + 3, ((j) + 3) & 3, 1); } \
      if ((MODE) == 2 && !(n & 1)) KB_EPI_LD(n >> 1); } \
    if ((MODE) == 1) { KB_ALL(2, 2); KB_DMA1(3, 3, 0); KB_DMA1(3, 3, 1024); KB_W1(3, 3, 0); KB_DMA1(3, 3, 2048); KB_W1(3, 3, 1); } \
    _Pragma("unroll") for (int n = 0; n < 9; ++n) { \
      if (n == 0) asm volatile("s_waitcnt lgkmcnt(6)" ::: "memory"); \
      if (n == 2) asm volatile("s_waitcnt lgkmcnt(5)" ::: "memory"); \
      if (n == 5) asm volatile("s_waitcnt lgkmcnt(5)" ::: "memory"); \
      ADSRD(ZB[n], ((j) < 2 ? rd01 : rd23), ((j) & 1) * KB_STAGE + 9216 + n * 1024); \
      AMFMA(acc[0][n], ZA[n], Wr[j][0]); AMFMA(acc[1][n], ZA[n], Wr[j][1]); \
      if ((MODE) < 2) { \
        if (n == 0) KB_DMA1((i) + 3, ((j) + 3) & 3, 3072); \
        if (n == 2) KB_W1((i) + 3, ((j) + 3) & 3, 2); \
        if (n == 4) KB_DMAH((i) + 3, ((j) + 3) & 3); \
        if (n == 6) KB_W1((i) + 3, ((j) + 3) & 3, 3); } \
      if ((MODE) == 2 && (n == 0 || n == 2 || n == 4)) KB_EPI_LD(5 + (n >> 1)); } \
    KB_WAIT(((j) + 1) & 3, WN); \
    __builtin_amdgcn_s_barrier(); \
    } while (0)

  KB_ITER(0, 0, 1, 18);
  KB_ITER(1, 1, 0, 18);
  KB_ITER(2, 2, 0, 18);
  KB_ITER(3, 3, 0, 18);
  for (int io = 4; io < 12; io += 4) {
    KB_ITER(io + 0, 0, 0, 18);
    KB_ITER(io + 1, 1, 0, 18);
    KB_ITER(io + 2, 2, 0, 18);
    KB_ITER(io + 3, 3, 0, 18);
  }
  KB_ITER(12, 0, 0, 18);
  KB_ITER(13, 1, 2, 17);
  KB_ITER(14, 2, 3, 8);
  {
#pragma unroll
    for (int n = 0; n < 9; ++n) { ADSRD(ZA[n], rd23, KB_STAGE + n * 1024); AMFMA(acc[0][n], ZB[n], Wr[2][2]); AMFMA(acc[1][n], ZB[n], Wr[2][3]); }
#pragma unroll
    for (int n = 0; n < 9; ++n) {
      if (n == 0) asm volatile("s_waitcnt lgkmcnt(6)" ::: "memory");
      if (n == 2) asm volatile("s_waitcnt lgkmcnt(5)" ::: "memory");
      if (n == 5) asm volatile("s_waitcnt lgkmcnt(5)" ::: "memory");
      ADSRD(ZB[n], rd23, KB_STAGE + 9216 + n * 1024);
      AMFMA(acc[0][n], ZA[n], Wr[3][0]); AMFMA(acc[1][n], ZA[n], Wr[3][1]); }
    asm volatile("s_waitcnt lgkmcnt(0)" ::: "memory");
#pragma unroll
    for (int n = 0; n < 9; ++n) { AMFMA(acc[0][n], ZB[n], Wr[3][2]); AMFMA(acc[1][n], ZB[n], Wr[3][3]); }
  }
  asm volatile("s_nop 15" : "+v"(acc[0][0]), "+v"(acc[0][1]), "+v"(acc[0][2]), "+v"(acc[0][3]), "+v"(acc[0][4]), "+v"(acc[0][5]), "+v"(acc[0][6]), "+v"(acc[0][7]), "+v"(acc[0][8]),
               "+v"(acc[1][0]), "+v"(acc[1][1]), "+v"(acc[1][2]), "+v"(acc[1][3]), "+v"(acc[1][4]), "+v"(acc[1][5]), "+v"(acc[1][6]), "+v"(acc[1][7]), "+v"(acc[1][8]) :: "memory");
  asm volatile("s_waitcnt vmcnt(0)" : "+v"(z1q[0][0]), "+v"(z1q[0][1]), "+v"(z1q[1][0]), "+v"(z1q[1][1]), "+v"(z1s[0]), "+v"(z1s[1]), "+v"(biasu[0]), "+v"(biasu[1]) :: "memory");
  SCHED();
  char* hp = ws + OFF_HT + ((size_t)(tp * 8 + wm * 2) * 16 + fb) * 4608;
  float bias[2] = {bf_lo(biasu[0]), bf_lo(biasu[1])};
  char* ex = smem + wm * 18432 + lane * 16;
  if (kh == 0) {
#pragma unroll
    for (int m = 0; m < 2; ++m)
#pragma unroll
      for (int n = 4; n < 8; ++n) *(f32x4*)(ex + (m * 9 + n) * 1024) = acc[m][n];
  } else {
#pragma unroll
    for (int m = 0; m < 2; ++m) {
#pragma unroll
      for (int n = 0; n < 4; ++n) *(f32x4*)(ex + (m * 9 + n) * 1024) = acc[m][n];
      *(f32x4*)(ex + (m * 9 + 8) * 1024) = acc[m][8];
    }
  }
  __syncthreads();
#pragma unroll
  for (int m = 0; m < 2; ++m) {
#pragma unroll
    for (int q = 0; q < 2; ++q) {
      f32x4 g0, g1;
      if (kh == 0) { g0 = acc[m][2 * q] + *(const f32x4*)(ex + (m * 9 + 2 * q) * 1024); g1 = acc[m][2 * q + 1] + *(const f32x4*)(ex + (m * 9 + 2 * q + 1) * 1024); }
      else { g0 = acc[m][4 + 2 * q] + *(const f32x4*)(ex + (m * 9 + 4 + 2 * q) * 1024); g1 = acc[m][5 + 2 * q] + *(const f32x4*)(ex + (m * 9 + 5 + 2 * q) * 1024); }
      u32x4 z = z1q[m][q], o;
      o[0] = pk_bf16(bf_lo(z[0]) * (g0[0] + bias[m]), bf_hi(z[0]) * (g0[1] + bias[m]));
      o[1] = pk_bf16(bf_lo(z[1]) * (g0[2] + bias[m]), bf_hi(z[1]) * (g0[3] + bias[m]));
      o[2] = pk_bf16(bf_lo(z[2]) * (g1[0] + bias[m]), bf_hi(z[2]) * (g1[1] + bias[m]));
      o[3] = pk_bf16(bf_lo(z[3]) * (g1[2] + bias[m]), bf_hi(z[3]) * (g1[3] + bias[m]));
      st16(hp + m * (16 * 4608) + (kh * 2 + q) * 1024 + lane * 16, o);
    }
    if (kh == 0) {
      f32x4 g0 = acc[m][8] + *(const f32x4*)(ex + (m * 9 + 8) * 1024);
      u32x2 z = z1s[m], o;
      o[0] = pk_bf16(bf_lo(z[0]) * (g0[0] + bias[m]), bf_hi(z[0]) * (g0[1] + bias[m]));
      o[1] = pk_bf16(bf_lo(z[1]) * (g0[2] + bias[m]), bf_hi(z[1]) * (g0[3] + bias[m]));
      st8(hp + m * (16 * 4608) + 4096 + lane * 8, o);
    }
  }
}

constexpr int KC_PITCH = 336;
constexpr int KC_PART = 64 * KC_PITCH;
constexpr int KC_LDS = 5 * KC_PART;
template <bool ISO>
__device__ __forceinline__ void kc_main(f32x4 (&acc)[4][5], rsrc_t rA, rsrc_t rW, unsigned vA16, unsigned vA8, unsigned vW, unsigned sA, unsigned sW) {
  constexpr unsigned TS = ISO ? 18432u : 16u * 4608u;
  constexpr int PD = 3;
  bf16x8 A[PD + 1][4], W[PD + 1][5];
  auto load = [&](int u, int slot) {
#pragma unroll
    for (int m = 0; m < 4; ++m) {
      if (ISO || (u % 9) == 4) {
        unsigned o0, o1;
        if (ISO) { o0 = sA + m * TS + u * 1024; o1 = o0 + 512; }
        else { o0 = sA + m * TS + (u / 9) * 2 * 4608 + 4096; o1 = o0 + 4608; }
        u32x2 lo = __builtin_amdgcn_raw_buffer_load_b64(rA, vA8, o0, 0);
        u32x2 hi = __builtin_amdgcn_raw_buffer_load_b64(rA, vA8, o1, 0);
        u32x4 v = {lo[0], lo[1], hi[0], hi[1]};
        A[slot][m] = __builtin_bit_cast(bf16x8, v);
      } else {
        int i = u % 9;
        unsigned o = sA + m * TS + (u / 9) * 2 * 4608 + (i < 4 ? i * 1024 : 4608 + (i - 5) * 1024);
        A[slot][m] = __builtin_bit_cast(bf16x8, __builtin_amdgcn_raw_buffer_load_b128(rA, vA16, o, 0));
      }
    }
#pragma unroll
    for (int n = 0; n < 5; ++n) W[slot][n] = __builtin_bit_cast(bf16x8, __builtin_amdgcn_raw_buffer_load_b128(rW, vW, sW + (u * 5 + n) * 1024, 0));
  };
#pragma unroll
  for (int u = 0; u < PD; ++u) load(u, u);
  SCHED();
#pragma unroll
  for (int u = 0; u < 18; ++u) {
    if (u + PD < 18) load(u + PD, (u + PD) % (PD + 1));
    SCHED();
#pragma unroll
    for (int m = 0; m < 4; ++m)
#pragma unroll
      for (int n = 0; n < 5; ++n) acc[m][n] = MFMA(W[u % (PD + 1)][n], A[u % (PD + 1)][m], acc[m][n]);
    SCHED();
  }
}
__global__ __launch_bounds__(320, 2) void kC(const char* __restrict__ ws_c, const u16* __restrict__ x, u16* __restrict__ out) {
  extern __shared__ __attribute__((aligned(16))) char smem[];
  const int tid = threadIdx.x, lane = tid & 63;
  const int w = RFL(tid >> 6);
  const int b = blockIdx.x, xcd = b & 7, idx = b >> 3;
  const int rt = (xcd & 3) * 8 + (idx & 7), ct = (xcd >> 2) * 4 + (idx >> 3);
  f32x4 acc[4][5];
#pragma unroll
  for (int m = 0; m < 4; ++m)
#pragma unroll
    for (int n = 0; n < 5; ++n) acc[m][n] = (f32x4){0.f, 0.f, 0.f, 0.f};
  const rsrc_t rW = mk_rsrc(ws_c + OFF_WC, (int)SZ_WC);
  const unsigned sW = RFL((unsigned)((ct * 90 + w * 18) * 5 * 1024));
  const int dstart = ct < 7 ? ct * 72 : 496;
  u32x4 xv[2]; unsigned gof[2];
#pragma unroll
  for (int e = 0; e < 2; ++e) {
    int c = tid + e * 320; c = c < 576 ? c : tid;
    const int r = c / 9, ch = c - r * 9;
    gof[e] = (unsigned)(rt * 64 + prow(r >> 4, r & 15)) * 1152u + (unsigned)(ct * 72 + ch * 8) * 2u;
    asm volatile("global_load_dwordx4 %0, %1, %2" : "=v"(xv[e]) : "v"(gof[e]), "s"(x));
  }
  if (w < 4) {
    const rsrc_t rA = mk_rsrc(ws_c + OFF_HT, (int)SZ_Z);
    kc_main<false>(acc, rA, rW, lane * 16, lane * 8, lane * 16, RFL((unsigned)((rt * 4 * 16 + w * 4) * 4608)), sW);
  } else {
    const rsrc_t rA = mk_rsrc(ws_c + OFF_OT, (int)SZ_OT);
    kc_main<true>(acc, rA, rW, lane * 16, lane * 8, lane * 16, RFL((unsigned)(rt * 4 * 18432)), sW);
  }
  {
    char* p = smem + w * KC_PART + (lane & 15) * KC_PITCH + (lane >> 4) * 16;
#pragma unroll
    for (int m = 0; m < 4; ++m)
#pragma unroll
      for (int n = 0; n < 5; ++n) *(f32x4*)(p + m * 16 * KC_PITCH + n * 64) = acc[m][n];
  }
  asm volatile("s_waitcnt vmcnt(0) lgkmcnt(0)\n\ts_barrier" : "+v"(xv[0]), "+v"(xv[1]) :: "memory");
  const int own0 = ct * 72 - dstart;
#pragma unroll
  for (int e = 0; e < 2; ++e) {
    const int c = tid + e * 320;
    if (c < 576) {
      const int r = c / 9, ch = c - r * 9;
      const char* p = smem + r * KC_PITCH + (own0 + ch * 8) * 4;
      f32x4 s0 = *(const f32x4*)p, s1 = *(const f32x4*)(p + 16);
#pragma unroll
      for (int ww = 1; ww < 5; ++ww) { s0 += *(const f32x4*)(p + ww * KC_PART); s1 += *(const f32x4*)(p + ww * KC_PART + 16); }
      u32x4 o;
      o[0] = pk_bf16(s0[0] + bf_lo(xv[e][0]), s0[1] + bf_hi(xv[e][0]));
      o[1] = pk_bf16(s0[2] + bf_lo(xv[e][1]), s0[3] + bf_hi(xv[e][1]));
      o[2] = pk_bf16(s1[0] + bf_lo(xv[e][2]), s1[1] + bf_hi(xv[e][2]));
      o[3] = pk_bf16(s1[2] + bf_lo(xv[e][3]), s1[3] + bf_hi(xv[e][3]));
      st16((char*)out + gof[e], o);
    }
  }
}

constexpr int K0_BLOCKS = 280;
#define K0LD(p) __builtin_nontemporal_load(p)
#define XLD(p) (*(p))
constexpr int PF0_BYTES = 12288;
#define PF_CODE_N 16
#define PF_TLB_N 16
#define PF_TLB_STRIDE 65536
__global__ __launch_bounds__(256) void k0(const u16* __restrict__ x, const u16* __restrict__ wqkv, const u16* __restrict__ win, char* __restrict__ ws, const u16* __restrict__ wsp, const u16* __restrict__ wout, const u16* __restrict__ wo, const u16* __restrict__ bsp) {
  __shared__ __attribute__((aligned(16))) char sm[32 * K0_PITCH];
  const int tid = threadIdx.x, lane = tid & 63, i = lane & 15, g = lane >> 4, w = tid >> 6, b = blockIdx.x;
  const unsigned smb = (unsigned)(size_t)(__attribute__((address_space(3))) char*)sm;
  unsigned pf0 = 0, pf1 = 0, pf2 = 0;
  if (w == 3 && (b >> 3) < PF_CODE_N) {
    const int u = b >> 3;
    unsigned long long pa, p0;
    asm volatile("s_getpc_b64 s[20:21]\n\ts_add_u32 s20, s20, _Z2kAPKcPcPKtS3_S3_@rel32@lo+4\n\ts_addc_u32 s21, s21, _Z2kAPKcPcPKtS3_S3_@rel32@hi+12\n\ts_mov_b64 %0, s[20:21]" : "=s"(pa) :: "scc", "s20", "s21");
    asm volatile("s_getpc_b64 s[20:21]\n\ts_add_u32 s20, s20, _Z2k0PKtS0_S0_PcS0_S0_S0_S0_@rel32@lo+4\n\ts_addc_u32 s21, s21, _Z2k0PKtS0_S0_PcS0_S0_S0_S0_@rel32@hi+12\n\ts_mov_b64 %0, s[20:21]" : "=s"(p0) :: "scc", "s20", "s21");
    const long long span = (long long)(p0 - pa);
    int off = (u < 2 ? u : u - 2) * 8192 + lane * 128;
    const int lim = u < 2 ? PF0_BYTES : (span > 0 && span < (1 << 20) ? (int)span : 0);
    off = off < lim ? off : 0;
    const char* a = (const char*)(u < 2 ? p0 : pa) + off;
    asm volatile("global_load_dword %0, %1, off" : "=v"(pf0) : "v"(a));
  }
  if (b == 217 && w == 1) { const char* a = (const char*)bsp + (lane & 31) * 128; asm volatile("global_load_dword %0, %1, off" : "=v"(pf1) : "v"(a)); }
  if (b < 144) {
    const int ks = b >> 3, cb = b & 7;
    u32x4 v[9];
#pragma unroll
    for (int k = 0; k < 9; ++k) { int c = tid + k * 256, r = c / 72, ch = c - r * 72; v[k] = K0LD((const u32x4*)(win + (size_t)(ks * 32 + r) * 4608 + cb * 576 + ch * 8)); }
#pragma unroll
    for (int k = 0; k < 9; ++k) { int c = tid + k * 256, r = c / 72, ch = c - r * 72; *(u32x4*)(sm + r * K0_PITCH + ch * 16) = v[k]; }
    __syncthreads();
    const unsigned a0 = smb + (8 * g + (i >> 2)) * K0_PITCH + (i & 3) * 8;
#pragma unroll
    for (int e = 0; e < 9; ++e) {
      int nt = w * 9 + e;
      int gt = cb * 36 + nt;
      int z2 = gt >= 144, t = gt - z2 * 144, fb = t / 9, n = t - fb * 9;
      int fast = z2 ? n < 5 : n >= 4, nz = 4 + fast;
      int fr = fb * 468 + (fast ? 0 : 252) + 72 + (z2 ? ks * nz + n - (fast ? 0 : 5) : 18 * nz + ks * nz + n - (fast ? 4 : 0));
      u32x4 o = tr_frag(a0 + nt * 32, a0 + nt * 32 + 4 * K0_PITCH);
      st16(ws + OFF_W1 + (size_t)fr * 1024 + lane * 16, o);
    }
  } else if (b < 216) {
    const int u = b - 144, ks = u >> 2, hq = u & 3;
    u32x4 v[7]; int dst[7], dw0[7];
#pragma unroll
    for (int k = 0; k < 7; ++k) {
      int c = tid + k * 256; c = c < 32 * 54 ? c : 32 * 54 - 1;
      const int r = c / 54, e = c - r * 54, part = e / 18, ch = e - part * 18;
      v[k] = K0LD((const u32x4*)(wqkv + (size_t)(ks * 32 + r) * 1728 + part * 576 + hq * 144 + ch * 8));
      dst[k] = r * K0_PITCH + (part == 0 ? 80 : (part == 1 ? 0 : 40)); dw0[k] = ch * 4;
    }
#pragma unroll
    for (int z = 0; z < 5; ++z) {
      const int c = tid + z * 256, r = c / 40, e = c - r * 40, hl = e / 5, jj = e - hl * 5;
      *(unsigned*)(sm + r * K0_PITCH + hl * 128 + (jj == 0 ? 9 : (jj == 1 ? 19 : 27 + jj)) * 4) = 0u;
    }
#pragma unroll
    for (int k = 0; k < 7; ++k)
#pragma unroll
      for (int q = 0; q < 4; ++q) { const int dw = dw0[k] + q, hl = (dw * 57) >> 9; *(unsigned*)(sm + dst[k] + hl * 128 + (dw - hl * 9) * 4) = v[k][q]; }
    __syncthreads();
    const unsigned a0 = smb + (8 * g + (i >> 2)) * K0_PITCH + (i & 3) * 8;
#pragma unroll
    for (int e = 0; e < 8; ++e) {
      const int f = w * 8 + e, hl = f >> 2, n = f & 3;
      u32x4 o = tr_frag(a0 + f * 32, a0 + f * 32 + 4 * K0_PITCH);
      st16(ws + OFF_W1 + (size_t)(((hq * 8 + hl) >> 1) * 468 + (hl & 1) * 252 + ks * 4 + n) * 1024 + lane * 16, o);
    }
  } else {
    static_assert((2048 * 16 + 16 * 256) / 16 == 64 * 36, "stat words");
    if (tid < 36) st16(ws + OFF_RS + ((b - 216) * 36 + tid) * 16, (u32x4){0, 0, 0, 0});
    const int u = (b - 216) * 4 + w, tt = u >> 1, slot = (u & 1) * 8 + (lane >> 3), c0 = lane & 7;
    const u16* xr = x + (size_t)prow(tt, slot) * 576 + c0 * 8;
    u32x4 v[9];
#pragma unroll
    for (int k = 0; k < 9; ++k) v[k] = XLD((const u32x4*)(xr + k * 64));
    float s = 0.f;
#pragma unroll
    for (int k = 0; k < 9; ++k)
#pragma unroll
      for (int j = 0; j < 4; ++j) s += bf_lo(v[k][j]) + bf_hi(v[k][j]);
    s += __shfl_xor(s, 1); s += __shfl_xor(s, 2); s += __shfl_xor(s, 4);
    const float mean = s * (1.f / 576.f);
    float q = 0.f;
#pragma unroll
    for (int k = 0; k < 9; ++k)
#pragma unroll
      for (int j = 0; j < 4; ++j) { float a = bf_lo(v[k][j]) - mean, c = bf_hi(v[k][j]) - mean; q += a * a + c * c; }
    q += __shfl_xor(q, 1); q += __shfl_xor(q, 2); q += __shfl_xor(q, 4);
    const float mb = rbf(mean), sd = rbf(sqrtf(rbf(rbf(q * (1.f / 576.f)) + 1e-5f))), rs = 1.f / sd;
    char* dst = ws + OFF_XT + (size_t)tt * 18432 + slot * 16;
#pragma unroll
    for (int k = 0; k < 9; ++k) {
      u32x4 o;
#pragma unroll
      for (int j = 0; j < 4; ++j) o[j] = pk_bf16(rbf(bf_lo(v[k][j]) - mb) * rs, rbf(bf_hi(v[k][j]) - mb) * rs);
      int c = c0 + 8 * k;
      st16(dst + (c >> 2) * 1024 + (c & 3) * 256, o);
    }
  }
  asm volatile("s_waitcnt vmcnt(0)" : "+v"(pf0), "+v"(pf1), "+v"(pf2) :: "memory");
}

extern "C" void kernel_launch(void* const* d_in, const int* in_sizes, int n_in, void* d_out, int out_size, void* d_ws, size_t ws_size, hipStream_t stream) {
  (void)in_sizes; (void)n_in; (void)out_size; (void)ws_size;
  const u16* x = (const u16*)d_in[0];
  char* ws = (char*)d_ws;
  (void)hipFuncSetAttribute((const void*)kA, hipFuncAttributeMaxDynamicSharedMemorySize, KA_LDS);
  (void)hipFuncSetAttribute((const void*)kB, hipFuncAttributeMaxDynamicSharedMemorySize, KB_LDS);
  (void)hipFuncSetAttribute((const void*)kC, hipFuncAttributeMaxDynamicSharedMemorySize, KC_LDS);
  k0<<<K0_BLOCKS, 256, 0, stream>>>(x, (const u16*)d_in[1], (const u16*)d_in[4], ws, (const u16*)d_in[5], (const u16*)d_in[7], (const u16*)d_in[2], (const u16*)d_in[6]);
  kA<<<512, 256, KA_LDS, stream>>>(ws, ws, (const u16*)d_in[5], (const u16*)d_in[7], (const u16*)d_in[2]);
  kB<<<256, 512, KB_LDS, stream>>>(ws, ws, (const u16*)d_in[6]);
  kC<<<256, 320, KC_LDS, stream>>>(ws, x, (u16*)d_out);
}
```

```cpp
#include <hip/hip_runtime.h>
#include <hip/hip_bf16.h>
#include <stdint.h>

typedef unsigned short u16;
typedef __attribute__((ext_vector_type(8))) short bf16x8;
typedef __attribute__((ext_vector_type(4))) short bf16x4;
typedef __attribute__((ext_vector_type(4))) float f32x4;
typedef __attribute__((ext_vector_type(4))) unsigned u32x4;
typedef __attribute__((ext_vector_type(2))) unsigned u32x2;
typedef __amdgpu_buffer_rsrc_t rsrc_t;

constexpr int S_ = 2048, D_ = 576, F_ = 4608, FH_ = 2304;
constexpr size_t OFF_Z2T = 0;
constexpr size_t SZ_Z = (size_t)16 * 64 * 9 * 1024;
constexpr size_t OFF_Z1T = OFF_Z2T + SZ_Z;
constexpr size_t OFF_HT = OFF_Z1T + SZ_Z;
constexpr size_t OFF_WT = OFF_HT + SZ_Z;
constexpr size_t SZ_WT = (size_t)16 * 64 * 8 * 1024;
constexpr size_t OFF_OT = OFF_WT + SZ_WT;
constexpr size_t SZ_OT = (size_t)128 * 144 * 128;
constexpr size_t OFF_WC = OFF_OT + SZ_OT;
constexpr size_t SZ_WC = (size_t)8 * 90 * 5 * 1024;
constexpr size_t OFF_XT = OFF_WC + SZ_WC;
constexpr size_t SZ_XT = (size_t)128 * 18 * 1024;
constexpr size_t OFF_W1 = OFF_XT + SZ_XT;
constexpr size_t SZ_W1 = (size_t)32 * 234 * 1024;
constexpr size_t OFF_RS = OFF_W1 + SZ_W1;
constexpr size_t OFF_CNT = OFF_RS + 2048 * 16;
constexpr size_t OFF_ST = OFF_CNT + 16 * 256;
constexpr size_t WS_END = OFF_ST + (1 << 20);

#define RFL(x) __builtin_amdgcn_readfirstlane(x)
#define SCHED() __builtin_amdgcn_sched_barrier(0)
__device__ __forceinline__ rsrc_t mk_rsrc(const void* p, int bytes) { return __builtin_amdgcn_make_buffer_rsrc((void*)p, (short)0, bytes, 0x00020000); }
__device__ __forceinline__ float bf_lo(unsigned w) { return __uint_as_float(w << 16); }
__device__ __forceinline__ float bf_hi(unsigned w) { return __uint_as_float(w & 0xffff0000u); }
__device__ __forceinline__ unsigned pk_bf16(float lo, float hi) {
  typedef __attribute__((ext_vector_type(2))) float f32x2; typedef __attribute__((ext_vector_type(2))) __bf16 bf2;
  f32x2 v = {lo, hi}; bf2 r = __builtin_convertvector(v, bf2); return __builtin_bit_cast(unsigned, r);
}
__device__ __forceinline__ float rbf(float f) { return bf_hi(pk_bf16(0.f, f)); }
__host__ __device__ __forceinline__ int prow(int tt, int i) { return 32 * (tt >> 1) + 8 * (i >> 2) + 4 * (tt & 1) + (i & 3); }
__device__ __forceinline__ void st16(void* p, u32x4 v) { asm volatile("global_store_dwordx4 %0, %1, off sc1\n\ts_nop 1" :: "v"(p), "v"(v) : "memory"); }
template <int IMM> __device__ __forceinline__ void st16s(const char* base, unsigned voff, u32x4 v) {
  asm volatile("global_store_dwordx4 %0, %1, %2 offset:%3 sc1\n\ts_nop 1" :: "v"(voff), "v"(v), "s"(base), "i"(IMM) : "memory"); }
__device__ __forceinline__ void st8(void* p, u32x2 v) { asm volatile("global_store_dwordx2 %0, %1, off sc1\n\ts_nop 1" :: "v"(p), "v"(v) : "memory"); }
__device__ __forceinline__ void st4(void* p, unsigned v) { asm volatile("global_store_dword %0, %1, off sc1\n\ts_nop 1" :: "v"(p), "v"(v) : "memory"); }
#define MFMA(a, b, c) __builtin_amdgcn_mfma_f32_16x16x32_bf16(a, b, c, 0, 0, 0)

#define DMA16(lds, voff, srd, soff, imm) asm volatile("s_mov_b32 m0, %0\n\ts_nop 0\n\tbuffer_load_dwordx4 %1, %2, %3 offen offset:" #imm " lds" :: "s"(lds), "v"(voff), "s"(srd), "s"(soff) : "memory")
#define DMA16H(lds, voff, srd, soff) asm volatile("s_mov_b32 m0, %0\n\ts_lshr_b64 exec, -1, 32\n\tbuffer_load_dwordx4 %1, %2, %3 offen lds\n\ts_mov_b64 exec, -1" :: "s"(lds), "v"(voff), "s"(srd), "s"(soff) : "memory")
typedef __attribute__((address_space(3))) char lds_char;
#define LDS_RD128(addr) (*(const __attribute__((address_space(3))) bf16x8*)(addr))
#define AMFMA(acc, a, b) asm volatile("v_mfma_f32_16x16x32_bf16 %0, %1, %2, %0" : "+v"(acc) : "v"(a), "v"(b))
#define ADSRD(dst, addr, off) asm volatile("ds_read_b128 %0, %1 offset:%2" : "=v"(dst) : "v"(addr), "i"(off))
#define OPAQUE(x) asm volatile("" : "+v"(x))
#define BLOAD16(dst, voff, srd, soff, imm) asm volatile("buffer_load_dwordx4 %0, %1, %2, %3 offen offset:" #imm : "=v"(dst) : "v"(voff), "s"(srd), "s"(soff))

constexpr int K0_PITCH = 1168;
__device__ __forceinline__ int wc_krow(int ks, int g, int j) {
  int r = j & 3, hi = j >> 2;
  if (ks >= 72) { int u = ks - 72; return 2304 + 4 * (8 * u + 4 * hi + g) + r; }
  int w = ks / 18, pi = (ks % 18) / 9, i = ks % 9, fb0 = 4 * w + 2 * pi;
  if (i == 4) return 144 * (fb0 + hi) + 128 + 4 * g + r;
  if (i < 4) return 144 * fb0 + 32 * i + 16 * hi + 4 * g + r;
  return 144 * (fb0 + 1) + 32 * (i - 5) + 16 * hi + 4 * g + r;
}
__device__ __forceinline__ u32x4 tr_frag(unsigned a0, unsigned a1) {
  u32x2 lo, hi;
  asm volatile("ds_read_b64_tr_b16 %0, %2\n\tds_read_b64_tr_b16 %1, %3\n\ts_waitcnt lgkmcnt(0)" : "=&v"(lo), "=&v"(hi) : "v"(a0), "v"(a1) : "memory");
  return (u32x4){lo[0], lo[1], hi[0], hi[1]};
}
#define KA_SLEEP 32
#define PFA_POL ""
#define SJLD(p) (*(p))
constexpr int KA_SLOT = 10240, KA_ATT = 6 * KA_SLOT, KA_PITCH = 144, KA_LDS = KA_ATT + 142 * KA_PITCH;
template <int FAST> struct KaCfg {
  static constexpr int NZ2 = 4 + FAST, NZ1 = 4 + FAST;
  static constexpr int nt(int s) { return s < 9 ? 4 : 4 + FAST; }
  static constexpr int goff(int s) { return s < 9 ? s * 8192 : 73728 + (s - 9) * (4 + FAST) * 2048; }
  static constexpr int ops(int s) { return s > 26 ? 0 : 2 + (nt(s) == 5 ? 1 : 0) + (s < 9 ? 4 + ((s & 1) ? 0 : 1) : 0); }
  static constexpr int wait(int s) { return s == 20 ? ops(23) + ops(24) : ops(s + 2) + ops(s + 3) + ops(s + 4); }
};
template <int V> struct IC { static constexpr int v = V; };
#define BLD(dst, voff, srd, soff, IMM) asm volatile("buffer_load_dwordx4 %0, %1, %2, %3 offen offset:%4" : "=v"(dst) : "v"(voff), "s"(srd), "s"(soff), "i"(IMM))
#define AMFMA0(acc, a, b) asm volatile("v_mfma_f32_16x16x32_bf16 %0, %1, %2, 0" : "=&v"(acc) : "v"(a), "v"(b))
typedef __attribute__((ext_vector_type(2))) float f32x2;
typedef __attribute__((ext_vector_type(2))) __bf16 bf2_t;
__device__ __forceinline__ float dot2(unsigned a, unsigned b, float c) { return __builtin_amdgcn_fdot2_f32_bf16(__builtin_bit_cast(bf2_t, a), __builtin_bit_cast(bf2_t, b), c, false); }
__device__ __forceinline__ f32x2 gelu2(f32x2 x) {
  f32x2 t = x * x, p = t * (-0.10294324f) + (-2.3022082f), e = x * p;
  f32x2 d = {__builtin_amdgcn_exp2f(e[0]), __builtin_amdgcn_exp2f(e[1])};
  d += 1.f;
  f32x2 r = {__builtin_amdgcn_rcpf(d[0]), __builtin_amdgcn_rcpf(d[1])};
  return x * r;
}
template <int CTRL> __device__ __forceinline__ float dpp_add(float v) {
  return v + __builtin_bit_cast(float, __builtin_amdgcn_update_dpp(0, __builtin_bit_cast(int, v), CTRL, 0xf, 0xf, true));
}

__device__ __forceinline__ float xmax16(float v) { auto r = __builtin_amdgcn_permlane16_swap(__float_as_uint(v), __float_as_uint(v), false, false); return fmaxf(__uint_as_float(r[0]), __uint_as_float(r[1])); }
__device__ __forceinline__ float xmax32(float v) { auto r = __builtin_amdgcn_permlane32_swap(__float_as_uint(v), __float_as_uint(v), false, false); return fmaxf(__uint_as_float(r[0]), __uint_as_float(r[1])); }
__device__ __forceinline__ float xadd16(float v) { auto r = __builtin_amdgcn_permlane16_swap(__float_as_uint(v), __float_as_uint(v), false, false); return __uint_as_float(r[0]) + __uint_as_float(r[1]); }
__device__ __forceinline__ float xadd32(float v) { auto r = __builtin_amdgcn_permlane32_swap(__float_as_uint(v), __float_as_uint(v), false, false); return __uint_as_float(r[0]) + __uint_as_float(r[1]); }
#define PRIO_HI()
#define PRIO_LO()
#define LBAR() asm volatile("s_waitcnt lgkmcnt(0)\n\ts_barrier" ::: "memory")
__device__ __forceinline__ float swap1(float v) { return __builtin_bit_cast(float, __builtin_amdgcn_update_dpp(0, __builtin_bit_cast(int, v), 0xB1, 0xf, 0xf, true)); }
template <int FAST>
__device__ __forceinline__ void ka_body(char* smem, const char* __restrict__ ws_c, char* __restrict__ ws, const int p, const int cg, const int sj, const u16* __restrict__ wsp, const u16* __restrict__ wout, const u16* __restrict__ wo) {
  typedef KaCfg<FAST> C;
  const int tid = threadIdx.x, lane = tid & 63, li = lane & 15, lg = lane >> 4;
  const int w = RFL(tid >> 6), fb = cg >> 1;
  const rsrc_t rW = mk_rsrc(ws_c + OFF_W1 + (size_t)fb * (468 * 1024) + (FAST ? 0 : 252 * 1024), (FAST ? 252 : 216) * 1024);
  const rsrc_t rX = mk_rsrc(ws_c + OFF_XT, (int)SZ_XT);
  const unsigned ldsb = (unsigned)(size_t)(__attribute__((address_space(3))) char*)smem;
  const unsigned vl16 = lane * 16;
  const unsigned vX = vl16 + (unsigned)(p * 8 + w * 2) * 18432u;
  const unsigned vH = p == 0 ? 0x80000000u : (unsigned)(p * 8 - 2 + (li >> 3)) * 18432u + (unsigned)(lg * 16 + (li | 8)) * 16u + (unsigned)w * 1024u;
  const unsigned sF = RFL((unsigned)w * 2048u), sH = RFL(8192u + (unsigned)w * 512u);
  const unsigned rd = ldsb + vl16;
  constexpr int KA_WARM = 0;
  bf16x8 X[2][18], HX[5], F[5];
  f32x4 acc[2][5], hacc[3];
#pragma unroll
  for (int n = 0; n < 3; ++n) hacc[n] = (f32x4){0.f, 0.f, 0.f, 0.f};

  bf16x8 (&X_)[2][18] = X; bf16x8 (&HX_)[5] = HX; bf16x8 (&F_)[5] = F; f32x4 (&acc_)[2][5] = acc; f32x4 (&hacc_)[3] = hacc;
  const rsrc_t rW_ = rW, rX_ = rX; const unsigned vl16_ = vl16, vX_ = vX, vH_ = vH, rd_ = rd;
#define KA_CAP bf16x8 (&X)[2][18] = X_; bf16x8 (&HX)[5] = HX_; bf16x8 (&F)[5] = F_; f32x4 (&acc)[2][5] = acc_; f32x4 (&hacc)[3] = hacc_; \
    const rsrc_t rW = rW_, rX = rX_; const unsigned vl16 = vl16_, vX = vX_, vH = vH_, rd = rd_; (void)X; (void)HX; (void)F; (void)acc; (void)hacc; (void)rW; (void)rX; (void)vl16; (void)vX; (void)vH; (void)rd;
  auto vop = [&](auto S_, auto K_) {
    KA_CAP
    constexpr int s = decltype(S_)::v, k = decltype(K_)::v;
    if constexpr (s <= 26) {
      constexpr int slot = s % 6, nd = 2 + (C::nt(s) == 5 ? 1 : 0);
      if constexpr (k == 0) { unsigned lo = ldsb + slot * KA_SLOT + sF, so = C::goff(s) + sF; DMA16(lo, vl16, rW, so, 0); }
      else if constexpr (k == 1) { unsigned lo = ldsb + slot * KA_SLOT + sF, so = C::goff(s) + sF; DMA16(lo, vl16, rW, so, 1024); }
      else if constexpr (k == 2 && nd == 3) { unsigned lo = ldsb + slot * KA_SLOT + sH, so = C::goff(s) + sH; DMA16H(lo, vl16, rW, so); }
      else if constexpr (s < 9 && k >= 2 && k < 6) {
        constexpr int m = (k - 2) & 1, ks = 2 * s + ((k - 2) >> 1);
        BLD(X[m][ks], vX, rX, m * 18432 + (ks & ~3) * 1024, (ks & 3) * 1024);
      } else if constexpr (s < 9 && k == 6 && !(s & 1)) { BLD(HX[s >> 1], vH, rX, (s >> 1) * 4096, 0); }
    }
  };
  auto group = [&](auto S_) { vop(S_, IC<0>{}); vop(S_, IC<1>{}); vop(S_, IC<2>{}); vop(S_, IC<3>{}); vop(S_, IC<4>{}); vop(S_, IC<5>{}); vop(S_, IC<6>{}); };
  auto sync = [&](auto S_) {
    KA_CAP
    constexpr int s = decltype(S_)::v;
    if constexpr (s < 26) {
      if constexpr (s + 1 < 9) {
        constexpr int k0 = 2 * (s + 1);
        asm volatile("s_waitcnt vmcnt(%5)" : "+v"(X[0][k0]), "+v"(X[1][k0]), "+v"(X[0][k0 + 1]), "+v"(X[1][k0 + 1]), "+v"(HX[(s + 1) >> 1]) : "i"(C::wait(s)) : "memory");
      } else asm volatile("s_waitcnt vmcnt(%0)" :: "i"(C::wait(s)) : "memory");
      __builtin_amdgcn_s_barrier();
    }
  };
  auto step = [&](auto S_, auto H_) {
    KA_CAP
    constexpr int s = decltype(S_)::v, h = decltype(H_)::v;
    constexpr int NT = C::nt(s), job = s / 9, ks = 2 * (s % 9) + h;
    constexpr int s2 = h ? s + 1 : s, h2 = h ^ 1, NT2 = s2 <= 26 ? C::nt(s2) : 0;
    constexpr int roff = (s2 % 6) * KA_SLOT + h2 * NT2 * 1024;
    if constexpr (ks == 0) asm volatile("s_waitcnt lgkmcnt(0)" ::: "memory");
    if constexpr (job == 0) {
      if (w == (ks & 3)) {
        asm volatile("s_waitcnt lgkmcnt(1)" ::: "memory");
#pragma unroll
        for (int n = 0; n < 3; ++n) AMFMA(hacc[n], F[n], HX[ks >> 2]);
      }
    }
#pragma unroll
    for (int n = 0; n < NT; ++n) {
      if constexpr (ks != 0) asm volatile("s_waitcnt lgkmcnt(%0)" :: "i"(NT - 1) : "memory");
      if constexpr (job == 1) {
        if constexpr (ks == 0) { AMFMA0(acc[0][n], X[0][ks], F[n]); AMFMA0(acc[1][n], X[1][ks], F[n]); }
        else { AMFMA(acc[0][n], X[0][ks], F[n]); AMFMA(acc[1][n], X[1][ks], F[n]); }
      } else {
        if constexpr (ks == 0) { AMFMA0(acc[0][n], F[n], X[0][ks]); AMFMA0(acc[1][n], F[n], X[1][ks]); }
        else { AMFMA(acc[0][n], F[n], X[0][ks]); AMFMA(acc[1][n], F[n], X[1][ks]); }
      }
      if (n < NT2) ADSRD(F[n], rd, roff + n * 1024);
      if constexpr (h == 1) {
        if (n == 0) { vop(IC<s + 5>{}, IC<0>{}); vop(IC<s + 5>{}, IC<2>{}); }
        if (n == 1) { vop(IC<s + 5>{}, IC<1>{}); vop(IC<s + 5>{}, IC<3>{}); }
        if (n == 2) { vop(IC<s + 5>{}, IC<4>{}); vop(IC<s + 5>{}, IC<5>{}); }
        if (n == 3) { vop(IC<s + 5>{}, IC<6>{}); }
      }
    }
    if constexpr (NT2 > NT) ADSRD(F[4], rd, roff + 4 * 1024);
  };
#define KA_ACC_DONE() do { asm volatile("s_nop 15" : "+v"(acc[0][0]), "+v"(acc[0][1]), "+v"(acc[0][2]), "+v"(acc[0][3]), "+v"(acc[1][0]), "+v"(acc[1][1]), "+v"(acc[1][2]), "+v"(acc[1][3]) :: "memory"); \
    if constexpr (FAST) asm volatile("" : "+v"(acc[0][4]), "+v"(acc[1][4]) :: "memory"); } while (0)
  auto iter = [&](auto S_) { step(S_, IC<0>{}); sync(S_); step(S_, IC<1>{}); };

  group(IC<0>{}); group(IC<1>{});
  asm volatile("s_waitcnt vmcnt(%5)" : "+v"(X[0][0]), "+v"(X[1][0]), "+v"(X[0][1]), "+v"(X[1][1]), "+v"(HX[0]) : "i"(C::ops(1) + KA_WARM) : "memory");
  __builtin_amdgcn_s_barrier();
#pragma unroll
  for (int n = 0; n < 4; ++n) ADSRD(F[n], rd, n * 1024);

  step(IC<0>{}, IC<0>{}); group(IC<2>{}); group(IC<3>{}); group(IC<4>{}); sync(IC<0>{}); step(IC<0>{}, IC<1>{});
  iter(IC<1>{}); iter(IC<2>{}); iter(IC<3>{}); iter(IC<4>{}); iter(IC<5>{}); iter(IC<6>{}); iter(IC<7>{}); iter(IC<8>{});
  asm volatile("s_nop 15" : "+v"(acc[0][0]), "+v"(acc[0][1]), "+v"(acc[0][2]), "+v"(acc[0][3]), "+v"(acc[1][0]), "+v"(acc[1][1]), "+v"(acc[1][2]), "+v"(acc[1][3]), "+v"(hacc[0]), "+v"(hacc[1]), "+v"(hacc[2]) :: "memory");

  SCHED();
  {
    char* att = smem + KA_ATT;
    char* hp = smem + 2 * KA_SLOT;
#pragma unroll
    for (int n = 0; n < 3; ++n) if (n != w) *(f32x4*)(hp + (n * 3 + (w > n ? w - 1 : w)) * 1024 + vl16) = hacc[n];
#pragma unroll
    for (int m = 0; m < 2; ++m) {
      char* rp = att + (14 + w * 32 + 8 * (li >> 2) + 4 * m + (li & 3)) * KA_PITCH + lg * 8;
#pragma unroll
      for (int n = 0; n < 4; ++n) *(u32x2*)(rp + n * 32) = (u32x2){pk_bf16(acc[m][n][0], acc[m][n][1]), pk_bf16(acc[m][n][2], acc[m][n][3])};
    }
    LBAR();
    if (w < 3) {
      f32x4 a = w == 0 ? hacc[0] : (w == 1 ? hacc[1] : hacc[2]);
#pragma unroll
      for (int q = 0; q < 3; ++q) a += *(const f32x4*)(hp + (w * 3 + q) * 1024 + vl16);
      int ho = (li < 8) ? 8 * (li >> 2) + (li & 3) : 8 * ((li - 8) >> 2) + 4 + (li & 3);
      if (ho >= 2) *(u32x2*)(att + (ho - 2) * KA_PITCH + w * 32 + lg * 8) = (u32x2){pk_bf16(a[0], a[1]), pk_bf16(a[2], a[3])};
    }
    LBAR();
  }
  SCHED();
  PRIO_LO();

  iter(IC<9>{}); iter(IC<10>{}); iter(IC<11>{}); iter(IC<12>{}); iter(IC<13>{}); iter(IC<14>{}); iter(IC<15>{}); iter(IC<16>{}); iter(IC<17>{});
  KA_ACC_DONE();

  SCHED();
  unsigned pfv = 0;
  if constexpr (FAST) {
    {
      const rsrc_t rS = mk_rsrc(wsp, 2048 * 4096);
      const int ks = (sj * 4 + w) & 63, tp = (sj * 4 + w) >> 6, r = (w & 1) * 64 + lane;
      const unsigned vo = (unsigned)prow(tp * 8 + (r >> 4), r & 15) * 4096u + (unsigned)(ks >> 1) * 128u;
      asm volatile("buffer_load_dword %0, %1, %2, 0 offen" : "+v"(pfv) : "v"(vo), "s"(rS));
    }
    if (sj < 90) {
      const int base0 = wc_krow(sj, 0, 0), base1 = wc_krow(sj, 0, 4);
      const rsrc_t rS = sj < 72 ? mk_rsrc(wout, 2304 * 1152) : mk_rsrc(wo - (size_t)2304 * 576, 2880 * 1152);
      const int c = tid, r = c / 9, l = c - r * 9;
      const unsigned vo = (unsigned)((r < 16 ? base0 : base1 - 16) + r) * 1152u + (unsigned)l * 128u;
      asm volatile("buffer_load_dword %0, %1, %2, 0 offen" : "+v"(pfv) : "v"(vo), "s"(rS));
      if (w == 0) {
        const int c2 = 256 + (lane & 31), r2 = c2 / 9, l2 = c2 - r2 * 9;
        const unsigned vo2 = (unsigned)(base1 - 16 + r2) * 1152u + (unsigned)l2 * 128u;
        asm volatile("buffer_load_dword %0, %1, %2, 0 offen" : "+v"(pfv) : "v"(vo2), "s"(rS));
      }
    }
  }
  SCHED();
  PRIO_HI();
  unsigned zs[C::NZ2][4];
  {
#pragma unroll
    for (int n = 0; n < C::NZ2; ++n) {
#pragma unroll
      for (int m = 0; m < 2; ++m) {
        f32x2 a = gelu2((f32x2){acc[m][n][0], acc[m][n][1]}), b = gelu2((f32x2){acc[m][n][2], acc[m][n][3]});
        zs[n][2 * m] = pk_bf16(a[0], a[1]); zs[n][2 * m + 1] = pk_bf16(b[0], b[1]);
      }
    }
    f32x2 s1[4], s2[4];
#pragma unroll
    for (int j = 0; j < 4; ++j) {
      s1[j] = (f32x2){0.f, 0.f}; s2[j] = s1[j];
#pragma unroll
      for (int n = 0; n < C::NZ2; ++n) { f32x2 v = {bf_lo(zs[n][j]), bf_hi(zs[n][j])}; s1[j] += v; s2[j] += v * v; }
    }
    float r[16];
#pragma unroll
    for (int j = 0; j < 4; ++j) { r[2 * j] = s1[j][0]; r[2 * j + 1] = s1[j][1]; r[8 + 2 * j] = s2[j][0]; r[9 + 2 * j] = s2[j][1]; }
    float sel;
    {
      float a[8], c[4];
#pragma unroll
      for (int j = 0; j < 8; j += 4)
        asm volatile("s_nop 1\n\t"
          "v_add_f32_dpp %0, %4, %4 row_ror:8 row_mask:0xf bank_mask:0x3\n\tv_add_f32_dpp %1, %5, %5 row_ror:8 row_mask:0xf bank_mask:0x3\n\t"
          "v_add_f32_dpp %2, %6, %6 row_ror:8 row_mask:0xf bank_mask:0x3\n\tv_add_f32_dpp %3, %7, %7 row_ror:8 row_mask:0xf bank_mask:0x3\n\t"
          "v_add_f32_dpp %0, %8, %8 row_ror:8 row_mask:0xf bank_mask:0xc\n\tv_add_f32_dpp %1, %9, %9 row_ror:8 row_mask:0xf bank_mask:0xc\n\t"
          "v_add_f32_dpp %2, %10, %10 row_ror:8 row_mask:0xf bank_mask:0xc\n\tv_add_f32_dpp %3, %11, %11 row_ror:8 row_mask:0xf bank_mask:0xc"
          : "=&v"(a[j]), "=&v"(a[j + 1]), "=&v"(a[j + 2]), "=&v"(a[j + 3])
          : "v"(r[j]), "v"(r[j + 1]), "v"(r[j + 2]), "v"(r[j + 3]), "v"(r[j + 8]), "v"(r[j + 9]), "v"(r[j + 10]), "v"(r[j + 11]));
      asm volatile("s_nop 1\n\t"
          "v_add_f32_dpp %0, %4, %4 row_half_mirror row_mask:0xf bank_mask:0x5\n\tv_add_f32_dpp %1, %5, %5 row_half_mirror row_mask:0xf bank_mask:0x5\n\t"
          "v_add_f32_dpp %2, %6, %6 row_half_mirror row_mask:0xf bank_mask:0x5\n\tv_add_f32_dpp %3, %7, %7 row_half_mirror row_mask:0xf bank_mask:0x5\n\t"
          "v_add_f32_dpp %0, %8, %8 row_half_mirror row_mask:0xf bank_mask:0xa\n\tv_add_f32_dpp %1, %9, %9 row_half_mirror row_mask:0xf bank_mask:0xa\n\t"
          "v_add_f32_dpp %2, %10, %10 row_half_mirror row_mask:0xf bank_mask:0xa\n\tv_add_f32_dpp %3, %11, %11 row_half_mirror row_mask:0xf bank_mask:0xa\n\ts_nop 1"
          : "=&v"(c[0]), "=&v"(c[1]), "=&v"(c[2]), "=&v"(c[3])
          : "v"(a[0]), "v"(a[1]), "v"(a[2]), "v"(a[3]), "v"(a[4]), "v"(a[5]), "v"(a[6]), "v"(a[7]));
#pragma unroll
      for (int j = 0; j < 4; ++j) c[j] = dpp_add<0x4E>(c[j]);
      float d0 = (li & 2) ? c[2] : c[0], d1 = (li & 2) ? c[3] : c[1];
      d0 = dpp_add<0xB1>(d0); d1 = dpp_add<0xB1>(d1);
      sel = (li & 1) ? d1 : d0;
    }
    double* dst = (double*)(ws + OFF_RS) + (size_t)(p * 128 + w * 32 + lg * 8 + (li & 7)) * 2 + (li >> 3);
    double dv = (double)sel;
    asm volatile("global_atomic_add_f64 %0, %1, off sc1" :: "v"(dst), "v"(dv) : "memory");
  }
  SCHED();

  PRIO_LO();

  unsigned* cnt = (unsigned*)(ws + OFF_CNT + p * 256);
  iter(IC<18>{}); iter(IC<19>{});
  step(IC<20>{}, IC<0>{}); sync(IC<20>{});
  if constexpr (FAST) asm volatile("" :: "v"(pfv));
  if (tid == 0) { unsigned one = 1; asm volatile("global_atomic_add %0, %1, off sc1" :: "v"(cnt), "v"(one) : "memory"); }
  step(IC<20>{}, IC<1>{});
  iter(IC<21>{}); iter(IC<22>{}); iter(IC<23>{}); iter(IC<24>{}); iter(IC<25>{});
  unsigned seen;
  asm volatile("global_load_dword %0, %1, off sc1" : "=v"(seen) : "v"(cnt) : "memory");
  iter(IC<26>{});
  KA_ACC_DONE();
  asm volatile("s_waitcnt vmcnt(0)" : "+v"(seen) :: "memory");
  PRIO_HI();
  const unsigned seen_s = RFL(seen);
  const char* sp = ws + OFF_RS + (size_t)(p * 128 + w * 32 + (lane & 31)) * 16;
  u32x4 st = {0, 0, 0, 0};
  asm volatile("s_cmp_lt_u32 %2, 32\n\ts_cbranch_scc1 1f\n\tglobal_load_dwordx4 %0, %1, off sc1\n1:" : "+v"(st) : "v"(sp), "s"(seen_s) : "memory", "scc");
  SCHED();
  u32x4 wt[8];
  if constexpr (FAST) {
    const int ks = (sj * 4 + w) & 63, tp = (sj * 4 + w) >> 6;
    const rsrc_t rS = mk_rsrc(wsp, 2048 * 4096);
    const unsigned vo = (unsigned)(8 * (li >> 2) + (li & 3)) * 4096u + (unsigned)lg * 16u, so = (unsigned)tp * (128u * 4096u) + (unsigned)ks * 64u;
#pragma unroll
    for (int m = 0; m < 8; ++m) wt[m] = __builtin_amdgcn_raw_buffer_load_b128(rS, vo, so + (unsigned)(32 * (m >> 1) + 4 * (m & 1)) * 4096u, 0);
  }
#pragma unroll
  for (int m = 0; m < 2; ++m) {
    const char* zp = ws + OFF_Z1T + ((size_t)(p * 8 + w * 2 + m) * 16 + fb) * 4608;
    unsigned pk[C::NZ1][2];
#pragma unroll
    for (int n = 0; n < C::NZ1; ++n) {
      f32x2 a = gelu2((f32x2){acc[m][n][0], acc[m][n][1]}), b = gelu2((f32x2){acc[m][n][2], acc[m][n][3]});
      pk[n][0] = pk_bf16(a[0], a[1]); pk[n][1] = pk_bf16(b[0], b[1]);
    }
    st16s<FAST * 2048>(zp, vl16, (u32x4){pk[0][0], pk[0][1], pk[1][0], pk[1][1]});
    st16s<FAST * 2048 + 1024>(zp, vl16, (u32x4){pk[2][0], pk[2][1], pk[3][0], pk[3][1]});
    if constexpr (FAST) st8((char*)zp + 4096 + lane * 8, (u32x2){pk[4][0], pk[4][1]});
  }
  SCHED();
  if constexpr (FAST) {
    const char* wb = ws + OFF_WT + (size_t)(sj * 4 + w) * 8192;
    st16s<0>(wb, vl16, wt[0]); st16s<1024>(wb, vl16, wt[1]); st16s<2048>(wb, vl16, wt[2]); st16s<3072>(wb, vl16, wt[3]);
    st16s<0>(wb + 4096, vl16, wt[4]); st16s<1024>(wb + 4096, vl16, wt[5]); st16s<2048>(wb + 4096, vl16, wt[6]); st16s<3072>(wb + 4096, vl16, wt[7]);
    if (sj < 90) {
      const int ks = sj, base0 = wc_krow(ks, 0, 0), base1 = wc_krow(ks, 0, 4);
      const rsrc_t rS = sj < 72 ? mk_rsrc(wout, 2304 * 1152) : mk_rsrc(wo - (size_t)2304 * 576, 2880 * 1152);
      const int r = tid >> 3;
      const unsigned vo = (unsigned)((r < 16 ? base0 : base1 - 16) + r) * 1152u + (unsigned)(tid & 7) * 16u;
      u32x4 v[9];
#pragma unroll
      for (int k = 0; k < 9; ++k) v[k] = __builtin_amdgcn_raw_buffer_load_b128(rS, vo, k * 128, 0);
      LBAR();
      char* dl = smem + r * K0_PITCH + (tid & 7) * 16;
#pragma unroll
      for (int k = 0; k < 9; ++k) *(u32x4*)(dl + k * 128) = v[k];
      LBAR();
      const unsigned a0 = ldsb + (4 * lg + (li >> 2)) * K0_PITCH + (li & 3) * 8;
#pragma unroll
      for (int e = 0; e < 10; ++e) {
        int f = w * 10 + e, ct = f / 5, n = f - ct * 5, d0 = (ct < 7 ? ct * 72 : 496) + n * 16;
        u32x4 o = tr_frag(a0 + d0 * 2, a0 + d0 * 2 + 16 * K0_PITCH);
        st16s<0>(ws + OFF_WC + (size_t)((ct * 90 + ks) * 5 + n) * 1024, vl16, o);
      }
    }
  }
  asm volatile("s_cmp_ge_u32 %2, 32\n\ts_cbranch_scc1 1f\n\tglobal_load_dword %0, %1, off sc1\n1:" : "+v"(seen) : "v"(cnt), "s"(seen_s) : "memory", "scc");
  unsigned seen_t;
  SCHED();
  {
    char* att = smem + KA_ATT;
    const unsigned attb = ldsb + KA_ATT;
    float mbias[8];
#pragma unroll
    for (int e = 0; e < 8; ++e) { int d = (e >> 2) * 16 + lg * 4 + (e & 3) - li; mbias[e] = (!(d & 1) && d >= 2 && d <= 16) ? 0.f : -__builtin_inff(); }
    bf16x8 Q[2], K[2][2]; u32x2 V[2][2][2];
#pragma unroll
    for (int jj = 0; jj < 2; ++jj) {
      const int j = w * 2 + jj;
      Q[jj] = *(const bf16x8*)(att + (14 + 16 * j + li) * KA_PITCH + 80 + (lg < 3 ? lg : 2) * 16);
#pragma unroll
      for (int kt = 0; kt < 2; ++kt) {
        int R = 16 * j - 2 + 16 * kt + li; R = R < 0 ? 0 : R;
        K[jj][kt] = *(const bf16x8*)(att + R * KA_PITCH + lg * 16);
        int Rv = 16 * j - 2 + 16 * kt + 4 * lg + (li >> 2); Rv = Rv < 0 ? 0 : Rv;
        const unsigned va = attb + Rv * KA_PITCH + 40 + (li & 3) * 8;
        asm volatile("ds_read_b64_tr_b16 %0, %2\n\tds_read_b64_tr_b16 %1, %2 offset:32" : "=&v"(V[jj][0][kt]), "=&v"(V[jj][1][kt]) : "v"(va) : "memory");
      }
    }
    f32x4 S[2][2];
#pragma unroll
    for (int jj = 0; jj < 2; ++jj) {
      if (lg == 3) Q[jj] = (bf16x8){0, 0, 0, 0, 0, 0, 0, 0};
#pragma unroll
      for (int kt = 0; kt < 2; ++kt) S[jj][kt] = MFMA(K[jj][kt], Q[jj], ((f32x4){0.f, 0.f, 0.f, 0.f}));
    }
    bf16x8 P[2];
#pragma unroll
    for (int jj = 0; jj < 2; ++jj) {
      float l[8];
#pragma unroll
      for (int e = 0; e < 8; ++e) l[e] = S[jj][e >> 2][e & 3] + mbias[e];
      if (p == 0 && w == 0 && jj == 0) {
#pragma unroll
        for (int e = 0; e < 4; ++e) l[e] = -__builtin_inff();
      }
      float mx = fmaxf(fmaxf(fmaxf(l[0], l[1]), fmaxf(l[2], l[3])), fmaxf(fmaxf(l[4], l[5]), fmaxf(l[6], l[7])));
      mx = xmax16(mx); mx = xmax32(mx);
      constexpr float CE = 0.23570226f * 1.44269504f;
      const float nm = -mx * CE;
      float sum = 0.f;
#pragma unroll
      for (int e = 0; e < 8; ++e) { l[e] = __builtin_amdgcn_exp2f(l[e] * CE + nm); sum += l[e]; }
      sum = xadd16(sum); sum = xadd32(sum);
      const float inv = __builtin_amdgcn_rcpf(sum);
      u32x4 pk = {pk_bf16(l[0] * inv, l[1] * inv), pk_bf16(l[2] * inv, l[3] * inv), pk_bf16(l[4] * inv, l[5] * inv), pk_bf16(l[6] * inv, l[7] * inv)};
      P[jj] = __builtin_bit_cast(bf16x8, pk);
    }
    asm volatile("s_waitcnt lgkmcnt(0)" : "+v"(V[0][0][0]), "+v"(V[0][0][1]), "+v"(V[0][1][0]), "+v"(V[0][1][1]), "+v"(V[1][0][0]), "+v"(V[1][0][1]), "+v"(V[1][1][0]), "+v"(V[1][1][1]) :: "memory");
    unsigned ov[2][3];
#pragma unroll
    for (int jj = 0; jj < 2; ++jj) {
      f32x4 O[2];
#pragma unroll
      for (int dt = 0; dt < 2; ++dt) {
        u32x4 vv = {V[jj][dt][0][0], V[jj][dt][0][1], V[jj][dt][1][0], V[jj][dt][1][1]};
        O[dt] = MFMA(__builtin_bit_cast(bf16x8, vv), P[jj], ((f32x4){0.f, 0.f, 0.f, 0.f}));
      }
      ov[jj][0] = pk_bf16(O[0][0], O[0][1]); ov[jj][1] = pk_bf16(O[0][2], O[0][3]); ov[jj][2] = pk_bf16(O[1][0], O[1][1]);
    }
    asm volatile("s_waitcnt vmcnt(0)" : "+v"(seen), "+v"(st) :: "memory");
    seen_t = RFL(seen);
    asm volatile("s_cmp_ge_u32 %2, 32\n\ts_cbranch_scc1 1f\n\ts_cmp_lt_u32 %3, 32\n\ts_cbranch_scc1 1f\n\tglobal_load_dwordx4 %0, %1, off sc1\n1:" : "+v"(st) : "v"(sp), "s"(seen_s), "s"(seen_t) : "memory", "scc");
#pragma unroll
    for (int jj = 0; jj < 2; ++jj) {
      const int tl = jj * 16 + li, tt = p * 8 + w * 2 + ((tl >> 2) & 1), slot = (tl >> 3) * 4 + (tl & 3);
      char* ob = ws + OFF_OT + (size_t)tt * 18432 + slot * 8;
      const int d0 = cg * 18 + lg * 4, d1 = d0 + 2, d2 = cg * 18 + 16;
      st4(ob + (d0 >> 2) * 128 + (d0 & 3) * 2, ov[jj][0]);
      st4(ob + (d1 >> 2) * 128 + (d1 & 3) * 2, ov[jj][1]);
      if (lg == 0) st4(ob + (d2 >> 2) * 128 + (d2 & 3) * 2, ov[jj][2]);
    }
  }
  SCHED();
  if (seen_t < 32u) {
    do {
      asm volatile("global_load_dword %0, %1, off sc1\n\ts_waitcnt vmcnt(0)" : "=v"(seen) : "v"(cnt) : "memory");
      if (RFL(seen) >= 32u) break;
      __builtin_amdgcn_s_sleep(KA_SLEEP);
    } while (true);
  }
  asm volatile("s_cmp_ge_u32 %2, 32\n\ts_cbranch_scc1 1f\n\tglobal_load_dwordx4 %0, %1, off sc1\n\ts_waitcnt vmcnt(0)\n1:\n\ts_waitcnt vmcnt(6)" : "+v"(st) : "v"(sp), "s"(seen_t) : "memory", "scc");
  {
    const double sm = __builtin_bit_cast(double, (u32x2){st[0], st[1]}), sq = __builtin_bit_cast(double, (u32x2){st[2], st[3]});
    const double mean = sm * (1.0 / 2304.0);
    const float var = fmaxf((float)(sq * (1.0 / 2304.0) - mean * mean), 0.f);
    const float mb = rbf((float)mean), rs = 1.f / rbf(sqrtf(rbf(rbf(var) + 1e-5f)));
    f32x2* sl = (f32x2*)(smem + 4 * KA_SLOT + w * 256);
    if (lane < 32) sl[lane] = (f32x2){mb, rs};
    __builtin_amdgcn_s_waitcnt(0xC07F);
    __builtin_amdgcn_wave_barrier();
    f32x4 ms[4];
#pragma unroll
    for (int j = 0; j < 4; ++j) ms[j] = *(const f32x4*)((const char*)sl + lg * 64 + j * 16);
    const char* zp = ws + OFF_Z2T + ((size_t)(fb * 64 + p * 4 + w) * 9 + (FAST ? 0 : 5)) * 1024;
    auto z2o = [&](int n) { u32x4 o;
#pragma unroll
      for (int j = 0; j < 4; ++j) o[j] = pk_bf16((bf_lo(zs[n][j]) - ms[j][0]) * ms[j][1], (bf_hi(zs[n][j]) - ms[j][2]) * ms[j][3]);
      return o; };
    st16s<0>(zp, vl16, z2o(0)); st16s<1024>(zp, vl16, z2o(1)); st16s<2048>(zp, vl16, z2o(2)); st16s<3072>(zp, vl16, z2o(3));
    if constexpr (FAST) st16s<0>(zp + 4096, vl16, z2o(4));
  }
}
__global__ __launch_bounds__(256, 2) void kA(const char* __restrict__ ws_c, char* __restrict__ ws, const u16* __restrict__ wsp, const u16* __restrict__ wout, const u16* __restrict__ wo) {
  extern __shared__ __attribute__((aligned(16))) char smem[];
  const int b = blockIdx.x, xcd = b & 7, idx = b >> 3;
  const int p = (xcd & 1) * 8 + (idx & 7), q = idx >> 3, cg = (xcd >> 1) * 8 + (q & 3) * 2 + (q >> 2);
  if (idx < 32) ka_body<1>(smem, ws_c, ws, p, cg, b, wsp, wout, wo); else ka_body<0>(smem, ws_c, ws, p, cg, b, wsp, wout, wo);
}

constexpr int KB_STAGE = 36864;
constexpr int KB_LDS = 4 * KB_STAGE;
__global__ __launch_bounds__(512, 2) void kB(const char* __restrict__ ws_c, char* __restrict__ ws, const u16* __restrict__ bsp) {
  extern __shared__ __attribute__((aligned(16))) char smem[];
  const int tid = threadIdx.x, lane = tid & 63;
  const int w = RFL(tid >> 6), wm = w & 3, kh = w >> 2;
  const int b = blockIdx.x, xcd = b & 7, idx = b >> 3;
  const int tp = (xcd & 1) * 8 + (idx & 7), fb = (xcd >> 1) * 4 + (idx >> 3);
  const rsrc_t rZ = mk_rsrc(ws_c + OFF_Z2T + (size_t)fb * (64 * 9216), 64 * 9216);
  const rsrc_t rW = mk_rsrc(ws_c + OFF_WT + (size_t)tp * (64 * 8192), 64 * 8192);
  const unsigned ldsb = (unsigned)(size_t)(__attribute__((address_space(3))) char*)smem;
  const unsigned vl16 = lane * 16;
  const unsigned vW = vl16 + wm * 2048 + kh * 16384;
  const unsigned dmaF = w * 4096, dmaH = 32768 + w * 512;
  unsigned rd01 = ldsb + kh * 18432 + lane * 16;
  unsigned rd23 = rd01 + 2 * KB_STAGE;

  f32x4 acc[2][9];
#pragma unroll
  for (int m = 0; m < 2; ++m)
#pragma unroll
    for (int n = 0; n < 9; ++n) acc[m][n] = (f32x4){0.f, 0.f, 0.f, 0.f};
  bf16x8 Wr[4][4];
  bf16x8 ZA[9], ZB[9];

#define KB_DMA1(stage, slot, imm) do { \
    unsigned so_ = RFL((unsigned)(stage) * KB_STAGE + dmaF), lo_ = RFL(ldsb + (slot) * KB_STAGE + dmaF); DMA16(lo_, vl16, rZ, so_, imm); } while (0)
#define KB_DMAH(stage, slot) do { \
    unsigned so2_ = RFL((unsigned)(stage) * KB_STAGE + dmaH), lo2_ = RFL(ldsb + (slot) * KB_STAGE + dmaH); DMA16H(lo2_, vl16, rZ, so2_); } while (0)
#define KB_W1(stage, slot, e) do { unsigned sw_ = RFL((unsigned)(stage) * 32768u + ((e) >> 1) * 8192u); \
    if ((e) & 1) BLOAD16(Wr[slot][e], vW, rW, sw_, 1024); else BLOAD16(Wr[slot][e], vW, rW, sw_, 0); } while (0)
#define KB_ALL(stage, slot) do { KB_DMA1(stage, slot, 0); KB_DMA1(stage, slot, 1024); KB_DMA1(stage, slot, 2048); KB_DMA1(stage, slot, 3072); KB_DMAH(stage, slot); \
    KB_W1(stage, slot, 0); KB_W1(stage, slot, 1); KB_W1(stage, slot, 2); KB_W1(stage, slot, 3); } while (0)
#define KB_WAIT(slot, N) asm volatile("s_waitcnt vmcnt(" #N ") lgkmcnt(0)" : "+v"(Wr[slot][0]), "+v"(Wr[slot][1]), "+v"(Wr[slot][2]), "+v"(Wr[slot][3]) :: "memory")

  KB_ALL(0, 0);
  __builtin_amdgcn_s_barrier();
  KB_ALL(1, 1);
  KB_WAIT(0, 9);
  __builtin_amdgcn_s_barrier();

  const rsrc_t rZ1 = mk_rsrc(ws_c + OFF_Z1T + ((size_t)(tp * 8 + wm * 2) * 16 + fb) * 4608, 17 * 4608);
  const rsrc_t rB = mk_rsrc(bsp, 4096);
  u32x4 z1q[2][2]; u32x2 z1s[2]; unsigned biasu[2];
  const unsigned vz1 = lane * 16 + kh * 2048, vz8 = lane * 8 + 4096;
  const unsigned vb = (tp * 128 + wm * 32 + ((lane & 15) >> 2) * 8 + (lane & 3)) * 2;
#define KB_EPI_LD(e) do { \
    if ((e) == 0) BLOAD16(z1q[0][0], vz1, rZ1, 0, 0); \
    if ((e) == 1) BLOAD16(z1q[0][1], vz1, rZ1, 0, 1024); \
    if ((e) == 2) asm volatile("buffer_load_dwordx4 %0, %1, %2, %3 offen" : "=v"(z1q[1][0]) : "v"(vz1), "s"(rZ1), "s"(16 * 4608)); \
    if ((e) == 3) asm volatile("buffer_load_dwordx4 %0, %1, %2, %3 offen offset:1024" : "=v"(z1q[1][1]) : "v"(vz1), "s"(rZ1), "s"(16 * 4608)); \
    if ((e) == 4) asm volatile("buffer_load_dwordx2 %0, %1, %2, 0 offen" : "=v"(z1s[0]) : "v"(vz8), "s"(rZ1)); \
    if ((e) == 5) asm volatile("buffer_load_dwordx2 %0, %1, %2, %3 offen" : "=v"(z1s[1]) : "v"(vz8), "s"(rZ1), "s"(16 * 4608)); \
    if ((e) == 6) asm volatile("buffer_load_ushort %0, %1, %2, 0 offen" : "=v"(biasu[0]) : "v"(vb), "s"(rB)); \
    if ((e) == 7) asm volatile("buffer_load_ushort %0, %1, %2, 0 offen offset:8" : "=v"(biasu[1]) : "v"(vb), "s"(rB)); } while (0)

#define KB_ITER(i, j, MODE, WN) do { \
    _Pragma("unroll") for (int n = 0; n < 9; ++n) { \
      ADSRD(ZA[n], ((j) < 2 ? rd01 : rd23), ((j) & 1) * KB_STAGE + n * 1024); \
      if ((MODE) != 1) { AMFMA(acc[0][n], ZB[n], Wr[((j) + 3) & 3][2]); AMFMA(acc[1][n], ZB[n], Wr[((j) + 3) & 3][3]); } \
      if ((MODE) == 0) { \
        if (n == 0) KB_DMA1((i) + 3, ((j) + 3) & 3, 0); \
        if (n == 2) KB_DMA1((i) + 3, ((j) + 3) & 3, 1024); \
        if (n == 4) KB_W1((i) + 3, ((j) + 3) & 3, 0); \
        if (n == 6) KB_DMA1((i) + 3, ((j) + 3) & 3, 2048); \
        if (n == 8) KB_W1((i) + 3, ((j) + 3) & 3, 1); } \
      if ((MODE) == 2 && !(n & 1)) KB_EPI_LD(n >> 1); } \
    if ((MODE) == 1) { KB_ALL(2, 2); KB_DMA1(3, 3, 0); KB_DMA1(3, 3, 1024); KB_W1(3, 3, 0); KB_DMA1(3, 3, 2048); KB_W1(3, 3, 1); } \
    _Pragma("unroll") for (int n = 0; n < 9; ++n) { \
      if (n == 0) asm volatile("s_waitcnt lgkmcnt(6)" ::: "memory"); \
      if (n == 2) asm volatile("s_waitcnt lgkmcnt(5)" ::: "memory"); \
      if (n == 5) asm volatile("s_waitcnt lgkmcnt(5)" ::: "memory"); \
      ADSRD(ZB[n], ((j) < 2 ? rd01 : rd23), ((j) & 1) * KB_STAGE + 9216 + n * 1024); \
      AMFMA(acc[0][n], ZA[n], Wr[j][0]); AMFMA(acc[1][n], ZA[n], Wr[j][1]); \
      if ((MODE) < 2) { \
        if (n == 0) KB_DMA1((i) + 3, ((j) + 3) & 3, 3072); \
        if (n == 2) KB_W1((i) + 3, ((j) + 3) & 3, 2); \
        if (n == 4) KB_DMAH((i) + 3, ((j) + 3) & 3); \
        if (n == 6) KB_W1((i) + 3, ((j) + 3) & 3, 3); } \
      if ((MODE) == 2 && (n == 0 || n == 2 || n == 4)) KB_EPI_LD(5 + (n >> 1)); } \
    KB_WAIT(((j) + 1) & 3, WN); \
    __builtin_amdgcn_s_barrier(); \
    } while (0)

  KB_ITER(0, 0, 1, 18);
  KB_ITER(1, 1, 0, 18);
  KB_ITER(2, 2, 0, 18);
  KB_ITER(3, 3, 0, 18);
  for (int io = 4; io < 12; io += 4) {
    KB_ITER(io + 0, 0, 0, 18);
    KB_ITER(io + 1, 1, 0, 18);
    KB_ITER(io + 2, 2, 0, 18);
    KB_ITER(io + 3, 3, 0, 18);
  }
  KB_ITER(12, 0, 0, 18);
  KB_ITER(13, 1, 2, 17);
  KB_ITER(14, 2, 3, 8);
  {
#pragma unroll
    for (int n = 0; n < 9; ++n) { ADSRD(ZA[n], rd23, KB_STAGE + n * 1024); AMFMA(acc[0][n], ZB[n], Wr[2][2]); AMFMA(acc[1][n], ZB[n], Wr[2][3]); }
#pragma unroll
    for (int n = 0; n < 9; ++n) {
      if (n == 0) asm volatile("s_waitcnt lgkmcnt(6)" ::: "memory");
      if (n == 2) asm volatile("s_waitcnt lgkmcnt(5)" ::: "memory");
      if (n == 5) asm volatile("s_waitcnt lgkmcnt(5)" ::: "memory");
      ADSRD(ZB[n], rd23, KB_STAGE + 9216 + n * 1024);
      AMFMA(acc[0][n], ZA[n], Wr[3][0]); AMFMA(acc[1][n], ZA[n], Wr[3][1]); }
    asm volatile("s_waitcnt lgkmcnt(0)" ::: "memory");
#pragma unroll
    for (int n = 0; n < 9; ++n) { AMFMA(acc[0][n], ZB[n], Wr[3][2]); AMFMA(acc[1][n], ZB[n], Wr[3][3]); }
  }
  asm volatile("s_nop 15" : "+v"(acc[0][0]), "+v"(acc[0][1]), "+v"(acc[0][2]), "+v"(acc[0][3]), "+v"(acc[0][4]), "+v"(acc[0][5]), "+v"(acc[0][6]), "+v"(acc[0][7]), "+v"(acc[0][8]),
               "+v"(acc[1][0]), "+v"(acc[1][1]), "+v"(acc[1][2]), "+v"(acc[1][3]), "+v"(acc[1][4]), "+v"(acc[1][5]), "+v"(acc[1][6]), "+v"(acc[1][7]), "+v"(acc[1][8]) :: "memory");
  asm volatile("s_waitcnt vmcnt(0)" : "+v"(z1q[0][0]), "+v"(z1q[0][1]), "+v"(z1q[1][0]), "+v"(z1q[1][1]), "+v"(z1s[0]), "+v"(z1s[1]), "+v"(biasu[0]), "+v"(biasu[1]) :: "memory");
  SCHED();
  char* hp = ws + OFF_HT + ((size_t)(tp * 8 + wm * 2) * 16 + fb) * 4608;
  float bias[2] = {bf_lo(biasu[0]), bf_lo(biasu[1])};
  char* ex = smem + wm * 18432 + lane * 16;
  if (kh == 0) {
#pragma unroll
    for (int m = 0; m < 2; ++m)
#pragma unroll
      for (int n = 4; n < 8; ++n) *(f32x4*)(ex + (m * 9 + n) * 1024) = acc[m][n];
  } else {
#pragma unroll
    for (int m = 0; m < 2; ++m) {
#pragma unroll
      for (int n = 0; n < 4; ++n) *(f32x4*)(ex + (m * 9 + n) * 1024) = acc[m][n];
      *(f32x4*)(ex + (m * 9 + 8) * 1024) = acc[m][8];
    }
  }
  __syncthreads();
#pragma unroll
  for (int m = 0; m < 2; ++m) {
#pragma unroll
    for (int q = 0; q < 2; ++q) {
      f32x4 g0, g1;
      if (kh == 0) { g0 = acc[m][2 * q] + *(const f32x4*)(ex + (m * 9 + 2 * q) * 1024); g1 = acc[m][2 * q + 1] + *(const f32x4*)(ex + (m * 9 + 2 * q + 1) * 1024); }
      else { g0 = acc[m][4 + 2 * q] + *(const f32x4*)(ex + (m * 9 + 4 + 2 * q) * 1024); g1 = acc[m][5 + 2 * q] + *(const f32x4*)(ex + (m * 9 + 5 + 2 * q) * 1024); }
      u32x4 z = z1q[m][q], o;
      o[0] = pk_bf16(bf_lo(z[0]) * (g0[0] + bias[m]), bf_hi(z[0]) * (g0[1] + bias[m]));
      o[1] = pk_bf16(bf_lo(z[1]) * (g0[2] + bias[m]), bf_hi(z[1]) * (g0[3] + bias[m]));
      o[2] = pk_bf16(bf_lo(z[2]) * (g1[0] + bias[m]), bf_hi(z[2]) * (g1[1] + bias[m]));
      o[3] = pk_bf16(bf_lo(z[3]) * (g1[2] + bias[m]), bf_hi(z[3]) * (g1[3] + bias[m]));
      st16(hp + m * (16 * 4608) + (kh * 2 + q) * 1024 + lane * 16, o);
    }
    if (kh == 0) {
      f32x4 g0 = acc[m][8] + *(const f32x4*)(ex + (m * 9 + 8) * 1024);
      u32x2 z = z1s[m], o;
      o[0] = pk_bf16(bf_lo(z[0]) * (g0[0] + bias[m]), bf_hi(z[0]) * (g0[1] + bias[m]));
      o[1] = pk_bf16(bf_lo(z[1]) * (g0[2] + bias[m]), bf_hi(z[1]) * (g0[3] + bias[m]));
      st8(hp + m * (16 * 4608) + 4096 + lane * 8, o);
    }
  }
}

constexpr int KC_PITCH = 336;
constexpr int KC_PART = 64 * KC_PITCH;
constexpr int KC_LDS = 5 * KC_PART;
template <bool ISO>
__device__ __forceinline__ void kc_main(f32x4 (&acc)[4][5], rsrc_t rA, rsrc_t rW, unsigned vA16, unsigned vA8, unsigned vW, unsigned sA, unsigned sW) {
  constexpr unsigned TS = ISO ? 18432u : 16u * 4608u;
  constexpr int PD = 3;
  bf16x8 A[PD + 1][4], W[PD + 1][5];
  auto load = [&](int u, int slot) {
#pragma unroll
    for (int m = 0; m < 4; ++m) {
      if (ISO || (u % 9) == 4) {
        unsigned o0, o1;
        if (ISO) { o0 = sA + m * TS + u * 1024; o1 = o0 + 512; }
        else { o0 = sA + m * TS + (u / 9) * 2 * 4608 + 4096; o1 = o0 + 4608; }
        u32x2 lo = __builtin_amdgcn_raw_buffer_load_b64(rA, vA8, o0, 0);
        u32x2 hi = __builtin_amdgcn_raw_buffer_load_b64(rA, vA8, o1, 0);
        u32x4 v = {lo[0], lo[1], hi[0], hi[1]};
        A[slot][m] = __builtin_bit_cast(bf16x8, v);
      } else {
        int i = u % 9;
        unsigned o = sA + m * TS + (u / 9) * 2 * 4608 + (i < 4 ? i * 1024 : 4608 + (i - 5) * 1024);
        A[slot][m] = __builtin_bit_cast(bf16x8, __builtin_amdgcn_raw_buffer_load_b128(rA, vA16, o, 0));
      }
    }
#pragma unroll
    for (int n = 0; n < 5; ++n) W[slot][n] = __builtin_bit_cast(bf16x8, __builtin_amdgcn_raw_buffer_load_b128(rW, vW, sW + (u * 5 + n) * 1024, 0));
  };
#pragma unroll
  for (int u = 0; u < PD; ++u) load(u, u);
  SCHED();
#pragma unroll
  for (int u = 0; u < 18; ++u) {
    if (u + PD < 18) load(u + PD, (u + PD) % (PD + 1));
    SCHED();
#pragma unroll
    for (int m = 0; m < 4; ++m)
#pragma unroll
      for (int n = 0; n < 5; ++n) acc[m][n] = MFMA(W[u % (PD + 1)][n], A[u % (PD + 1)][m], acc[m][n]);
    SCHED();
  }
}
__global__ __launch_bounds__(320, 2) void kC(const char* __restrict__ ws_c, const u16* __restrict__ x, u16* __restrict__ out) {
  extern __shared__ __attribute__((aligned(16))) char smem[];
  const int tid = threadIdx.x, lane = tid & 63;
  const int w = RFL(tid >> 6);
  const int b = blockIdx.x, xcd = b & 7, idx = b >> 3;
  const int rt = (xcd & 3) * 8 + (idx & 7), ct = (xcd >> 2) * 4 + (idx >> 3);
  f32x4 acc[4][5];
#pragma unroll
  for (int m = 0; m < 4; ++m)
#pragma unroll
    for (int n = 0; n < 5; ++n) acc[m][n] = (f32x4){0.f, 0.f, 0.f, 0.f};
  const rsrc_t rW = mk_rsrc(ws_c + OFF_WC, (int)SZ_WC);
  const unsigned sW = RFL((unsigned)((ct * 90 + w * 18) * 5 * 1024));
  const int dstart = ct < 7 ? ct * 72 : 496;
  u32x4 xv[2]; unsigned gof[2];
#pragma unroll
  for (int e = 0; e < 2; ++e) {
    int c = tid + e * 320; c = c < 576 ? c : tid;
    const int r = c / 9, ch = c - r * 9;
    gof[e] = (unsigned)(rt * 64 + prow(r >> 4, r & 15)) * 1152u + (unsigned)(ct * 72 + ch * 8) * 2u;
    asm volatile("global_load_dwordx4 %0, %1, %2" : "=v"(xv[e]) : "v"(gof[e]), "s"(x));
  }
  if (w < 4) {
    const rsrc_t rA = mk_rsrc(ws_c + OFF_HT, (int)SZ_Z);
    kc_main<false>(acc, rA, rW, lane * 16, lane * 8, lane * 16, RFL((unsigned)((rt * 4 * 16 + w * 4) * 4608)), sW);
  } else {
    const rsrc_t rA = mk_rsrc(ws_c + OFF_OT, (int)SZ_OT);
    kc_main<true>(acc, rA, rW, lane * 16, lane * 8, lane * 16, RFL((unsigned)(rt * 4 * 18432)), sW);
  }
  {
    char* p = smem + w * KC_PART + (lane & 15) * KC_PITCH + (lane >> 4) * 16;
#pragma unroll
    for (int m = 0; m < 4; ++m)
#pragma unroll
      for (int n = 0; n < 5; ++n) *(f32x4*)(p + m * 16 * KC_PITCH + n * 64) = acc[m][n];
  }
  asm volatile("s_waitcnt vmcnt(0) lgkmcnt(0)\n\ts_barrier" : "+v"(xv[0]), "+v"(xv[1]) :: "memory");
  const int own0 = ct * 72 - dstart;
#pragma unroll
  for (int e = 0; e < 2; ++e) {
    const int c = tid + e * 320;
    if (c < 576) {
      const int r = c / 9, ch = c - r * 9;
      const char* p = smem + r * KC_PITCH + (own0 + ch * 8) * 4;
      f32x4 s0 = *(const f32x4*)p, s1 = *(const f32x4*)(p + 16);
#pragma unroll
      for (int ww = 1; ww < 5; ++ww) { s0 += *(const f32x4*)(p + ww * KC_PART); s1 += *(const f32x4*)(p + ww * KC_PART + 16); }
      u32x4 o;
      o[0] = pk_bf16(s0[0] + bf_lo(xv[e][0]), s0[1] + bf_hi(xv[e][0]));
      o[1] = pk_bf16(s0[2] + bf_lo(xv[e][1]), s0[3] + bf_hi(xv[e][1]));
      o[2] = pk_bf16(s1[0] + bf_lo(xv[e][2]), s1[1] + bf_hi(xv[e][2]));
      o[3] = pk_bf16(s1[2] + bf_lo(xv[e][3]), s1[3] + bf_hi(xv[e][3]));
      st16((char*)out + gof[e], o);
    }
  }
}

constexpr int K0_BLOCKS = 280;
#define K0LD(p) __builtin_nontemporal_load(p)
#define XLD(p) (*(p))
constexpr int PF0_BYTES = 12288;
#define PF_CODE_N 16
__global__ __launch_bounds__(256) void k0(const u16* __restrict__ x, const u16* __restrict__ wqkv, const u16* __restrict__ win, char* __restrict__ ws, const u16* __restrict__ wsp, const u16* __restrict__ wout, const u16* __restrict__ wo, const u16* __restrict__ bsp) {
  __shared__ __attribute__((aligned(16))) char sm[32 * K0_PITCH];
  const int tid = threadIdx.x, lane = tid & 63, i = lane & 15, g = lane >> 4, w = tid >> 6, braw = blockIdx.x;
  const int b = braw < 64 ? braw + 216 : braw - 64;
  const unsigned smb = (unsigned)(size_t)(__attribute__((address_space(3))) char*)sm;
  unsigned pf0 = 0, pf1 = 0, pf2 = 0;
  if (w == 3 && (braw >> 3) < PF_CODE_N) {
    const int u = braw >> 3;
    unsigned long long pa, p0;
    asm volatile("s_getpc_b64 s[20:21]\n\ts_add_u32 s20, s20, _Z2kAPKcPcPKtS3_S3_@rel32@lo+4\n\ts_addc_u32 s21, s21, _Z2kAPKcPcPKtS3_S3_@rel32@hi+12\n\ts_mov_b64 %0, s[20:21]" : "=s"(pa) :: "scc", "s20", "s21");
    asm volatile("s_getpc_b64 s[20:21]\n\ts_add_u32 s20, s20, _Z2k0PKtS0_S0_PcS0_S0_S0_S0_@rel32@lo+4\n\ts_addc_u32 s21, s21, _Z2k0PKtS0_S0_PcS0_S0_S0_S0_@rel32@hi+12\n\ts_mov_b64 %0, s[20:21]" : "=s"(p0) :: "scc", "s20", "s21");
    const long long span = (long long)(p0 - pa);
    int off = (u < 2 ? u : u - 2) * 8192 + lane * 128;
    const int lim = u < 2 ? PF0_BYTES : (span > 0 && span < (1 << 20) ? (int)span : 0);
    off = off < lim ? off : 0;
    const char* a = (const char*)(u < 2 ? p0 : pa) + off;
    asm volatile("global_load_dword %0, %1, off" : "=v"(pf0) : "v"(a));
  }
  if (b == 217 && w == 1) { const char* a = (const char*)bsp + (lane & 31) * 128; asm volatile("global_load_dword %0, %1, off" : "=v"(pf1) : "v"(a)); }
  if (b < 144) {
    const int ks = b >> 3, cb = b & 7;
    u32x4 v[9];
    const u16* src = win + (size_t)(ks * 32 + (tid >> 3)) * 4608 + cb * 576 + (tid & 7) * 8;
#pragma unroll
    for (int k = 0; k < 9; ++k) v[k] = K0LD((const u32x4*)(src + k * 64));
    char* dl = sm + (tid >> 3) * K0_PITCH + (tid & 7) * 16;
#pragma unroll
    for (int k = 0; k < 9; ++k) *(u32x4*)(dl + k * 128) = v[k];
    __syncthreads();
    const unsigned a0 = smb + (8 * g + (i >> 2)) * K0_PITCH + (i & 3) * 8;
#pragma unroll
    for (int e = 0; e < 9; ++e) {
      int nt = w * 9 + e;
      int gt = cb * 36 + nt;
      int z2 = gt >= 144, t = gt - z2 * 144, fb = t / 9, n = t - fb * 9;
      int fast = z2 ? n < 5 : n >= 4, nz = 4 + fast;
      int fr = fb * 468 + (fast ? 0 : 252) + 72 + (z2 ? ks * nz + n - (fast ? 0 : 5) : 18 * nz + ks * nz + n - (fast ? 4 : 0));
      u32x4 o = tr_frag(a0 + nt * 32, a0 + nt * 32 + 4 * K0_PITCH);
      st16(ws + OFF_W1 + (size_t)fr * 1024 + lane * 16, o);
    }
  } else if (b < 216) {
    const int u = b - 144, ks = u >> 2, hq = u & 3;
    u32x4 v[7]; int dst[7], dw0[7];
#pragma unroll
    for (int k = 0; k < 7; ++k) {
      int c = tid + k * 256; c = c < 32 * 54 ? c : 32 * 54 - 1;
      const int r = c / 54, e = c - r * 54, part = e / 18, ch = e - part * 18;
      v[k] = K0LD((const u32x4*)(wqkv + (size_t)(ks * 32 + r) * 1728 + part * 576 + hq * 144 + ch * 8));
      dst[k] = r * K0_PITCH + (part == 0 ? 80 : (part == 1 ? 0 : 40)); dw0[k] = ch * 4;
    }
#pragma unroll
    for (int z = 0; z < 5; ++z) {
      const int c = tid + z * 256, r = c / 40, e = c - r * 40, hl = e / 5, jj = e - hl * 5;
      *(unsigned*)(sm + r * K0_PITCH + hl * 128 + (jj == 0 ? 9 : (jj == 1 ? 19 : 27 + jj)) * 4) = 0u;
    }
#pragma unroll
    for (int k = 0; k < 7; ++k)
#pragma unroll
      for (int q = 0; q < 4; ++q) { const int dw = dw0[k] + q, hl = (dw * 57) >> 9; *(unsigned*)(sm + dst[k] + hl * 128 + (dw - hl * 9) * 4) = v[k][q]; }
    __syncthreads();
    const unsigned a0 = smb + (8 * g + (i >> 2)) * K0_PITCH + (i & 3) * 8;
#pragma unroll
    for (int e = 0; e < 8; ++e) {
      const int f = w * 8 + e, hl = f >> 2, n = f & 3;
      u32x4 o = tr_frag(a0 + f * 32, a0 + f * 32 + 4 * K0_PITCH);
      st16(ws + OFF_W1 + (size_t)(((hq * 8 + hl) >> 1) * 468 + (hl & 1) * 252 + ks * 4 + n) * 1024 + lane * 16, o);
    }
  } else {
    static_assert((2048 * 16 + 16 * 256) / 16 == 64 * 36, "stat words");
    if (tid < 36) st16(ws + OFF_RS + ((b - 216) * 36 + tid) * 16, (u32x4){0, 0, 0, 0});
    const int u = (b - 216) * 4 + w, tt = u >> 1, slot = (u & 1) * 8 + (lane >> 3), c0 = lane & 7;
    const u16* xr = x + (size_t)prow(tt, slot) * 576 + c0 * 8;
    u32x4 v[9];
#pragma unroll
    for (int k = 0; k < 9; ++k) v[k] = XLD((const u32x4*)(xr + k * 64));
    float s = 0.f;
#pragma unroll
    for (int k = 0; k < 9; ++k)
#pragma unroll
      for (int j = 0; j < 4; ++j) s += bf_lo(v[k][j]) + bf_hi(v[k][j]);
    s += __shfl_xor(s, 1); s += __shfl_xor(s, 2); s += __shfl_xor(s, 4);
    const float mean = s * (1.f / 576.f);
    float q = 0.f;
#pragma unroll
    for (int k = 0; k < 9; ++k)
#pragma unroll
      for (int j = 0; j < 4; ++j) { float a = bf_lo(v[k][j]) - mean, c = bf_hi(v[k][j]) - mean; q += a * a + c * c; }
    q += __shfl_xor(q, 1); q += __shfl_xor(q, 2); q += __shfl_xor(q, 4);
    const float mb = rbf(mean), sd = rbf(sqrtf(rbf(rbf(q * (1.f / 576.f)) + 1e-5f))), rs = 1.f / sd;
    char* dst = ws + OFF_XT + (size_t)tt * 18432 + slot * 16;
#pragma unroll
    for (int k = 0; k < 9; ++k) {
      u32x4 o;
#pragma unroll
      for (int j = 0; j < 4; ++j) o[j] = pk_bf16(rbf(bf_lo(v[k][j]) - mb) * rs, rbf(bf_hi(v[k][j]) - mb) * rs);
      int c = c0 + 8 * k;
      st16(dst + (c >> 2) * 1024 + (c & 3) * 256, o);
    }
  }
  asm volatile("s_waitcnt vmcnt(0)" : "+v"(pf0), "+v"(pf1), "+v"(pf2) :: "memory");
}

extern "C" void kernel_launch(void* const* d_in, const int* in_sizes, int n_in, void* d_out, int out_size, void* d_ws, size_t ws_size, hipStream_t stream) {
  (void)in_sizes; (void)n_in; (void)out_size; (void)ws_size;
  const u16* x = (const u16*)d_in[0];
  char* ws = (char*)d_ws;
  (void)hipFuncSetAttribute((const void*)kA, hipFuncAttributeMaxDynamicSharedMemorySize, KA_LDS);
  (void)hipFuncSetAttribute((const void*)kB, hipFuncAttributeMaxDynamicSharedMemorySize, KB_LDS);
  (void)hipFuncSetAttribute((const void*)kC, hipFuncAttributeMaxDynamicSharedMemorySize, KC_LDS);
  k0<<<K0_BLOCKS, 256, 0, stream>>>(x, (const u16*)d_in[1], (const u16*)d_in[4], ws, (const u16*)d_in[5], (const u16*)d_in[7], (const u16*)d_in[2], (const u16*)d_in[6]);
  kA<<<512, 256, KA_LDS, stream>>>(ws, ws, (const u16*)d_in[5], (const u16*)d_in[7], (const u16*)d_in[2]);
  kB<<<256, 512, KB_LDS, stream>>>(ws, ws, (const u16*)d_in[6]);
  kC<<<256, 320, KC_LDS, stream>>>(ws, x, (u16*)d_out);
}
```

```cpp
#include <hip/hip_runtime.h>
#include <hip/hip_bf16.h>
#include <stdint.h>

typedef unsigned short u16;
typedef __attribute__((ext_vector_type(8))) short bf16x8;
typedef __attribute__((ext_vector_type(4))) short bf16x4;
typedef __attribute__((ext_vector_type(4))) float f32x4;
typedef __attribute__((ext_vector_type(4))) unsigned u32x4;
typedef __attribute__((ext_vector_type(2))) unsigned u32x2;
typedef __amdgpu_buffer_rsrc_t rsrc_t;

constexpr int S_ = 2048, D_ = 576, F_ = 4608, FH_ = 2304;
constexpr size_t OFF_Z2T = 0;
constexpr size_t SZ_Z = (size_t)16 * 64 * 9 * 1024;
constexpr size_t OFF_Z1T = OFF_Z2T + SZ_Z;
constexpr size_t OFF_HT = OFF_Z1T + SZ_Z;
constexpr size_t OFF_WT = OFF_HT + SZ_Z;
constexpr size_t SZ_WT = (size_t)16 * 64 * 8 * 1024;
constexpr size_t OFF_OT = OFF_WT + SZ_WT;
constexpr size_t SZ_OT = (size_t)128 * 144 * 128;
constexpr size_t OFF_WC = OFF_OT + SZ_OT;
constexpr size_t SZ_WC = (size_t)8 * 90 * 5 * 1024;
constexpr size_t OFF_XT = OFF_WC + SZ_WC;
constexpr size_t SZ_XT = (size_t)128 * 18 * 1024;
constexpr size_t OFF_W1 = OFF_XT + SZ_XT;
constexpr size_t SZ_W1 = (size_t)32 * 234 * 1024;
constexpr size_t OFF_RS = OFF_W1 + SZ_W1;
constexpr size_t OFF_CNT = OFF_RS + 2048 * 16;
constexpr size_t OFF_ST = OFF_CNT + 16 * 256;
constexpr size_t WS_END = OFF_ST + (1 << 20);

#define RFL(x) __builtin_amdgcn_readfirstlane(x)
#define SCHED() __builtin_amdgcn_sched_barrier(0)
__device__ __forceinline__ rsrc_t mk_rsrc(const void* p, int bytes) { return __builtin_amdgcn_make_buffer_rsrc((void*)p, (short)0, bytes, 0x00020000); }
__device__ __forceinline__ float bf_lo(unsigned w) { return __uint_as_float(w << 16); }
__device__ __forceinline__ float bf_hi(unsigned w) { return __uint_as_float(w & 0xffff0000u); }
__device__ __forceinline__ unsigned pk_bf16(float lo, float hi) {
  typedef __attribute__((ext_vector_type(2))) float f32x2; typedef __attribute__((ext_vector_type(2))) __bf16 bf2;
  f32x2 v = {lo, hi}; bf2 r = __builtin_convertvector(v, bf2); return __builtin_bit_cast(unsigned, r);
}
__device__ __forceinline__ float rbf(float f) { return bf_hi(pk_bf16(0.f, f)); }
__host__ __device__ __forceinline__ int prow(int tt, int i) { return 32 * (tt >> 1) + 8 * (i >> 2) + 4 * (tt & 1) + (i & 3); }
__device__ __forceinline__ void st16(void* p, u32x4 v) { asm volatile("global_store_dwordx4 %0, %1, off sc1\n\ts_nop 1" :: "v"(p), "v"(v) : "memory"); }
template <int IMM> __device__ __forceinline__ void st16s(const char* base, unsigned voff, u32x4 v) {
  asm volatile("global_store_dwordx4 %0, %1, %2 offset:%3 sc1\n\ts_nop 1" :: "v"(voff), "v"(v), "s"(base), "i"(IMM) : "memory"); }
__device__ __forceinline__ void st8(void* p, u32x2 v) { asm volatile("global_store_dwordx2 %0, %1, off sc1\n\ts_nop 1" :: "v"(p), "v"(v) : "memory"); }
__device__ __forceinline__ void st4(void* p, unsigned v) { asm volatile("global_store_dword %0, %1, off sc1\n\ts_nop 1" :: "v"(p), "v"(v) : "memory"); }
#define MFMA(a, b, c) __builtin_amdgcn_mfma_f32_16x16x32_bf16(a, b, c, 0, 0, 0)

#define DMA16(lds, voff, srd, soff, imm) asm volatile("s_mov_b32 m0, %0\n\ts_nop 0\n\tbuffer_load_dwordx4 %1, %2, %3 offen offset:" #imm " lds" :: "s"(lds), "v"(voff), "s"(srd), "s"(soff) : "memory")
#define DMA16H(lds, voff, srd, soff) asm volatile("s_mov_b32 m0, %0\n\ts_lshr_b64 exec, -1, 32\n\tbuffer_load_dwordx4 %1, %2, %3 offen lds\n\ts_mov_b64 exec, -1" :: "s"(lds), "v"(voff), "s"(srd), "s"(soff) : "memory")
typedef __attribute__((address_space(3))) char lds_char;
#define LDS_RD128(addr) (*(const __attribute__((address_space(3))) bf16x8*)(addr))
#define AMFMA(acc, a, b) asm volatile("v_mfma_f32_16x16x32_bf16 %0, %1, %2, %0" : "+v"(acc) : "v"(a), "v"(b))
#define ADSRD(dst, addr, off) asm volatile("ds_read_b128 %0, %1 offset:%2" : "=v"(dst) : "v"(addr), "i"(off))
#define OPAQUE(x) asm volatile("" : "+v"(x))
#define BLOAD16(dst, voff, srd, soff, imm) asm volatile("buffer_load_dwordx4 %0, %1, %2, %3 offen offset:" #imm : "=v"(dst) : "v"(voff), "s"(srd), "s"(soff))

constexpr int K0_PITCH = 1168;
__device__ __forceinline__ int wc_krow(int ks, int g, int j) {
  int r = j & 3, hi = j >> 2;
  if (ks >= 72) { int u = ks - 72; return 2304 + 4 * (8 * u + 4 * hi + g) + r; }
  int w = ks / 18, pi = (ks % 18) / 9, i = ks % 9, fb0 = 4 * w + 2 * pi;
  if (i == 4) return 144 * (fb0 + hi) + 128 + 4 * g + r;
  if (i < 4) return 144 * fb0 + 32 * i + 16 * hi + 4 * g + r;
  return 144 * (fb0 + 1) + 32 * (i - 5) + 16 * hi + 4 * g + r;
}
__device__ __forceinline__ u32x4 tr_frag(unsigned a0, unsigned a1) {
  u32x2 lo, hi;
  asm volatile("ds_read_b64_tr_b16 %0, %2\n\tds_read_b64_tr_b16 %1, %3\n\ts_waitcnt lgkmcnt(0)" : "=&v"(lo), "=&v"(hi) : "v"(a0), "v"(a1) : "memory");
  return (u32x4){lo[0], lo[1], hi[0], hi[1]};
}
#define KA_SLEEP 32
#define PFA_POL ""
#define SJLD(p) (*(p))
constexpr int KA_SLOT = 10240, KA_ATT = 6 * KA_SLOT, KA_PITCH = 144, KA_LDS = KA_ATT + 142 * KA_PITCH;
template <int FAST> struct KaCfg {
  static constexpr int NZ2 = 4 + FAST, NZ1 = 4 + FAST;
  static constexpr int nt(int s) { return s < 9 ? 4 : 4 + FAST; }
  static constexpr int goff(int s) { return s < 9 ? s * 8192 : 73728 + (s - 9) * (4 + FAST) * 2048; }
  static constexpr int ops(int s) { return s > 26 ? 0 : 2 + (nt(s) == 5 ? 1 : 0) + (s < 9 ? 4 + ((s & 1) ? 0 : 1) : 0); }
  static constexpr int wait(int s) { return s == 20 ? ops(23) + ops(24) : ops(s + 2) + ops(s + 3) + ops(s + 4) + ((s >= 9 && s <= 12) ? 6 : 0); }
};
template <int V> struct IC { static constexpr int v = V; };
#define BLD(dst, voff, srd, soff, IMM) asm volatile("buffer_load_dwordx4 %0, %1, %2, %3 offen offset:%4" : "=v"(dst) : "v"(voff), "s"(srd), "s"(soff), "i"(IMM))
#define AMFMA0(acc, a, b) asm volatile("v_mfma_f32_16x16x32_bf16 %0, %1, %2, 0" : "=&v"(acc) : "v"(a), "v"(b))
typedef __attribute__((ext_vector_type(2))) float f32x2;
typedef __attribute__((ext_vector_type(2))) __bf16 bf2_t;
__device__ __forceinline__ float dot2(unsigned a, unsigned b, float c) { return __builtin_amdgcn_fdot2_f32_bf16(__builtin_bit_cast(bf2_t, a), __builtin_bit_cast(bf2_t, b), c, false); }
__device__ __forceinline__ f32x2 gelu2(f32x2 x) {
  f32x2 t = x * x, p = t * (-0.10294324f) + (-2.3022082f), e = x * p;
  f32x2 d = {__builtin_amdgcn_exp2f(e[0]), __builtin_amdgcn_exp2f(e[1])};
  d += 1.f;
  f32x2 r = {__builtin_amdgcn_rcpf(d[0]), __builtin_amdgcn_rcpf(d[1])};
  return x * r;
}
template <int CTRL> __device__ __forceinline__ float dpp_add(float v) {
  return v + __builtin_bit_cast(float, __builtin_amdgcn_update_dpp(0, __builtin_bit_cast(int, v), CTRL, 0xf, 0xf, true));
}

__device__ __forceinline__ float xmax16(float v) { auto r = __builtin_amdgcn_permlane16_swap(__float_as_uint(v), __float_as_uint(v), false, false); return fmaxf(__uint_as_float(r[0]), __uint_as_float(r[1])); }
__device__ __forceinline__ float xmax32(float v) { auto r = __builtin_amdgcn_permlane32_swap(__float_as_uint(v), __float_as_uint(v), false, false); return fmaxf(__uint_as_float(r[0]), __uint_as_float(r[1])); }
__device__ __forceinline__ float xadd16(float v) { auto r = __builtin_amdgcn_permlane16_swap(__float_as_uint(v), __float_as_uint(v), false, false); return __uint_as_float(r[0]) + __uint_as_float(r[1]); }
__device__ __forceinline__ float xadd32(float v) { auto r = __builtin_amdgcn_permlane32_swap(__float_as_uint(v), __float_as_uint(v), false, false); return __uint_as_float(r[0]) + __uint_as_float(r[1]); }
#define PRIO_HI()
#define PRIO_LO()
#define LBAR() asm volatile("s_waitcnt lgkmcnt(0)\n\ts_barrier" ::: "memory")
__device__ __forceinline__ float swap1(float v) { return __builtin_bit_cast(float, __builtin_amdgcn_update_dpp(0, __builtin_bit_cast(int, v), 0xB1, 0xf, 0xf, true)); }
template <int FAST>
__device__ __forceinline__ void ka_body(char* smem, const char* __restrict__ ws_c, char* __restrict__ ws, const int p, const int cg, const int sj, const u16* __restrict__ wsp, const u16* __restrict__ wout, const u16* __restrict__ wo) {
  typedef KaCfg<FAST> C;
  const int tid = threadIdx.x, lane = tid & 63, li = lane & 15, lg = lane >> 4;
  const int w = RFL(tid >> 6), fb = cg >> 1;
  const rsrc_t rW = mk_rsrc(ws_c + OFF_W1 + (size_t)fb * (468 * 1024) + (FAST ? 0 : 252 * 1024), (FAST ? 252 : 216) * 1024);
  const rsrc_t rX = mk_rsrc(ws_c + OFF_XT, (int)SZ_XT);
  const unsigned ldsb = (unsigned)(size_t)(__attribute__((address_space(3))) char*)smem;
  const unsigned vl16 = lane * 16;
  const unsigned vX = vl16 + (unsigned)(p * 8 + w * 2) * 18432u;
  const unsigned vH = p == 0 ? 0x80000000u : (unsigned)(p * 8 - 2 + (li >> 3)) * 18432u + (unsigned)(lg * 16 + (li | 8)) * 16u + (unsigned)w * 1024u;
  const unsigned sF = RFL((unsigned)w * 2048u), sH = RFL(8192u + (unsigned)w * 512u);
  const unsigned rd = ldsb + vl16;
  constexpr int KA_WARM = 0;
  bf16x8 X[2][18], HX[5], F[5];
  f32x4 acc[2][5], hacc[3];
#pragma unroll
  for (int n = 0; n < 3; ++n) hacc[n] = (f32x4){0.f, 0.f, 0.f, 0.f};

  bf16x8 (&X_)[2][18] = X; bf16x8 (&HX_)[5] = HX; bf16x8 (&F_)[5] = F; f32x4 (&acc_)[2][5] = acc; f32x4 (&hacc_)[3] = hacc;
  const rsrc_t rW_ = rW, rX_ = rX; const unsigned vl16_ = vl16, vX_ = vX, vH_ = vH, rd_ = rd;
#define KA_CAP bf16x8 (&X)[2][18] = X_; bf16x8 (&HX)[5] = HX_; bf16x8 (&F)[5] = F_; f32x4 (&acc)[2][5] = acc_; f32x4 (&hacc)[3] = hacc_; \
    const rsrc_t rW = rW_, rX = rX_; const unsigned vl16 = vl16_, vX = vX_, vH = vH_, rd = rd_; (void)X; (void)HX; (void)F; (void)acc; (void)hacc; (void)rW; (void)rX; (void)vl16; (void)vX; (void)vH; (void)rd;
  auto vop = [&](auto S_, auto K_) {
    KA_CAP
    constexpr int s = decltype(S_)::v, k = decltype(K_)::v;
    if constexpr (s <= 26) {
      constexpr int slot = s % 6, nd = 2 + (C::nt(s) == 5 ? 1 : 0);
      if constexpr (k == 0) { unsigned lo = ldsb + slot * KA_SLOT + sF, so = C::goff(s) + sF; DMA16(lo, vl16, rW, so, 0); }
      else if constexpr (k == 1) { unsigned lo = ldsb + slot * KA_SLOT + sF, so = C::goff(s) + sF; DMA16(lo, vl16, rW, so, 1024); }
      else if constexpr (k == 2 && nd == 3) { unsigned lo = ldsb + slot * KA_SLOT + sH, so = C::goff(s) + sH; DMA16H(lo, vl16, rW, so); }
      else if constexpr (s < 9 && k >= 2 && k < 6) {
        constexpr int m = (k - 2) & 1, ks = 2 * s + ((k - 2) >> 1);
        BLD(X[m][ks], vX, rX, m * 18432 + (ks & ~3) * 1024, (ks & 3) * 1024);
      } else if constexpr (s < 9 && k == 6 && !(s & 1)) { BLD(HX[s >> 1], vH, rX, (s >> 1) * 4096, 0); }
    }
  };
  auto group = [&](auto S_) { vop(S_, IC<0>{}); vop(S_, IC<1>{}); vop(S_, IC<2>{}); vop(S_, IC<3>{}); vop(S_, IC<4>{}); vop(S_, IC<5>{}); vop(S_, IC<6>{}); };
  auto sync = [&](auto S_) {
    KA_CAP
    constexpr int s = decltype(S_)::v;
    if constexpr (s < 26) {
      if constexpr (s + 1 < 9) {
        constexpr int k0 = 2 * (s + 1);
        asm volatile("s_waitcnt vmcnt(%5)" : "+v"(X[0][k0]), "+v"(X[1][k0]), "+v"(X[0][k0 + 1]), "+v"(X[1][k0 + 1]), "+v"(HX[(s + 1) >> 1]) : "i"(C::wait(s)) : "memory");
      } else asm volatile("s_waitcnt vmcnt(%0)" :: "i"(C::wait(s)) : "memory");
      __builtin_amdgcn_s_barrier();
    }
  };
  auto step = [&](auto S_, auto H_) {
    KA_CAP
    constexpr int s = decltype(S_)::v, h = decltype(H_)::v;
    constexpr int NT = C::nt(s), job = s / 9, ks = 2 * (s % 9) + h;
    constexpr int s2 = h ? s + 1 : s, h2 = h ^ 1, NT2 = s2 <= 26 ? C::nt(s2) : 0;
    constexpr int roff = (s2 % 6) * KA_SLOT + h2 * NT2 * 1024;
    if constexpr (ks == 0) asm volatile("s_waitcnt lgkmcnt(0)" ::: "memory");
    if constexpr (job == 0) {
      if (w == (ks & 3)) {
        asm volatile("s_waitcnt lgkmcnt(1)" ::: "memory");
#pragma unroll
        for (int n = 0; n < 3; ++n) AMFMA(hacc[n], F[n], HX[ks >> 2]);
      }
    }
#pragma unroll
    for (int n = 0; n < NT; ++n) {
      if constexpr (ks != 0) asm volatile("s_waitcnt lgkmcnt(%0)" :: "i"(NT - 1) : "memory");
      if constexpr (job == 1) {
        if constexpr (ks == 0) { AMFMA0(acc[0][n], X[0][ks], F[n]); AMFMA0(acc[1][n], X[1][ks], F[n]); }
        else { AMFMA(acc[0][n], X[0][ks], F[n]); AMFMA(acc[1][n], X[1][ks], F[n]); }
      } else {
        if constexpr (ks == 0) { AMFMA0(acc[0][n], F[n], X[0][ks]); AMFMA0(acc[1][n], F[n], X[1][ks]); }
        else { AMFMA(acc[0][n], F[n], X[0][ks]); AMFMA(acc[1][n], F[n], X[1][ks]); }
      }
      if (n < NT2) ADSRD(F[n], rd, roff + n * 1024);
      if constexpr (h == 1) {
        if (n == 0) { vop(IC<s + 5>{}, IC<0>{}); vop(IC<s + 5>{}, IC<2>{}); }
        if (n == 1) { vop(IC<s + 5>{}, IC<1>{}); vop(IC<s + 5>{}, IC<3>{}); }
        if (n == 2) { vop(IC<s + 5>{}, IC<4>{}); vop(IC<s + 5>{}, IC<5>{}); }
        if (n == 3) { vop(IC<s + 5>{}, IC<6>{}); }
      }
    }
    if constexpr (NT2 > NT) ADSRD(F[4], rd, roff + 4 * 1024);
  };
#define KA_ACC_DONE() do { asm volatile("s_nop 15" : "+v"(acc[0][0]), "+v"(acc[0][1]), "+v"(acc[0][2]), "+v"(acc[0][3]), "+v"(acc[1][0]), "+v"(acc[1][1]), "+v"(acc[1][2]), "+v"(acc[1][3]) :: "memory"); \
    if constexpr (FAST) asm volatile("" : "+v"(acc[0][4]), "+v"(acc[1][4]) :: "memory"); } while (0)
  auto iter = [&](auto S_) { step(S_, IC<0>{}); sync(S_); step(S_, IC<1>{}); };

  group(IC<0>{}); group(IC<1>{});
  asm volatile("s_waitcnt vmcnt(%5)" : "+v"(X[0][0]), "+v"(X[1][0]), "+v"(X[0][1]), "+v"(X[1][1]), "+v"(HX[0]) : "i"(C::ops(1) + KA_WARM) : "memory");
  __builtin_amdgcn_s_barrier();
#pragma unroll
  for (int n = 0; n < 4; ++n) ADSRD(F[n], rd, n * 1024);

  step(IC<0>{}, IC<0>{}); group(IC<2>{}); group(IC<3>{}); group(IC<4>{}); sync(IC<0>{}); step(IC<0>{}, IC<1>{});
  iter(IC<1>{}); iter(IC<2>{}); iter(IC<3>{}); iter(IC<4>{}); iter(IC<5>{}); iter(IC<6>{}); iter(IC<7>{}); iter(IC<8>{});
  asm volatile("s_nop 15" : "+v"(acc[0][0]), "+v"(acc[0][1]), "+v"(acc[0][2]), "+v"(acc[0][3]), "+v"(acc[1][0]), "+v"(acc[1][1]), "+v"(acc[1][2]), "+v"(acc[1][3]), "+v"(hacc[0]), "+v"(hacc[1]), "+v"(hacc[2]) :: "memory");

  SCHED();
  {
    char* att = smem + KA_ATT;
    char* hp = smem + 2 * KA_SLOT;
#pragma unroll
    for (int n = 0; n < 3; ++n) if (n != w) *(f32x4*)(hp + (n * 3 + (w > n ? w - 1 : w)) * 1024 + vl16) = hacc[n];
#pragma unroll
    for (int m = 0; m < 2; ++m) {
      char* rp = att + (14 + w * 32 + 8 * (li >> 2) + 4 * m + (li & 3)) * KA_PITCH + lg * 8;
#pragma unroll
      for (int n = 0; n < 4; ++n) *(u32x2*)(rp + n * 32) = (u32x2){pk_bf16(acc[m][n][0], acc[m][n][1]), pk_bf16(acc[m][n][2], acc[m][n][3])};
    }
    LBAR();
    if (w < 3) {
      f32x4 a = w == 0 ? hacc[0] : (w == 1 ? hacc[1] : hacc[2]);
#pragma unroll
      for (int q = 0; q < 3; ++q) a += *(const f32x4*)(hp + (w * 3 + q) * 1024 + vl16);
      int ho = (li < 8) ? 8 * (li >> 2) + (li & 3) : 8 * ((li - 8) >> 2) + 4 + (li & 3);
      if (ho >= 2) *(u32x2*)(att + (ho - 2) * KA_PITCH + w * 32 + lg * 8) = (u32x2){pk_bf16(a[0], a[1]), pk_bf16(a[2], a[3])};
    }
    LBAR();
    const unsigned attb = ldsb + KA_ATT;
    float mbias[8];
#pragma unroll
    for (int e = 0; e < 8; ++e) { int d = (e >> 2) * 16 + lg * 4 + (e & 3) - li; mbias[e] = (!(d & 1) && d >= 2 && d <= 16) ? 0.f : -__builtin_inff(); }
    bf16x8 Q[2], K[2][2]; u32x2 V[2][2][2];
#pragma unroll
    for (int jj = 0; jj < 2; ++jj) {
      const int j = w * 2 + jj;
      Q[jj] = *(const bf16x8*)(att + (14 + 16 * j + li) * KA_PITCH + 80 + (lg < 3 ? lg : 2) * 16);
#pragma unroll
      for (int kt = 0; kt < 2; ++kt) {
        int R = 16 * j - 2 + 16 * kt + li; R = R < 0 ? 0 : R;
        K[jj][kt] = *(const bf16x8*)(att + R * KA_PITCH + lg * 16);
        int Rv = 16 * j - 2 + 16 * kt + 4 * lg + (li >> 2); Rv = Rv < 0 ? 0 : Rv;
        const unsigned va = attb + Rv * KA_PITCH + 40 + (li & 3) * 8;
        asm volatile("ds_read_b64_tr_b16 %0, %2\n\tds_read_b64_tr_b16 %1, %2 offset:32" : "=&v"(V[jj][0][kt]), "=&v"(V[jj][1][kt]) : "v"(va) : "memory");
      }
    }
    f32x4 S[2][2];
#pragma unroll
    for (int jj = 0; jj < 2; ++jj) {
      if (lg == 3) Q[jj] = (bf16x8){0, 0, 0, 0, 0, 0, 0, 0};
#pragma unroll
      for (int kt = 0; kt < 2; ++kt) S[jj][kt] = MFMA(K[jj][kt], Q[jj], ((f32x4){0.f, 0.f, 0.f, 0.f}));
    }
    bf16x8 P[2];
#pragma unroll
    for (int jj = 0; jj < 2; ++jj) {
      float l[8];
#pragma unroll
      for (int e = 0; e < 8; ++e) l[e] = S[jj][e >> 2][e & 3] + mbias[e];
      if (p == 0 && w == 0 && jj == 0) {
#pragma unroll
        for (int e = 0; e < 4; ++e) l[e] = -__builtin_inff();
      }
      float mx = fmaxf(fmaxf(fmaxf(l[0], l[1]), fmaxf(l[2], l[3])), fmaxf(fmaxf(l[4], l[5]), fmaxf(l[6], l[7])));
      mx = xmax16(mx); mx = xmax32(mx);
      constexpr float CE = 0.23570226f * 1.44269504f;
      const float nm = -mx * CE;
      float sum = 0.f;
#pragma unroll
      for (int e = 0; e < 8; ++e) { l[e] = __builtin_amdgcn_exp2f(l[e] * CE + nm); sum += l[e]; }
      sum = xadd16(sum); sum = xadd32(sum);
      const float inv = __builtin_amdgcn_rcpf(sum);
      u32x4 pk = {pk_bf16(l[0] * inv, l[1] * inv), pk_bf16(l[2] * inv, l[3] * inv), pk_bf16(l[4] * inv, l[5] * inv), pk_bf16(l[6] * inv, l[7] * inv)};
      P[jj] = __builtin_bit_cast(bf16x8, pk);
    }
    asm volatile("s_waitcnt lgkmcnt(0)" : "+v"(V[0][0][0]), "+v"(V[0][0][1]), "+v"(V[0][1][0]), "+v"(V[0][1][1]), "+v"(V[1][0][0]), "+v"(V[1][0][1]), "+v"(V[1][1][0]), "+v"(V[1][1][1]) :: "memory");
#pragma unroll
    for (int jj = 0; jj < 2; ++jj) {
      f32x4 O[2];
#pragma unroll
      for (int dt = 0; dt < 2; ++dt) {
        u32x4 vv = {V[jj][dt][0][0], V[jj][dt][0][1], V[jj][dt][1][0], V[jj][dt][1][1]};
        O[dt] = MFMA(__builtin_bit_cast(bf16x8, vv), P[jj], ((f32x4){0.f, 0.f, 0.f, 0.f}));
      }
      const int tl = jj * 16 + li, tt = p * 8 + w * 2 + ((tl >> 2) & 1), slot = (tl >> 3) * 4 + (tl & 3);
      char* ob = ws + OFF_OT + (size_t)tt * 18432 + slot * 8;
      const int d0 = cg * 18 + lg * 4, d1 = d0 + 2, d2 = cg * 18 + 16;
      st4(ob + (d0 >> 2) * 128 + (d0 & 3) * 2, pk_bf16(O[0][0], O[0][1]));
      st4(ob + (d1 >> 2) * 128 + (d1 & 3) * 2, pk_bf16(O[0][2], O[0][3]));
      if (lg == 0) st4(ob + (d2 >> 2) * 128 + (d2 & 3) * 2, pk_bf16(O[1][0], O[1][1]));
    }
  }
  SCHED();
  PRIO_LO();

  iter(IC<9>{}); iter(IC<10>{}); iter(IC<11>{}); iter(IC<12>{}); iter(IC<13>{}); iter(IC<14>{}); iter(IC<15>{}); iter(IC<16>{}); iter(IC<17>{});
  KA_ACC_DONE();

  SCHED();
  if constexpr (FAST) {
    const unsigned dump = RFL(ldsb + KA_ATT + 2048 + (unsigned)w * 256u);
    {
      const rsrc_t rS = mk_rsrc(wsp, 2048 * 4096);
      const int ks = (sj * 4 + w) & 63, tp = (sj * 4 + w) >> 6, r = (w & 1) * 64 + lane;
      const unsigned vo = (unsigned)prow(tp * 8 + (r >> 4), r & 15) * 4096u + (unsigned)(ks >> 1) * 128u;
      asm volatile("s_mov_b32 m0, %0\n\ts_nop 0\n\tbuffer_load_dword %1, %2, 0 offen" PFA_POL " lds" :: "s"(dump), "v"(vo), "s"(rS) : "memory");
    }
    if (sj < 90) {
      const int base0 = wc_krow(sj, 0, 0), base1 = wc_krow(sj, 0, 4);
      const rsrc_t rS = sj < 72 ? mk_rsrc(wout, 2304 * 1152) : mk_rsrc(wo - (size_t)2304 * 576, 2880 * 1152);
      const int c = tid, r = c / 9, l = c - r * 9;
      const unsigned vo = (unsigned)((r < 16 ? base0 : base1 - 16) + r) * 1152u + (unsigned)l * 128u;
      asm volatile("s_mov_b32 m0, %0\n\ts_nop 0\n\tbuffer_load_dword %1, %2, 0 offen" PFA_POL " lds" :: "s"(dump), "v"(vo), "s"(rS) : "memory");
      if (w == 0) {
        const int c2 = 256 + (lane & 31), r2 = c2 / 9, l2 = c2 - r2 * 9;
        const unsigned vo2 = (unsigned)(base1 - 16 + r2) * 1152u + (unsigned)l2 * 128u;
        asm volatile("s_mov_b32 m0, %0\n\ts_nop 0\n\tbuffer_load_dword %1, %2, 0 offen" PFA_POL " lds" :: "s"(dump), "v"(vo2), "s"(rS) : "memory");
      }
    }
  }
  SCHED();
  PRIO_HI();
  unsigned zs[C::NZ2][4];
  {
#pragma unroll
    for (int n = 0; n < C::NZ2; ++n) {
#pragma unroll
      for (int m = 0; m < 2; ++m) {
        f32x2 a = gelu2((f32x2){acc[m][n][0], acc[m][n][1]}), b = gelu2((f32x2){acc[m][n][2], acc[m][n][3]});
        zs[n][2 * m] = pk_bf16(a[0], a[1]); zs[n][2 * m + 1] = pk_bf16(b[0], b[1]);
      }
    }
    f32x2 s1[4], s2[4];
#pragma unroll
    for (int j = 0; j < 4; ++j) {
      s1[j] = (f32x2){0.f, 0.f}; s2[j] = s1[j];
#pragma unroll
      for (int n = 0; n < C::NZ2; ++n) { f32x2 v = {bf_lo(zs[n][j]), bf_hi(zs[n][j])}; s1[j] += v; s2[j] += v * v; }
    }
    float r[16];
#pragma unroll
    for (int j = 0; j < 4; ++j) { r[2 * j] = s1[j][0]; r[2 * j + 1] = s1[j][1]; r[8 + 2 * j] = s2[j][0]; r[9 + 2 * j] = s2[j][1]; }
    float sel;
    {
      float a[8], c[4];
#pragma unroll
      for (int j = 0; j < 8; j += 4)
        asm volatile("s_nop 1\n\t"
          "v_add_f32_dpp %0, %4, %4 row_ror:8 row_mask:0xf bank_mask:0x3\n\tv_add_f32_dpp %1, %5, %5 row_ror:8 row_mask:0xf bank_mask:0x3\n\t"
          "v_add_f32_dpp %2, %6, %6 row_ror:8 row_mask:0xf bank_mask:0x3\n\tv_add_f32_dpp %3, %7, %7 row_ror:8 row_mask:0xf bank_mask:0x3\n\t"
          "v_add_f32_dpp %0, %8, %8 row_ror:8 row_mask:0xf bank_mask:0xc\n\tv_add_f32_dpp %1, %9, %9 row_ror:8 row_mask:0xf bank_mask:0xc\n\t"
          "v_add_f32_dpp %2, %10, %10 row_ror:8 row_mask:0xf bank_mask:0xc\n\tv_add_f32_dpp %3, %11, %11 row_ror:8 row_mask:0xf bank_mask:0xc"
          : "=&v"(a[j]), "=&v"(a[j + 1]), "=&v"(a[j + 2]), "=&v"(a[j + 3])
          : "v"(r[j]), "v"(r[j + 1]), "v"(r[j + 2]), "v"(r[j + 3]), "v"(r[j + 8]), "v"(r[j + 9]), "v"(r[j + 10]), "v"(r[j + 11]));
      asm volatile("s_nop 1\n\t"
          "v_add_f32_dpp %0, %4, %4 row_half_mirror row_mask:0xf bank_mask:0x5\n\tv_add_f32_dpp %1, %5, %5 row_half_mirror row_mask:0xf bank_mask:0x5\n\t"
          "v_add_f32_dpp %2, %6, %6 row_half_mirror row_mask:0xf bank_mask:0x5\n\tv_add_f32_dpp %3, %7, %7 row_half_mirror row_mask:0xf bank_mask:0x5\n\t"
          "v_add_f32_dpp %0, %8, %8 row_half_mirror row_mask:0xf bank_mask:0xa\n\tv_add_f32_dpp %1, %9, %9 row_half_mirror row_mask:0xf bank_mask:0xa\n\t"
          "v_add_f32_dpp %2, %10, %10 row_half_mirror row_mask:0xf bank_mask:0xa\n\tv_add_f32_dpp %3, %11, %11 row_half_mirror row_mask:0xf bank_mask:0xa\n\ts_nop 1"
          : "=&v"(c[0]), "=&v"(c[1]), "=&v"(c[2]), "=&v"(c[3])
          : "v"(a[0]), "v"(a[1]), "v"(a[2]), "v"(a[3]), "v"(a[4]), "v"(a[5]), "v"(a[6]), "v"(a[7]));
#pragma unroll
      for (int j = 0; j < 4; ++j) c[j] = dpp_add<0x4E>(c[j]);
      float d0 = (li & 2) ? c[2] : c[0], d1 = (li & 2) ? c[3] : c[1];
      d0 = dpp_add<0xB1>(d0); d1 = dpp_add<0xB1>(d1);
      sel = (li & 1) ? d1 : d0;
    }
    double* dst = (double*)(ws + OFF_RS) + (size_t)(p * 128 + w * 32 + lg * 8 + (li & 7)) * 2 + (li >> 3);
    double dv = (double)sel;
    asm volatile("global_atomic_add_f64 %0, %1, off sc1" :: "v"(dst), "v"(dv) : "memory");
  }
  SCHED();

  PRIO_LO();

  unsigned* cnt = (unsigned*)(ws + OFF_CNT + p * 256);
  iter(IC<18>{}); iter(IC<19>{});
  step(IC<20>{}, IC<0>{}); sync(IC<20>{});
  if (tid == 0) { unsigned one = 1; asm volatile("global_atomic_add %0, %1, off sc1" :: "v"(cnt), "v"(one) : "memory"); }
  step(IC<20>{}, IC<1>{});
  iter(IC<21>{}); iter(IC<22>{}); iter(IC<23>{}); iter(IC<24>{}); iter(IC<25>{});
  unsigned seen;
  asm volatile("global_load_dword %0, %1, off sc1" : "=v"(seen) : "v"(cnt) : "memory");
  iter(IC<26>{});
  KA_ACC_DONE();
  asm volatile("s_waitcnt vmcnt(0)" : "+v"(seen) :: "memory");
  PRIO_HI();
  const unsigned seen_s = RFL(seen);
  const bool early = seen_s >= 32u;
  const char* sp = ws + OFF_RS + (size_t)(p * 128 + w * 32 + (lane & 31)) * 16;
  u32x4 st = {0, 0, 0, 0};
  asm volatile("s_cmp_lt_u32 %2, 32\n\ts_cbranch_scc1 1f\n\tglobal_load_dwordx4 %0, %1, off sc1\n1:" : "+v"(st) : "v"(sp), "s"(seen_s) : "memory", "scc");
  SCHED();
  u32x4 wt[8];
  if constexpr (FAST) {
    const int ks = (sj * 4 + w) & 63, tp = (sj * 4 + w) >> 6;
    const rsrc_t rS = mk_rsrc(wsp, 2048 * 4096);
    const unsigned vo = (unsigned)(8 * (li >> 2) + (li & 3)) * 4096u + (unsigned)lg * 16u, so = (unsigned)tp * (128u * 4096u) + (unsigned)ks * 64u;
#pragma unroll
    for (int m = 0; m < 8; ++m) wt[m] = __builtin_amdgcn_raw_buffer_load_b128(rS, vo, so + (unsigned)(32 * (m >> 1) + 4 * (m & 1)) * 4096u, 0);
  }
#pragma unroll
  for (int m = 0; m < 2; ++m) {
    const char* zp = ws + OFF_Z1T + ((size_t)(p * 8 + w * 2 + m) * 16 + fb) * 4608;
    unsigned pk[C::NZ1][2];
#pragma unroll
    for (int n = 0; n < C::NZ1; ++n) {
      f32x2 a = gelu2((f32x2){acc[m][n][0], acc[m][n][1]}), b = gelu2((f32x2){acc[m][n][2], acc[m][n][3]});
      pk[n][0] = pk_bf16(a[0], a[1]); pk[n][1] = pk_bf16(b[0], b[1]);
    }
    st16s<FAST * 2048>(zp, vl16, (u32x4){pk[0][0], pk[0][1], pk[1][0], pk[1][1]});
    st16s<FAST * 2048 + 1024>(zp, vl16, (u32x4){pk[2][0], pk[2][1], pk[3][0], pk[3][1]});
    if constexpr (FAST) st8((char*)zp + 4096 + lane * 8, (u32x2){pk[4][0], pk[4][1]});
  }
  SCHED();
  if constexpr (FAST) {
    const char* wb = ws + OFF_WT + (size_t)(sj * 4 + w) * 8192;
    st16s<0>(wb, vl16, wt[0]); st16s<1024>(wb, vl16, wt[1]); st16s<2048>(wb, vl16, wt[2]); st16s<3072>(wb, vl16, wt[3]);
    st16s<0>(wb + 4096, vl16, wt[4]); st16s<1024>(wb + 4096, vl16, wt[5]); st16s<2048>(wb + 4096, vl16, wt[6]); st16s<3072>(wb + 4096, vl16, wt[7]);
    if (sj < 90) {
      const int ks = sj, base0 = wc_krow(ks, 0, 0), base1 = wc_krow(ks, 0, 4);
      const rsrc_t rS = sj < 72 ? mk_rsrc(wout, 2304 * 1152) : mk_rsrc(wo - (size_t)2304 * 576, 2880 * 1152);
      const int r = tid >> 3;
      const unsigned vo = (unsigned)((r < 16 ? base0 : base1 - 16) + r) * 1152u + (unsigned)(tid & 7) * 16u;
      u32x4 v[9];
#pragma unroll
      for (int k = 0; k < 9; ++k) v[k] = __builtin_amdgcn_raw_buffer_load_b128(rS, vo, k * 128, 0);
      LBAR();
      char* dl = smem + r * K0_PITCH + (tid & 7) * 16;
#pragma unroll
      for (int k = 0; k < 9; ++k) *(u32x4*)(dl + k * 128) = v[k];
      LBAR();
      const unsigned a0 = ldsb + (4 * lg + (li >> 2)) * K0_PITCH + (li & 3) * 8;
#pragma unroll
      for (int e = 0; e < 10; ++e) {
        int f = w * 10 + e, ct = f / 5, n = f - ct * 5, d0 = (ct < 7 ? ct * 72 : 496) + n * 16;
        u32x4 o = tr_frag(a0 + d0 * 2, a0 + d0 * 2 + 16 * K0_PITCH);
        st16s<0>(ws + OFF_WC + (size_t)((ct * 90 + ks) * 5 + n) * 1024, vl16, o);
      }
    }
  }
  if (!early) {
    while (RFL(seen) < 32u) {
      __builtin_amdgcn_s_sleep(KA_SLEEP);
      asm volatile("global_load_dword %0, %1, off sc1\n\ts_waitcnt vmcnt(0)" : "=v"(seen) : "v"(cnt) : "memory");
    }
  }
  asm volatile("s_cmp_ge_u32 %2, 32\n\ts_cbranch_scc1 1f\n\tglobal_load_dwordx4 %0, %1, off sc1\n1:\n\ts_waitcnt vmcnt(0)" : "+v"(st) : "v"(sp), "s"(seen_s) : "memory", "scc");
  {
    const double sm = __builtin_bit_cast(double, (u32x2){st[0], st[1]}), sq = __builtin_bit_cast(double, (u32x2){st[2], st[3]});
    const double mean = sm * (1.0 / 2304.0);
    const float var = fmaxf((float)(sq * (1.0 / 2304.0) - mean * mean), 0.f);
    const float mb = rbf((float)mean), rs = 1.f / rbf(sqrtf(rbf(rbf(var) + 1e-5f)));
    f32x2* sl = (f32x2*)(smem + KA_ATT + w * 256);
    if (lane < 32) sl[lane] = (f32x2){mb, rs};
    __builtin_amdgcn_s_waitcnt(0xC07F);
    __builtin_amdgcn_wave_barrier();
    f32x4 ms[4];
#pragma unroll
    for (int j = 0; j < 4; ++j) ms[j] = *(const f32x4*)((const char*)sl + lg * 64 + j * 16);
    const char* zp = ws + OFF_Z2T + ((size_t)(fb * 64 + p * 4 + w) * 9 + (FAST ? 0 : 5)) * 1024;
    auto z2o = [&](int n) { u32x4 o;
#pragma unroll
      for (int j = 0; j < 4; ++j) o[j] = pk_bf16((bf_lo(zs[n][j]) - ms[j][0]) * ms[j][1], (bf_hi(zs[n][j]) - ms[j][2]) * ms[j][3]);
      return o; };
    st16s<0>(zp, vl16, z2o(0)); st16s<1024>(zp, vl16, z2o(1)); st16s<2048>(zp, vl16, z2o(2)); st16s<3072>(zp, vl16, z2o(3));
    if constexpr (FAST) st16s<0>(zp + 4096, vl16, z2o(4));
  }
}
__global__ __launch_bounds__(256, 2) void kA(const char* __restrict__ ws_c, char* __restrict__ ws, const u16* __restrict__ wsp, const u16* __restrict__ wout, const u16* __restrict__ wo) {
  extern __shared__ __attribute__((aligned(16))) char smem[];
  const int b = blockIdx.x, xcd = b & 7, idx = b >> 3;
  const int p = (xcd & 1) * 8 + (idx & 7), q = idx >> 3, cg = (xcd >> 1) * 8 + (q & 3) * 2 + (q >> 2);
  if (idx < 32) ka_body<1>(smem, ws_c, ws, p, cg, b, wsp, wout, wo); else ka_body<0>(smem, ws_c, ws, p, cg, b, wsp, wout, wo);
}

constexpr int KB_STAGE = 36864;
constexpr int KB_LDS = 4 * KB_STAGE;
__global__ __launch_bounds__(512, 2) void kB(const char* __restrict__ ws_c, char* __restrict__ ws, const u16* __restrict__ bsp) {
  extern __shared__ __attribute__((aligned(16))) char smem[];
  const int tid = threadIdx.x, lane = tid & 63;
  const int w = RFL(tid >> 6), wm = w & 3, kh = w >> 2;
  const int b = blockIdx.x, xcd = b & 7, idx = b >> 3;
  const int tp = (xcd & 1) * 8 + (idx & 7), fb = (xcd >> 1) * 4 + (idx >> 3);
  const rsrc_t rZ = mk_rsrc(ws_c + OFF_Z2T + (size_t)fb * (64 * 9216), 64 * 9216);
  const rsrc_t rW = mk_rsrc(ws_c + OFF_WT + (size_t)tp * (64 * 8192), 64 * 8192);
  const unsigned ldsb = (unsigned)(size_t)(__attribute__((address_space(3))) char*)smem;
  const unsigned vl16 = lane * 16;
  const unsigned vW = vl16 + wm * 2048 + kh * 16384;
  const unsigned dmaF = w * 4096, dmaH = 32768 + w * 512;
  unsigned rd01 = ldsb + kh * 18432 + lane * 16;
  unsigned rd23 = rd01 + 2 * KB_STAGE;

  f32x4 acc[2][9];
#pragma unroll
  for (int m = 0; m < 2; ++m)
#pragma unroll
    for (int n = 0; n < 9; ++n) acc[m][n] = (f32x4){0.f, 0.f, 0.f, 0.f};
  bf16x8 Wr[4][4];
  bf16x8 ZA[9], ZB[9];

#define KB_DMA1(stage, slot, imm) do { \
    unsigned so_ = RFL((unsigned)(stage) * KB_STAGE + dmaF), lo_ = RFL(ldsb + (slot) * KB_STAGE + dmaF); DMA16(lo_, vl16, rZ, so_, imm); } while (0)
#define KB_DMAH(stage, slot) do { \
    unsigned so2_ = RFL((unsigned)(stage) * KB_STAGE + dmaH), lo2_ = RFL(ldsb + (slot) * KB_STAGE + dmaH); DMA16H(lo2_, vl16, rZ, so2_); } while (0)
#define KB_W1(stage, slot, e) do { unsigned sw_ = RFL((unsigned)(stage) * 32768u + ((e) >> 1) * 8192u); \
    if ((e) & 1) BLOAD16(Wr[slot][e], vW, rW, sw_, 1024); else BLOAD16(Wr[slot][e], vW, rW, sw_, 0); } while (0)
#define KB_ALL(stage, slot) do { KB_DMA1(stage, slot, 0); KB_DMA1(stage, slot, 1024); KB_DMA1(stage, slot, 2048); KB_DMA1(stage, slot, 3072); KB_DMAH(stage, slot); \
    KB_W1(stage, slot, 0); KB_W1(stage, slot, 1); KB_W1(stage, slot, 2); KB_W1(stage, slot, 3); } while (0)
#define KB_WAIT(slot, N) asm volatile("s_waitcnt vmcnt(" #N ") lgkmcnt(0)" : "+v"(Wr[slot][0]), "+v"(Wr[slot][1]), "+v"(Wr[slot][2]), "+v"(Wr[slot][3]) :: "memory")

  KB_ALL(0, 0);
  __builtin_amdgcn_s_barrier();
  KB_ALL(1, 1);
  KB_WAIT(0, 9);
  __builtin_amdgcn_s_barrier();

  const rsrc_t rZ1 = mk_rsrc(ws_c + OFF_Z1T + ((size_t)(tp * 8 + wm * 2) * 16 + fb) * 4608, 17 * 4608);
  const rsrc_t rB = mk_rsrc(bsp, 4096);
  u32x4 z1q[2][2]; u32x2 z1s[2]; unsigned biasu[2];
  const unsigned vz1 = lane * 16 + kh * 2048, vz8 = lane * 8 + 4096;
  const unsigned vb = (tp * 128 + wm * 32 + ((lane & 15) >> 2) * 8 + (lane & 3)) * 2;
#define KB_EPI_LD(e) do { \
    if ((e) == 0) BLOAD16(z1q[0][0], vz1, rZ1, 0, 0); \
    if ((e) == 1) BLOAD16(z1q[0][1], vz1, rZ1, 0, 1024); \
    if ((e) == 2) asm volatile("buffer_load_dwordx4 %0, %1, %2, %3 offen" : "=v"(z1q[1][0]) : "v"(vz1), "s"(rZ1), "s"(16 * 4608)); \
    if ((e) == 3) asm volatile("buffer_load_dwordx4 %0, %1, %2, %3 offen offset:1024" : "=v"(z1q[1][1]) : "v"(vz1), "s"(rZ1), "s"(16 * 4608)); \
    if ((e) == 4) asm volatile("buffer_load_dwordx2 %0, %1, %2, 0 offen" : "=v"(z1s[0]) : "v"(vz8), "s"(rZ1)); \
    if ((e) == 5) asm volatile("buffer_load_dwordx2 %0, %1, %2, %3 offen" : "=v"(z1s[1]) : "v"(vz8), "s"(rZ1), "s"(16 * 4608)); \
    if ((e) == 6) asm volatile("buffer_load_ushort %0, %1, %2, 0 offen" : "=v"(biasu[0]) : "v"(vb), "s"(rB)); \
    if ((e) == 7) asm volatile("buffer_load_ushort %0, %1, %2, 0 offen offset:8" : "=v"(biasu[1]) : "v"(vb), "s"(rB)); } while (0)

#define KB_ITER(i, j, MODE, WN) do { \
    _Pragma("unroll") for (int n = 0; n < 9; ++n) { \
      ADSRD(ZA[n], ((j) < 2 ? rd01 : rd23), ((j) & 1) * KB_STAGE + n * 1024); \
      if ((MODE) != 1) { AMFMA(acc[0][n], ZB[n], Wr[((j) + 3) & 3][2]); AMFMA(acc[1][n], ZB[n], Wr[((j) + 3) & 3][3]); } \
      if ((MODE) == 0) { \
        if (n == 0) KB_DMA1((i) + 3, ((j) + 3) & 3, 0); \
        if (n == 2) KB_DMA1((i) + 3, ((j) + 3) & 3, 1024); \
        if (n == 4) KB_W1((i) + 3, ((j) + 3) & 3, 0); \
        if (n == 6) KB_DMA1((i) + 3, ((j) + 3) & 3, 2048); \
        if (n == 8) KB_W1((i) + 3, ((j) + 3) & 3, 1); } \
      if ((MODE) == 2 && !(n & 1)) KB_EPI_LD(n >> 1); } \
    if ((MODE) == 1) { KB_ALL(2, 2); KB_DMA1(3, 3, 0); KB_DMA1(3, 3, 1024); KB_W1(3, 3, 0); KB_DMA1(3, 3, 2048); KB_W1(3, 3, 1); } \
    _Pragma("unroll") for (int n = 0; n < 9; ++n) { \
      if (n == 0) asm volatile("s_waitcnt lgkmcnt(6)" ::: "memory"); \
      if (n == 2) asm volatile("s_waitcnt lgkmcnt(5)" ::: "memory"); \
      if (n == 5) asm volatile("s_waitcnt lgkmcnt(5)" ::: "memory"); \
      ADSRD(ZB[n], ((j) < 2 ? rd01 : rd23), ((j) & 1) * KB_STAGE + 9216 + n * 1024); \
      AMFMA(acc[0][n], ZA[n], Wr[j][0]); AMFMA(acc[1][n], ZA[n], Wr[j][1]); \
      if ((MODE) < 2) { \
        if (n == 0) KB_DMA1((i) + 3, ((j) + 3) & 3, 3072); \
        if (n == 2) KB_W1((i) + 3, ((j) + 3) & 3, 2); \
        if (n == 4) KB_DMAH((i) + 3, ((j) + 3) & 3); \
        if (n == 6) KB_W1((i) + 3, ((j) + 3) & 3, 3); } \
      if ((MODE) == 2 && (n == 0 || n == 2 || n == 4)) KB_EPI_LD(5 + (n >> 1)); } \
    KB_WAIT(((j) + 1) & 3, WN); \
    __builtin_amdgcn_s_barrier(); \
    } while (0)

  KB_ITER(0, 0, 1, 18);
  KB_ITER(1, 1, 0, 18);
  KB_ITER(2, 2, 0, 18);
  KB_ITER(3, 3, 0, 18);
  for (int io = 4; io < 12; io += 4) {
    KB_ITER(io + 0, 0, 0, 18);
    KB_ITER(io + 1, 1, 0, 18);
    KB_ITER(io + 2, 2, 0, 18);
    KB_ITER(io + 3, 3, 0, 18);
  }
  KB_ITER(12, 0, 0, 18);
  KB_ITER(13, 1, 2, 17);
  KB_ITER(14, 2, 3, 8);
  {
#pragma unroll
    for (int n = 0; n < 9; ++n) { ADSRD(ZA[n], rd23, KB_STAGE + n * 1024); AMFMA(acc[0][n], ZB[n], Wr[2][2]); AMFMA(acc[1][n], ZB[n], Wr[2][3]); }
#pragma unroll
    for (int n = 0; n < 9; ++n) {
      if (n == 0) asm volatile("s_waitcnt lgkmcnt(6)" ::: "memory");
      if (n == 2) asm volatile("s_waitcnt lgkmcnt(5)" ::: "memory");
      if (n == 5) asm volatile("s_waitcnt lgkmcnt(5)" ::: "memory");
      ADSRD(ZB[n], rd23, KB_STAGE + 9216 + n * 1024);
      AMFMA(acc[0][n], ZA[n], Wr[3][0]); AMFMA(acc[1][n], ZA[n], Wr[3][1]); }
    asm volatile("s_waitcnt lgkmcnt(0)" ::: "memory");
#pragma unroll
    for (int n = 0; n < 9; ++n) { AMFMA(acc[0][n], ZB[n], Wr[3][2]); AMFMA(acc[1][n], ZB[n], Wr[3][3]); }
  }
  asm volatile("s_nop 15" : "+v"(acc[0][0]), "+v"(acc[0][1]), "+v"(acc[0][2]), "+v"(acc[0][3]), "+v"(acc[0][4]), "+v"(acc[0][5]), "+v"(acc[0][6]), "+v"(acc[0][7]), "+v"(acc[0][8]),
               "+v"(acc[1][0]), "+v"(acc[1][1]), "+v"(acc[1][2]), "+v"(acc[1][3]), "+v"(acc[1][4]), "+v"(acc[1][5]), "+v"(acc[1][6]), "+v"(acc[1][7]), "+v"(acc[1][8]) :: "memory");
  asm volatile("s_waitcnt vmcnt(0)" : "+v"(z1q[0][0]), "+v"(z1q[0][1]), "+v"(z1q[1][0]), "+v"(z1q[1][1]), "+v"(z1s[0]), "+v"(z1s[1]), "+v"(biasu[0]), "+v"(biasu[1]) :: "memory");
  SCHED();
  char* hp = ws + OFF_HT + ((size_t)(tp * 8 + wm * 2) * 16 + fb) * 4608;
  float bias[2] = {bf_lo(biasu[0]), bf_lo(biasu[1])};
  char* ex = smem + wm * 18432 + lane * 16;
  if (kh == 0) {
#pragma unroll
    for (int m = 0; m < 2; ++m)
#pragma unroll
      for (int n = 4; n < 8; ++n) *(f32x4*)(ex + (m * 9 + n) * 1024) = acc[m][n];
  } else {
#pragma unroll
    for (int m = 0; m < 2; ++m) {
#pragma unroll
      for (int n = 0; n < 4; ++n) *(f32x4*)(ex + (m * 9 + n) * 1024) = acc[m][n];
      *(f32x4*)(ex + (m * 9 + 8) * 1024) = acc[m][8];
    }
  }
  __syncthreads();
#pragma unroll
  for (int m = 0; m < 2; ++m) {
#pragma unroll
    for (int q = 0; q < 2; ++q) {
      f32x4 g0, g1;
      if (kh == 0) { g0 = acc[m][2 * q] + *(const f32x4*)(ex + (m * 9 + 2 * q) * 1024); g1 = acc[m][2 * q + 1] + *(const f32x4*)(ex + (m * 9 + 2 * q + 1) * 1024); }
      else { g0 = acc[m][4 + 2 * q] + *(const f32x4*)(ex + (m * 9 + 4 + 2 * q) * 1024); g1 = acc[m][5 + 2 * q] + *(const f32x4*)(ex + (m * 9 + 5 + 2 * q) * 1024); }
      u32x4 z = z1q[m][q], o;
      o[0] = pk_bf16(bf_lo(z[0]) * (g0[0] + bias[m]), bf_hi(z[0]) * (g0[1] + bias[m]));
      o[1] = pk_bf16(bf_lo(z[1]) * (g0[2] + bias[m]), bf_hi(z[1]) * (g0[3] + bias[m]));
      o[2] = pk_bf16(bf_lo(z[2]) * (g1[0] + bias[m]), bf_hi(z[2]) * (g1[1] + bias[m]));
      o[3] = pk_bf16(bf_lo(z[3]) * (g1[2] + bias[m]), bf_hi(z[3]) * (g1[3] + bias[m]));
      st16(hp + m * (16 * 4608) + (kh * 2 + q) * 1024 + lane * 16, o);
    }
    if (kh == 0) {
      f32x4 g0 = acc[m][8] + *(const f32x4*)(ex + (m * 9 + 8) * 1024);
      u32x2 z = z1s[m], o;
      o[0] = pk_bf16(bf_lo(z[0]) * (g0[0] + bias[m]), bf_hi(z[0]) * (g0[1] + bias[m]));
      o[1] = pk_bf16(bf_lo(z[1]) * (g0[2] + bias[m]), bf_hi(z[1]) * (g0[3] + bias[m]));
      st8(hp + m * (16 * 4608) + 4096 + lane * 8, o);
    }
  }
}

constexpr int KC_PITCH = 336;
constexpr int KC_PART = 64 * KC_PITCH;
constexpr int KC_LDS = 5 * KC_PART;
template <bool ISO>
__device__ __forceinline__ void kc_main(f32x4 (&acc)[4][5], rsrc_t rA, rsrc_t rW, unsigned vA16, unsigned vA8, unsigned vW, unsigned sA, unsigned sW) {
  constexpr unsigned TS = ISO ? 18432u : 16u * 4608u;
  constexpr int PD = 3;
  bf16x8 A[PD + 1][4], W[PD + 1][5];
  auto load = [&](int u, int slot) {
#pragma unroll
    for (int m = 0; m < 4; ++m) {
      if (ISO || (u % 9) == 4) {
        unsigned o0, o1;
        if (ISO) { o0 = sA + m * TS + u * 1024; o1 = o0 + 512; }
        else { o0 = sA + m * TS + (u / 9) * 2 * 4608 + 4096; o1 = o0 + 4608; }
        u32x2 lo = __builtin_amdgcn_raw_buffer_load_b64(rA, vA8, o0, 0);
        u32x2 hi = __builtin_amdgcn_raw_buffer_load_b64(rA, vA8, o1, 0);
        u32x4 v = {lo[0], lo[1], hi[0], hi[1]};
        A[slot][m] = __builtin_bit_cast(bf16x8, v);
      } else {
        int i = u % 9;
        unsigned o = sA + m * TS + (u / 9) * 2 * 4608 + (i < 4 ? i * 1024 : 4608 + (i - 5) * 1024);
        A[slot][m] = __builtin_bit_cast(bf16x8, __builtin_amdgcn_raw_buffer_load_b128(rA, vA16, o, 0));
      }
    }
#pragma unroll
    for (int n = 0; n < 5; ++n) W[slot][n] = __builtin_bit_cast(bf16x8, __builtin_amdgcn_raw_buffer_load_b128(rW, vW, sW + (u * 5 + n) * 1024, 0));
  };
#pragma unroll
  for (int u = 0; u < PD; ++u) load(u, u);
  SCHED();
#pragma unroll
  for (int u = 0; u < 18; ++u) {
    if (u + PD < 18) load(u + PD, (u + PD) % (PD + 1));
    SCHED();
#pragma unroll
    for (int m = 0; m < 4; ++m)
#pragma unroll
      for (int n = 0; n < 5; ++n) acc[m][n] = MFMA(W[u % (PD + 1)][n], A[u % (PD + 1)][m], acc[m][n]);
    SCHED();
  }
}
__global__ __launch_bounds__(320, 2) void kC(const char* __restrict__ ws_c, const u16* __restrict__ x, u16* __restrict__ out) {
  extern __shared__ __attribute__((aligned(16))) char smem[];
  const int tid = threadIdx.x, lane = tid & 63;
  const int w = RFL(tid >> 6);
  const int b = blockIdx.x, xcd = b & 7, idx = b >> 3;
  const int rt = (xcd & 3) * 8 + (idx & 7), ct = (xcd >> 2) * 4 + (idx >> 3);
  f32x4 acc[4][5];
#pragma unroll
  for (int m = 0; m < 4; ++m)
#pragma unroll
    for (int n = 0; n < 5; ++n) acc[m][n] = (f32x4){0.f, 0.f, 0.f, 0.f};
  const rsrc_t rW = mk_rsrc(ws_c + OFF_WC, (int)SZ_WC);
  const unsigned sW = RFL((unsigned)((ct * 90 + w * 18) * 5 * 1024));
  const int dstart = ct < 7 ? ct * 72 : 496;
  u32x4 xv[2]; unsigned gof[2];
#pragma unroll
  for (int e = 0; e < 2; ++e) {
    int c = tid + e * 320; c = c < 576 ? c : tid;
    const int r = c / 9, ch = c - r * 9;
    gof[e] = (unsigned)(rt * 64 + prow(r >> 4, r & 15)) * 1152u + (unsigned)(ct * 72 + ch * 8) * 2u;
    asm volatile("global_load_dwordx4 %0, %1, %2" : "=v"(xv[e]) : "v"(gof[e]), "s"(x));
  }
  if (w < 4) {
    const rsrc_t rA = mk_rsrc(ws_c + OFF_HT, (int)SZ_Z);
    kc_main<false>(acc, rA, rW, lane * 16, lane * 8, lane * 16, RFL((unsigned)((rt * 4 * 16 + w * 4) * 4608)), sW);
  } else {
    const rsrc_t rA = mk_rsrc(ws_c + OFF_OT, (int)SZ_OT);
    kc_main<true>(acc, rA, rW, lane * 16, lane * 8, lane * 16, RFL((unsigned)(rt * 4 * 18432)), sW);
  }
  {
    char* p = smem + w * KC_PART + (lane & 15) * KC_PITCH + (lane >> 4) * 16;
#pragma unroll
    for (int m = 0; m < 4; ++m)
#pragma unroll
      for (int n = 0; n < 5; ++n) *(f32x4*)(p + m * 16 * KC_PITCH + n * 64) = acc[m][n];
  }
  asm volatile("s_waitcnt vmcnt(0) lgkmcnt(0)\n\ts_barrier" : "+v"(xv[0]), "+v"(xv[1]) :: "memory");
  const int own0 = ct * 72 - dstart;
#pragma unroll
  for (int e = 0; e < 2; ++e) {
    const int c = tid + e * 320;
    if (c < 576) {
      const int r = c / 9, ch = c - r * 9;
      const char* p = smem + r * KC_PITCH + (own0 + ch * 8) * 4;
      f32x4 s0 = *(const f32x4*)p, s1 = *(const f32x4*)(p + 16);
#pragma unroll
      for (int ww = 1; ww < 5; ++ww) { s0 += *(const f32x4*)(p + ww * KC_PART); s1 += *(const f32x4*)(p + ww * KC_PART + 16); }
      u32x4 o;
      o[0] = pk_bf16(s0[0] + bf_lo(xv[e][0]), s0[1] + bf_hi(xv[e][0]));
      o[1] = pk_bf16(s0[2] + bf_lo(xv[e][1]), s0[3] + bf_hi(xv[e][1]));
      o[2] = pk_bf16(s1[0] + bf_lo(xv[e][2]), s1[1] + bf_hi(xv[e][2]));
      o[3] = pk_bf16(s1[2] + bf_lo(xv[e][3]), s1[3] + bf_hi(xv[e][3]));
      st16((char*)out + gof[e], o);
    }
  }
}

constexpr int K0_BLOCKS = 280;
#define K0LD(p) __builtin_nontemporal_load(p)
#define XLD(p) (*(p))
constexpr int PF0_BYTES = 12288;
#define PF_CODE_N 16
__global__ __launch_bounds__(256) void k0(const u16* __restrict__ x, const u16* __restrict__ wqkv, const u16* __restrict__ win, char* __restrict__ ws, const u16* __restrict__ wsp, const u16* __restrict__ wout, const u16* __restrict__ wo, const u16* __restrict__ bsp) {
  __shared__ __attribute__((aligned(16))) char sm[32 * K0_PITCH];
  const int tid = threadIdx.x, lane = tid & 63, i = lane & 15, g = lane >> 4, w = tid >> 6, braw = blockIdx.x;
  const int b = braw < 64 ? braw + 216 : braw - 64;
  const unsigned smb = (unsigned)(size_t)(__attribute__((address_space(3))) char*)sm;
  unsigned pf0 = 0, pf1 = 0, pf2 = 0;
  if (w == 3 && (braw >> 3) < PF_CODE_N) {
    const int u = braw >> 3;
    unsigned long long pa, p0;
    asm volatile("s_getpc_b64 s[20:21]\n\ts_add_u32 s20, s20, _Z2kAPKcPcPKtS3_S3_@rel32@lo+4\n\ts_addc_u32 s21, s21, _Z2kAPKcPcPKtS3_S3_@rel32@hi+12\n\ts_mov_b64 %0, s[20:21]" : "=s"(pa) :: "scc", "s20", "s21");
    asm volatile("s_getpc_b64 s[20:21]\n\ts_add_u32 s20, s20, _Z2k0PKtS0_S0_PcS0_S0_S0_S0_@rel32@lo+4\n\ts_addc_u32 s21, s21, _Z2k0PKtS0_S0_PcS0_S0_S0_S0_@rel32@hi+12\n\ts_mov_b64 %0, s[20:21]" : "=s"(p0) :: "scc", "s20", "s21");
    const long long span = (long long)(p0 - pa);
    int off = (u < 2 ? u : u - 2) * 8192 + lane * 128;
    const int lim = u < 2 ? PF0_BYTES : (span > 0 && span < (1 << 20) ? (int)span : 0);
    off = off < lim ? off : 0;
    const char* a = (const char*)(u < 2 ? p0 : pa) + off;
    asm volatile("global_load_dword %0, %1, off" : "=v"(pf0) : "v"(a));
  }
  if (b == 217 && w == 1) { const char* a = (const char*)bsp + (lane & 31) * 128; asm volatile("global_load_dword %0, %1, off" : "=v"(pf1) : "v"(a)); }
  if (b < 144) {
    const int ks = b >> 3, cb = b & 7;
    u32x4 v[9];
    const u16* src = win + (size_t)(ks * 32 + (tid >> 3)) * 4608 + cb * 576 + (tid & 7) * 8;
#pragma unroll
    for (int k = 0; k < 9; ++k) v[k] = K0LD((const u32x4*)(src + k * 64));
    char* dl = sm + (tid >> 3) * K0_PITCH + (tid & 7) * 16;
#pragma unroll
    for (int k = 0; k < 9; ++k) *(u32x4*)(dl + k * 128) = v[k];
    __syncthreads();
    const unsigned a0 = smb + (8 * g + (i >> 2)) * K0_PITCH + (i & 3) * 8;
#pragma unroll
    for (int e = 0; e < 9; ++e) {
      int nt = w * 9 + e;
      int gt = cb * 36 + nt;
      int z2 = gt >= 144, t = gt - z2 * 144, fb = t / 9, n = t - fb * 9;
      int fast = z2 ? n < 5 : n >= 4, nz = 4 + fast;
      int fr = fb * 468 + (fast ? 0 : 252) + 72 + (z2 ? ks * nz + n - (fast ? 0 : 5) : 18 * nz + ks * nz + n - (fast ? 4 : 0));
      u32x4 o = tr_frag(a0 + nt * 32, a0 + nt * 32 + 4 * K0_PITCH);
      st16(ws + OFF_W1 + (size_t)fr * 1024 + lane * 16, o);
    }
  } else if (b < 216) {
    const int u = b - 144, ks = u >> 2, hq = u & 3;
    u32x4 v[7]; int dst[7], dw0[7];
#pragma unroll
    for (int k = 0; k < 7; ++k) {
      int c = tid + k * 256; c = c < 32 * 54 ? c : 32 * 54 - 1;
      const int r = c / 54, e = c - r * 54, part = e / 18, ch = e - part * 18;
      v[k] = K0LD((const u32x4*)(wqkv + (size_t)(ks * 32 + r) * 1728 + part * 576 + hq * 144 + ch * 8));
      dst[k] = r * K0_PITCH + (part == 0 ? 80 : (part == 1 ? 0 : 40)); dw0[k] = ch * 4;
    }
#pragma unroll
    for (int z = 0; z < 5; ++z) {
      const int c = tid + z * 256, r = c / 40, e = c - r * 40, hl = e / 5, jj = e - hl * 5;
      *(unsigned*)(sm + r * K0_PITCH + hl * 128 + (jj == 0 ? 9 : (jj == 1 ? 19 : 27 + jj)) * 4) = 0u;
    }
#pragma unroll
    for (int k = 0; k < 7; ++k)
#pragma unroll
      for (int q = 0; q < 4; ++q) { const int dw = dw0[k] + q, hl = (dw * 57) >> 9; *(unsigned*)(sm + dst[k] + hl * 128 + (dw - hl * 9) * 4) = v[k][q]; }
    __syncthreads();
    const unsigned a0 = smb + (8 * g + (i >> 2)) * K0_PITCH + (i & 3) * 8;
#pragma unroll
    for (int e = 0; e < 8; ++e) {
      const int f = w * 8 + e, hl = f >> 2, n = f & 3;
      u32x4 o = tr_frag(a0 + f * 32, a0 + f * 32 + 4 * K0_PITCH);
      st16(ws + OFF_W1 + (size_t)(((hq * 8 + hl) >> 1) * 468 + (hl & 1) * 252 + ks * 4 + n) * 1024 + lane * 16, o);
    }
  } else {
    if (tid < 36) st16(ws + OFF_RS + ((b - 216) * 36 + tid) * 16, (u32x4){0, 0, 0, 0});
    const int u = (b - 216) * 4 + w, tt = u >> 1, slot = (u & 1) * 8 + (lane >> 3), c0 = lane & 7;
    const u16* xr = x + (size_t)prow(tt, slot) * 576 + c0 * 8;
    u32x4 v[9];
#pragma unroll
    for (int k = 0; k < 9; ++k) v[k] = XLD((const u32x4*)(xr + k * 64));
    float s = 0.f;
#pragma unroll
    for (int k = 0; k < 9; ++k)
#pragma unroll
      for (int j = 0; j < 4; ++j) s += bf_lo(v[k][j]) + bf_hi(v[k][j]);
    s += __shfl_xor(s, 1); s += __shfl_xor(s, 2); s += __shfl_xor(s, 4);
    const float mean = s * (1.f / 576.f);
    float q = 0.f;
#pragma unroll
    for (int k = 0; k < 9; ++k)
#pragma unroll
      for (int j = 0; j < 4; ++j) { float a = bf_lo(v[k][j]) - mean, c = bf_hi(v[k][j]) - mean; q += a * a + c * c; }
    q += __shfl_xor(q, 1); q += __shfl_xor(q, 2); q += __shfl_xor(q, 4);
    const float mb = rbf(mean), sd = rbf(sqrtf(rbf(rbf(q * (1.f / 576.f)) + 1e-5f))), rs = 1.f / sd;
    char* dst = ws + OFF_XT + (size_t)tt * 18432 + slot * 16;
#pragma unroll
    for (int k = 0; k < 9; ++k) {
      u32x4 o;
#pragma unroll
      for (int j = 0; j < 4; ++j) o[j] = pk_bf16(rbf(bf_lo(v[k][j]) - mb) * rs, rbf(bf_hi(v[k][j]) - mb) * rs);
      int c = c0 + 8 * k;
      st16(dst + (c >> 2) * 1024 + (c & 3) * 256, o);
    }
  }
  asm volatile("s_waitcnt vmcnt(0)" : "+v"(pf0), "+v"(pf1), "+v"(pf2) :: "memory");
}

extern "C" void kernel_launch(void* const* d_in, const int* in_sizes, int n_in, void* d_out, int out_size, void* d_ws, size_t ws_size, hipStream_t stream) {
  (void)in_sizes; (void)n_in; (void)out_size; (void)ws_size;
  const u16* x = (const u16*)d_in[0];
  char* ws = (char*)d_ws;
  (void)hipFuncSetAttribute((const void*)kA, hipFuncAttributeMaxDynamicSharedMemorySize, KA_LDS);
  (void)hipFuncSetAttribute((const void*)kB, hipFuncAttributeMaxDynamicSharedMemorySize, KB_LDS);
  (void)hipFuncSetAttribute((const void*)kC, hipFuncAttributeMaxDynamicSharedMemorySize, KC_LDS);
  k0<<<K0_BLOCKS, 256, 0, stream>>>(x, (const u16*)d_in[1], (const u16*)d_in[4], ws, (const u16*)d_in[5], (const u16*)d_in[7], (const u16*)d_in[2], (const u16*)d_in[6]);
  kA<<<512, 256, KA_LDS, stream>>>(ws, ws, (const u16*)d_in[5], (const u16*)d_in[7], (const u16*)d_in[2]);
  kB<<<256, 512, KB_LDS, stream>>>(ws, ws, (const u16*)d_in[6]);
  kC<<<256, 320, KC_LDS, stream>>>(ws, x, (u16*)d_out);
}
```
